```python
import torch

def _softmax(x, axis=-1):
    return torch.softmax(x, dim=axis)
def _gelu(x):
    return torch.nn.functional.gelu(x, approximate="tanh")
def _silu(x):
    return torch.nn.functional.silu(x)
def _sigmoid(x):
    return torch.sigmoid(x)
def _layernorm(x, axis=-1, eps=1e-5):
    m = x.mean(dim=axis, keepdim=True)
    v = x.var(dim=axis, keepdim=True, unbiased=False)
    return (x - m) / torch.sqrt(v + eps)
def _scatter_add(tgt, idx, src, axis=0):
    return tgt.clone().index_add_(axis, idx, src)
def _segment_sum(src, seg, n):
    out = torch.zeros((n,) + src.shape[1:], dtype=src.dtype)
    return out.index_add_(0, seg, src)
def _pad(x, pw):
    flat = tuple(v for pair in reversed(pw) for v in pair)
    return torch.nn.functional.pad(x, flat)

kernel_name = 'interleave_layers__ssm_conv_s4d__linear_kernel_hedgehog__prompt'

def setup_inputs():

    _w = (lambda a: (lambda t: t.to(torch.bfloat16) if t.is_floating_point() else t)(torch.as_tensor(a)))
    def _A_setup():
        Bsz, L, D, N = 1, 2048, 512, 128
        return {
            "u": _w((lambda *s: torch.randn(*s, dtype=torch.bfloat16) * 0.05)(Bsz, L, D)),
            "log_A_real": _w((lambda *s: torch.randn(*s, dtype=torch.bfloat16) * 3.2)(D, N) * 0.5),
            "A_imag": _w((lambda *s: torch.randn(*s, dtype=torch.bfloat16) * 0.05)(D, N) * 3.14),
            "C_real": _w((lambda *s: torch.randn(*s, dtype=torch.bfloat16) * 0.05)(D, N) / N ** 0.5),
            "C_imag": _w((lambda *s: torch.randn(*s, dtype=torch.bfloat16) * 0.05)(D, N) / N ** 0.5),
            "log_dt": _w((lambda *s: torch.randn(*s, dtype=torch.bfloat16) * 0.05)(D) * 0.5 - 4.0),
            "D_skip": _w((lambda *s: torch.randn(*s, dtype=torch.bfloat16) * 0.05)(D)),
        }
    _A_in = _A_setup()

    def _B_setup():
        B, S, D, H = 1, 2048, 512, 32
        Dh = D // H
        return {
            "x": _w((lambda *s: torch.randn(*s, dtype=torch.bfloat16) * 0.05)(B, S, D)),
            "wqkv": _w((lambda *s: torch.randn(*s, dtype=torch.bfloat16) * 0.4)(D, 3 * D)),
            "w_phi": _w((lambda *s: torch.randn(*s, dtype=torch.bfloat16) * 0.4)(H, Dh, Dh) * (Dh ** -0.5)),
            "wo": _w((lambda *s: torch.randn(*s, dtype=torch.bfloat16) * 0.05)(D, D)),
        }
    _B_in = _B_setup()

    return {
        "A_u": _A_in["u"],
        "A_log_A_real": _A_in["log_A_real"],
        "A_A_imag": _A_in["A_imag"],
        "A_C_real": _A_in["C_real"],
        "A_C_imag": _A_in["C_imag"],
        "A_log_dt": _A_in["log_dt"],
        "A_D_skip": _A_in["D_skip"],
        "B_x": _B_in["x"],
        "B_wqkv": _B_in["wqkv"],
        "B_w_phi": _B_in["w_phi"],
        "B_wo": _B_in["wo"],
    }

def reference(A_u, A_log_A_real, A_A_imag, A_C_real, A_C_imag, A_log_dt, A_D_skip, B_x, B_wqkv, B_w_phi, B_wo):
    def _A_ref(u, log_A_real, A_imag, C_real, C_imag, log_dt, D_skip):
        L = u.shape[1]
        dt = torch.exp(log_dt)
        A = -torch.exp(log_A_real) + 1j * A_imag
        C = (C_real + 1j * C_imag) * dt[:, None]
        pos = (lambda x, dt: x.to(dt))(torch.arange(L), log_dt.dtype)
        P = torch.exp((A * dt[:, None])[:, :, None] * pos[None, None, :])
        K = 2.0 * torch.einsum("dn,dnl->dl", C, P).real
        L2 = 2 * L
        u_t = (lambda x, *d: x.permute(*d))(u, 0, 2, 1)
        Kf = (lambda x, *a, **k: torch.fft.rfft(x.float(), *a, **k))(K, L2)
        Uf = (lambda x, *a, **k: torch.fft.rfft(x.float(), *a, **k))(u_t, L2)
        y = (lambda x, *a, **k: torch.fft.irfft(x, *a, **k).to(torch.bfloat16))(Uf * Kf[None, :, :], L2)[:, :, :L]
        y = (lambda x, *d: x.permute(*d))(y, 0, 2, 1)
        return y + u * D_skip[None, None, :]

    def _B_ref(x, wqkv, w_phi, wo):
        B, S, D = x.shape
        H = 32; Dh = D // H
        qkv = (lambda x, *d: x.permute(*d))((x @ wqkv).reshape(B, S, 3, H, Dh), 2, 0, 3, 1, 4)
        q, k, v = qkv[0], qkv[1], qkv[2]
        pq = torch.einsum("bhsd,hdf->bhsf", q, w_phi)
        pk = torch.einsum("bhsd,hdf->bhsf", k, w_phi)
        phi_q = torch.concatenate([_softmax(pq, axis=-1), _softmax(-pq, axis=-1)], axis=-1)
        phi_k = torch.concatenate([_softmax(pk, axis=-1), _softmax(-pk, axis=-1)], axis=-1)
        kv = torch.einsum("bhsf,bhsd->bhfd", phi_k, v)
        z  = torch.einsum("bhsf->bhf", phi_k)
        num = torch.einsum("bhqf,bhfd->bhqd", phi_q, kv)
        den = torch.einsum("bhqf,bhf->bhq", phi_q, z)[..., None] + 1e-6
        o = num / den
        return (lambda x, *d: x.permute(*d))(o, 0, 2, 1, 3).reshape(B, S, D) @ wo

    x = A_u
    x = x + _A_ref(_layernorm(x, axis=-1), A_log_A_real, A_A_imag, A_C_real, A_C_imag, A_log_dt, A_D_skip)
    x = x + _B_ref(_layernorm(x, axis=-1), B_wqkv, B_w_phi, B_wo)
    x = x + _A_ref(_layernorm(x, axis=-1), A_log_A_real, A_A_imag, A_C_real, A_C_imag, A_log_dt, A_D_skip)
    x = x + _B_ref(_layernorm(x, axis=-1), B_wqkv, B_w_phi, B_wo)
    return x
```

```cpp
#include <hip/hip_runtime.h>
#include <hip/hip_bf16.h>
#include <stdint.h>

#define MK_STAMP 0
#define STAMP(ws, i) do { } while (0)
typedef short s4 __attribute__((ext_vector_type(4)));
typedef short s8 __attribute__((ext_vector_type(8)));
typedef float f2 __attribute__((ext_vector_type(2)));
typedef float f4 __attribute__((ext_vector_type(4)));
typedef unsigned u2 __attribute__((ext_vector_type(2)));
typedef unsigned u4 __attribute__((ext_vector_type(4)));
typedef __bf16 bf2 __attribute__((ext_vector_type(2)));
#define GAS __attribute__((address_space(1)))
#define LAS __attribute__((address_space(3)))
#define DI __device__ __forceinline__
#define RLX_AGENT __ATOMIC_RELAXED, __HIP_MEMORY_SCOPE_AGENT
typedef GAS unsigned gu32;
typedef LAS unsigned char lds_t;
typedef __amdgpu_buffer_rsrc_t rsrc_t;

constexpr int SEQ = 2048, DM = 512, NST = 128, NH = 32;
constexpr float LOG2E = 1.4426950408889634f;
constexpr float LN_EPS = 1e-5f;
constexpr int AUX_SC1 = 16;

constexpr int XT_STRIDE = 2112;
constexpr unsigned WS_CTRL = 0;
constexpr unsigned WS_KREV = 131072;
constexpr unsigned WS_WFRAG = WS_KREV + 512u * 2048 * 2;
constexpr unsigned WS_XT = WS_WFRAG + 32u * 3 * 16 * 1024;
constexpr unsigned XT_BYTES = 512u * XT_STRIDE * 2;
constexpr unsigned WS_ST = WS_XT + 2 * XT_BYTES;
constexpr unsigned WS_X1 = WS_ST + 2u * 2048 * 8;
constexpr unsigned X1_BYTES = 2048u * 512 * 2;
constexpr unsigned WS_KVP = WS_X1 + 2 * X1_BYTES;
constexpr unsigned KVP_BYTES = 32u * 16 * 2048;
constexpr unsigned WS_ZP = WS_KVP + 2 * KVP_BYTES;
constexpr unsigned ZP_BYTES = 32u * 16 * 128;
constexpr unsigned WS_OB = WS_ZP + 2 * ZP_BYTES;
constexpr unsigned OB_BYTES = 2048u * 512 * 2;
constexpr unsigned WS_END = WS_OB + 2 * OB_BYTES;
constexpr int CW_TMO = 0;
constexpr int NCW_A2A = 3 * 8 * 16, NCW_GRP = 2 * 2 * 16;
constexpr int CW_STRIDE = 64;
constexpr int CWI_A2A = 1, CWI_GRP = CWI_A2A + NCW_A2A, CWI_END = CWI_GRP + NCW_GRP;
static_assert(CWI_END * CW_STRIDE * 4 <= 131072, "control block");

struct Params {
  const unsigned short* u;
  const unsigned short* lar;
  const unsigned short* aim;
  const unsigned short* cre;
  const unsigned short* cim;
  const unsigned short* ldt;
  const unsigned short* dsk;
  const unsigned short* wqkv;
  const unsigned short* wphi;
  const unsigned short* wo;
  unsigned short* out;
  unsigned char* ws;
};

DI float bf2f(unsigned short b) { return __builtin_bit_cast(float, (unsigned)b << 16); }
DI float blo(unsigned x) { return __builtin_bit_cast(float, x << 16); }
DI float bhi(unsigned x) { return __builtin_bit_cast(float, x & 0xffff0000u); }
DI unsigned pack2(float a, float b) { bf2 v = {(__bf16)a, (__bf16)b}; return __builtin_bit_cast(unsigned, v); }
DI float rbf(float a) { return blo(pack2(a, a)); }
DI f4 mfma32(u4 a, u4 b, f4 c) { return __builtin_amdgcn_mfma_f32_16x16x32_bf16(__builtin_bit_cast(s8, a), __builtin_bit_cast(s8, b), c, 0, 0, 0); }
DI f4 mfma16(u2 a, u2 b, f4 c) { return __builtin_amdgcn_mfma_f32_16x16x16bf16_1k(__builtin_bit_cast(s4, a), __builtin_bit_cast(s4, b), c, 0, 0, 0); }
DI u2 lds_tr(lds_t* p) { return __builtin_bit_cast(u2, __builtin_amdgcn_ds_read_tr16_b64_v4i16((LAS s4*)p)); }
DI u4 cat(u2 a, u2 b) { return u4{a.x, a.y, b.x, b.y}; }
DI rsrc_t make_rsrc(const void* p, unsigned bytes) { return __builtin_amdgcn_make_buffer_rsrc((void*)p, 0, (int)bytes, 0x00020000); }
DI u4 ld16(rsrc_t r, unsigned off) { return __builtin_amdgcn_raw_buffer_load_b128(r, off, 0, AUX_SC1); }
DI void st16(rsrc_t r, unsigned off, u4 v) { __builtin_amdgcn_raw_buffer_store_b128(v, r, off, 0, AUX_SC1); }
DI void drain() { asm volatile("s_waitcnt vmcnt(0)" ::: "memory"); }
DI void dma4_sc1(rsrc_t r, unsigned v0, unsigned v1, unsigned soff, unsigned lds) {
  asm volatile("s_mov_b32 m0, %4\n\ts_nop 0\n\t"
               "buffer_load_dwordx4 %0, %2, %3 offen sc1 lds\n\tbuffer_load_dwordx4 %1, %2, %3 offen offset:1024 sc1 lds\n\t"
               "buffer_load_dwordx4 %0, %2, %3 offen offset:2048 sc1 lds\n\tbuffer_load_dwordx4 %1, %2, %3 offen offset:3072 sc1 lds"
               :: "v"(v0), "v"(v1), "s"(r), "s"(soff), "s"(lds) : "memory");
}
DI void dma4(rsrc_t r, unsigned v0, unsigned v1, unsigned v2, unsigned v3, unsigned soff, unsigned lds) {
  asm volatile("s_mov_b32 m0, %6\n\ts_nop 0\n\t"
               "buffer_load_dwordx4 %0, %4, %5 offen lds\n\tbuffer_load_dwordx4 %1, %4, %5 offen offset:1024 lds\n\t"
               "buffer_load_dwordx4 %2, %4, %5 offen offset:2048 lds\n\tbuffer_load_dwordx4 %3, %4, %5 offen offset:3072 lds"
               :: "v"(v0), "v"(v1), "v"(v2), "v"(v3), "s"(r), "s"(soff), "s"(lds) : "memory");
}
#define SB() __builtin_amdgcn_sched_barrier(0)
#define SGB(mask, n) __builtin_amdgcn_sched_group_barrier(mask, n, 0)
constexpr int SG_MFMA = 0x8, SG_DSR = 0x100;
template <int CTRL> DI float dpp_f(float v) { return __builtin_bit_cast(float, __builtin_amdgcn_mov_dpp(__builtin_bit_cast(int, v), CTRL, 0xf, 0xf, true)); }
DI float row_sum(float v) { v += dpp_f<0x128>(v); v += dpp_f<0x124>(v); v += dpp_f<0x122>(v); v += dpp_f<0x121>(v); return v; }
DI f2 swap32(float a, float b) { auto r = __builtin_amdgcn_permlane32_swap(__builtin_bit_cast(unsigned, a), __builtin_bit_cast(unsigned, b), false, false);
  const unsigned r0 = r[0], r1 = r[1];
  return f2{__builtin_bit_cast(float, r0), __builtin_bit_cast(float, r1)}; }
DI f2 swap16(float a, float b) { auto r = __builtin_amdgcn_permlane16_swap(__builtin_bit_cast(unsigned, a), __builtin_bit_cast(unsigned, b), false, false);
  const unsigned r0 = r[0], r1 = r[1];
  return f2{__builtin_bit_cast(float, r0), __builtin_bit_cast(float, r1)}; }
DI f2 xrow_max2(float a, float b) { f2 s = swap32(a, b); float c = fmaxf(s.x, s.y); s = swap16(c, c); c = fmaxf(s.x, s.y); return swap32(c, c); }
DI f2 xrow_sum2(float a, float b) { f2 s = swap32(a, b); float c = s.x + s.y; s = swap16(c, c); c = s.x + s.y; return swap32(c, c); }
DI float wave_sum(float v) { v = row_sum(v); f2 s = swap16(v, v); v = s.x + s.y; s = swap32(v, v); return s.x + s.y; }

struct cpx { float r, i; };
DI cpx cmul(cpx a, cpx b) { return cpx{a.r * b.r - a.i * b.i, a.r * b.i + a.i * b.r}; }
DI cpx cexp_p(float wr2, float wrev, float p) {
  const float e = __builtin_amdgcn_exp2f(wr2 * p);
  const float a = __builtin_amdgcn_fractf(wrev * p);
  return cpx{e * __builtin_amdgcn_cosf(a), e * __builtin_amdgcn_sinf(a)};
}
constexpr int KG_ROW = 528;
constexpr int KG_A = 0;
constexpr int KG_B = 2 * 48 * KG_ROW;
constexpr int KG_T = KG_B + 2 * 16 * KG_ROW;
constexpr int KG_X = KG_T + 2 * 2056 * 4;
constexpr int KG_LDS = KG_X + 64 * 136;

__global__ void __launch_bounds__(512) k_prep(Params p) {
  extern __shared__ __attribute__((aligned(16))) unsigned char smem[];
  lds_t* L = (lds_t*)smem;
  const int tid = threadIdx.x, c = blockIdx.x, lane = tid & 63, w = __builtin_amdgcn_readfirstlane(tid >> 6);
  STAMP(p.ws, 0);
  const int half = tid >> 8, chl = (tid >> 7) & 1, n = tid & 127, d = 2 * c + chl;
  const unsigned short lar_b = p.lar[d * NST + n], aim_b = p.aim[d * NST + n], cre_b = p.cre[d * NST + n], cim_b = p.cim[d * NST + n], ldt_b = p.ldt[d];
  const int pb = c >> 3, cb = c & 7;
  const u4 xt = *(const u4*)(p.u + (64 * pb + (tid >> 3)) * DM + 64 * cb + 8 * (tid & 7));
  const u4 xr = *(const u4*)(p.u + (8 * c + w) * DM + 8 * lane);
  u2 wa0 = {0, 0}, wa1 = {0, 0}, wb = {0, 0};
  const int F = 6 * c + w, fh = F / 48, fm = (F % 48) >> 4, fks = F & 15;
  if (w < 6) {
    const int row = lane & 15, kq = lane >> 4;
    wa0 = *(const u2*)(p.wqkv + (32 * fks + row) * 1536 + fm * 512 + 16 * fh + 4 * kq);
    wa1 = *(const u2*)(p.wqkv + (32 * fks + 16 + row) * 1536 + fm * 512 + 16 * fh + 4 * kq);
    if (fm < 2) {
      const unsigned short* q = p.wphi + fh * 256 + (4 * kq) * 16 + row;
      wb.x = q[0] | ((unsigned)q[16] << 16); wb.y = q[32] | ((unsigned)q[48] << 16);
    } else {
      const int dd = row - 4 * kq;
      wb.x = (dd == 0 ? 0x3f80u : 0u) | (dd == 1 ? 0x3f800000u : 0u); wb.y = (dd == 2 ? 0x3f80u : 0u) | (dd == 3 ? 0x3f800000u : 0u);
    }
  }
  {
    GAS unsigned* cw = (GAS unsigned*)(p.ws + WS_CTRL);
    if (c < 8) { const int i = c * 512 + tid; if (i < CWI_END) cw[i * CW_STRIDE] = 0u; }
    if (c >= 8 && c < 16) ((GAS unsigned long long*)(p.ws + WS_ST + 2048 * 8))[(c - 8) * 256 + (tid & 255)] = 0ull;
  }
  {
    const float dt = rbf(__expf(bf2f(ldt_b)));
    const float ar = -rbf(__expf(bf2f(lar_b)));
    const float wr2 = (ar * dt) * LOG2E, wrev = (bf2f(aim_b) * dt) * 0.15915494309189535f;
    const cpx c2 = {2.0f * (bf2f(cre_b) * dt), 2.0f * (bf2f(cim_b) * dt)};
    LAS unsigned* TA = (LAS unsigned*)(L + KG_A + chl * 48 * KG_ROW) + n;
    LAS unsigned* TB = (LAS unsigned*)(L + KG_B + chl * 16 * KG_ROW) + n;
    constexpr int RS = KG_ROW / 4;
    const cpx e128 = cexp_p(wr2, wrev, 128.f);
    if (half == 0) {
      const cpx e8 = cexp_p(wr2, wrev, 8.f);
      cpx y = {1.f, 0.f};
#pragma unroll
      for (int j = 0; j < 16; ++j) { TB[j * RS] = pack2(y.r, y.i); y = (j == 0) ? e8 : cmul(y, e8); }
      cpx g = cmul(c2, cexp_p(wr2, wrev, 1024.f));
#pragma unroll
      for (int a = 0; a < 9; ++a) { TA[(32 + a) * RS] = pack2(g.r, -g.i); g = cmul(g, e128); }
    } else {
      cpx e[8]; e[0] = cpx{1.f, 0.f}; e[1] = cexp_p(wr2, wrev, 1.f);
      e[2] = cmul(e[1], e[1]); e[3] = cmul(e[2], e[1]); e[4] = cmul(e[2], e[2]); e[5] = cmul(e[4], e[1]); e[6] = cmul(e[3], e[3]); e[7] = cmul(e[4], e[3]);
      cpx g = c2;
#pragma unroll
      for (int a = 0; a < 2; ++a) {
#pragma unroll
        for (int k = 0; k < 8; ++k) { const cpx x = k ? cmul(g, e[k]) : g; TA[(8 * a + k) * RS] = pack2(x.r, -x.i); }
        g = cmul(g, e128);
      }
#pragma unroll
      for (int a = 0; a < 2; ++a) {
#pragma unroll
        for (int k = 0; k < 4; ++k) { const cpx x = k ? cmul(g, e[2 * k]) : g; TA[(16 + 4 * a + k) * RS] = pack2(x.r, -x.i); }
        g = cmul(g, e128);
      }
#pragma unroll
      for (int a = 0; a < 4; ++a) {
#pragma unroll
        for (int k = 0; k < 2; ++k) { const cpx x = k ? cmul(g, e[4]) : g; TA[(24 + 2 * a + k) * RS] = pack2(x.r, -x.i); }
        g = cmul(g, e128);
      }
    }
  }
  STAMP(p.ws, 1);
  *(LAS u2*)(L + KG_X + (tid >> 3) * 136 + (tid & 7) * 16) = u2{xt.x, xt.y};
  *(LAS u2*)(L + KG_X + (tid >> 3) * 136 + (tid & 7) * 16 + 8) = u2{xt.z, xt.w};
  {
    float s = 0.f, q = 0.f;
#pragma unroll
    for (int i = 0; i < 4; ++i) { const float a = blo(xr[i]), b = bhi(xr[i]); s += a + b; q += a * a + b * b; }
    s = wave_sum(s); q = wave_sum(q);
    if (lane == 0) ((GAS f2*)(p.ws + WS_ST))[8 * c + w] = f2{s, q};
  }
  if (w < 6) {
    f4 d0 = mfma16(wa0, wb, f4{0, 0, 0, 0}), d1 = mfma16(wa1, wb, f4{0, 0, 0, 0});
    if (fm < 2) {
      const bool f0 = (lane & 15) == 0;
#pragma unroll
      for (int r = 0; r < 4; ++r) {
        const float b0 = dpp_f<0x150>(d0[r]), b1 = dpp_f<0x150>(d1[r]);
        d0[r] = f0 ? 1.f : d0[r] - b0; d1[r] = f0 ? 1.f : d1[r] - b1;
      }
    }
    *(GAS u4*)(p.ws + WS_WFRAG + (size_t)F * 1024 + lane * 16) = u4{pack2(d0[0], d0[1]), pack2(d0[2], d0[3]), pack2(d1[0], d1[1]), pack2(d1[2], d1[3])};
  }
  __syncthreads();
  {
    const int g = lane >> 4, q = (lane & 15) >> 2, pp = lane & 3;
    lds_t* a = L + KG_X + (8 * w + q) * 136 + (16 * g + 4 * pp) * 2;
    const u2 t0 = lds_tr(a), t1 = lds_tr(a + 4 * 136);
    *(GAS u4*)(p.ws + WS_XT + ((size_t)(64 * cb + lane) * XT_STRIDE + 64 * pb + 8 * w) * 2) = cat(t0, t1);
  }
  STAMP(p.ws, 2);
  if (w < 6) {
    const int ch = w / 3, mt = w % 3;
    f4 acc = {0, 0, 0, 0};
    lds_t* a = L + KG_A + (ch * 48 + 16 * mt + (lane & 15)) * KG_ROW + (lane >> 4) * 16;
    lds_t* b = L + KG_B + (ch * 16 + (lane & 15)) * KG_ROW + (lane >> 4) * 16;
#pragma unroll
    for (int ks = 0; ks < 8; ++ks) acc = mfma32(*(LAS u4*)(a + ks * 64), *(LAS u4*)(b + ks * 64), acc);
    LAS float* T = (LAS float*)(L + KG_T) + ch * 2056;
    const int j = lane & 15, g = lane >> 4;
#pragma unroll
    for (int r = 0; r < 4; ++r) {
      const int i = 4 * g + r;
      int ph;
      if (mt == 0) ph = 128 * (i >> 3) + (i & 7);
      else if (mt == 1) ph = i < 8 ? 256 + 128 * (i >> 2) + 2 * (i & 3) : 512 + 128 * ((i - 8) >> 1) + 4 * (i & 1);
      else ph = 1024 + 128 * i;
      const int pos = ph + 8 * j;
      if (pos <= 2048) T[pos] = acc[r];
    }
  }
  __syncthreads();
  {
    const int ch = tid >> 8, q = tid & 255;
    LAS float* T = (LAS float*)(L + KG_T) + ch * 2056;
    float v[8];
#pragma unroll
    for (int j = 0; j < 8; ++j) {
      const int l = 2047 - (8 * q + j);
      v[j] = T[(int)rbf((float)l)];
    }
    *(GAS u4*)(p.ws + WS_KREV + ((size_t)(2 * c + ch) * 2048 + 8 * q) * 2) = u4{pack2(v[0], v[1]), pack2(v[2], v[3]), pack2(v[4], v[5]), pack2(v[6], v[7])};
  }
  STAMP(p.ws, 3);
}

constexpr int LD_XT = 0;
constexpr int LD_KC = 65536;
constexpr int KC_COPY = 4160, KC_CH = 4 * KC_COPY;
constexpr int LD_U = LD_KC + 2 * KC_CH;
constexpr int U_CH = 4608;
constexpr int LD_X = LD_U + 2 * U_CH;
constexpr int LD_EX = 0;
constexpr int LD_CONV_END = LD_X + 8192;
constexpr int LD_MISC = 131072;
constexpr int LD_PV = LD_MISC;
constexpr int PV_ROW = 104, PV_WAVE = 64 * PV_ROW;
constexpr int LD_STAT = LD_PV + 4 * PV_WAVE;
constexpr int LD_FLAG = LD_STAT + 1024;
constexpr int LD_OST = LD_PV;
constexpr int LDS_BYTES = 163840;
static_assert(LD_CONV_END <= LD_MISC && LD_FLAG + 64 <= LDS_BYTES, "LDS map");

struct Ctx {
  lds_t* L; rsrc_t ws; GAS unsigned* cw; unsigned char* wsp;
  int tid, lane, w, c, rb, hp, layer;
};

DI void spin_fail(const Ctx& x) { __hip_atomic_store(x.cw + CW_TMO, 1u, RLX_AGENT); }
constexpr unsigned SPIN_CAP = 1u << 18;
DI void a2a_signal(const Ctx& x, int e) {
  drain(); __syncthreads();
  if (x.tid < 8) __hip_atomic_fetch_add(x.cw + (CWI_A2A + (e * 8 + x.tid) * 16 + (x.c >> 4)) * CW_STRIDE, 1u, RLX_AGENT);
}
DI void a2a_wait(const Ctx& x, int e) {
  if (x.w == 0) {
    GAS unsigned* q = x.cw + (CWI_A2A + (e * 8 + (x.c & 7)) * 16 + (x.lane & 15)) * CW_STRIDE;
    for (unsigned s = 0;; ++s) {
      const unsigned v = __hip_atomic_load(q, RLX_AGENT);
      if (__all(v >= 16u)) break;
      if (s > SPIN_CAP) { spin_fail(x); break; }
    }
    __builtin_amdgcn_fence(__ATOMIC_ACQUIRE, "wavefront");
  }
  __syncthreads();
}
DI void grp_signal(const Ctx& x, int e) {
  drain(); __syncthreads();
  if (x.tid == 0) __hip_atomic_fetch_add(x.cw + (CWI_GRP + e) * CW_STRIDE, 1u, RLX_AGENT);
}
DI void grp_wait(const Ctx& x, int e) {
  if (x.w == 0) {
    GAS unsigned* q = x.cw + (CWI_GRP + e) * CW_STRIDE;
    for (unsigned s = 0;; ++s) {
      const unsigned v = __hip_atomic_load(q, RLX_AGENT);
      if (__builtin_amdgcn_readfirstlane(v) >= 16u) break;
      if (s > SPIN_CAP) { spin_fail(x); break; }
    }
    __builtin_amdgcn_fence(__ATOMIC_ACQUIRE, "wavefront");
  }
  __syncthreads();
}

DI void conv_load_k(const Ctx& x, u4 (&kd)[3]) {
  const int ch = x.tid >> 7, q = x.tid & 127;
  const unsigned off = WS_KREV + ((2 * x.c + ch) * 2048 + 16 * q) * 2;
  kd[0] = __builtin_amdgcn_raw_buffer_load_b128(x.ws, off, 0, 0);
  kd[1] = __builtin_amdgcn_raw_buffer_load_b128(x.ws, off + 16, 0, 0);
  kd[2] = __builtin_amdgcn_raw_buffer_load_b128(x.ws, off + 32, 0, 0);
}
DI void conv_build_k(const Ctx& x, const u4 (&kd)[3]) {
  const int ch = x.tid >> 7, q = x.tid & 127;
  const unsigned d8 = q == 127 ? 0u : kd[2].x, d9 = q == 127 ? 0u : kd[2].y;
  const unsigned d[10] = {kd[0].x, kd[0].y, kd[0].z, kd[0].w, kd[1].x, kd[1].y, kd[1].z, kd[1].w, d8, d9};
  lds_t* b = x.L + LD_KC + ch * KC_CH + 32 * q;
  *(LAS u4*)(b) = u4{d[0], d[1], d[2], d[3]}; *(LAS u4*)(b + 16) = u4{d[4], d[5], d[6], d[7]};
  *(LAS u4*)(b + 2 * KC_COPY) = u4{d[1], d[2], d[3], d[4]}; *(LAS u4*)(b + 2 * KC_COPY + 16) = u4{d[5], d[6], d[7], d[8]};
  unsigned e[9];
#pragma unroll
  for (int i = 0; i < 9; ++i) e[i] = __builtin_amdgcn_alignbit(d[i + 1], d[i], 16);
  *(LAS u4*)(b + KC_COPY) = u4{e[0], e[1], e[2], e[3]}; *(LAS u4*)(b + KC_COPY + 16) = u4{e[4], e[5], e[6], e[7]};
  *(LAS u4*)(b + 3 * KC_COPY) = u4{e[1], e[2], e[3], e[4]}; *(LAS u4*)(b + 3 * KC_COPY + 16) = u4{e[5], e[6], e[7], e[8]};
  if (x.tid < 16) {
    *(LAS u4*)(x.L + LD_KC + (x.tid >> 1) * KC_COPY + 4096 + (x.tid & 1) * 16) = u4{0, 0, 0, 0};
  }
  if (x.tid >= 64 && x.tid < 128) {
    const int i = x.tid - 64;
    *(LAS u4*)(x.L + LD_U + (i >> 5) * U_CH + (i & 31) * 16) = u4{0, 0, 0, 0};
  }
}
template <int PAR> struct Mu { static constexpr int v(int k) { return PAR ? (k == 0 ? 1 : k == 1 ? 2 : k == 2 ? 5 : 6) : (k == 0 ? 0 : k == 1 ? 3 : k == 2 ? 4 : 7); } };
template <int PAR> DI void conv_load_a(const Ctx& x, u4 (&a)[32]) {
  const int b = x.lane & 15, kq = x.lane >> 4, ch = x.w >> 1;
  const int s0 = 2047 - 16 * (kq >> 1) - b + 8 * (kq & 1), cc = s0 & 3;
  unsigned bo = LD_KC + ch * KC_CH + cc * KC_COPY + 2 * (s0 - cc) - 64 * 63;
  asm volatile("" : "+v"(bo));
  lds_t* base = x.L + bo;
#pragma unroll
  for (int k = 0; k < 4; ++k)
#pragma unroll
    for (int i = 0; i < 8; ++i) {
      const int m = 8 * Mu<PAR>::v(k) + i;
      a[8 * k + i] = cat(*(LAS u2*)(base + 64 * (63 - m)), *(LAS u2*)(base + 64 * (63 - m) + 8));
    }
}
DI void q_load_w(const Ctx& x, u4 (&wf)[48]) {
  const unsigned voff = WS_WFRAG + (2 * x.hp + (x.w & 1)) * 48 * 1024 + x.lane * 16;
#pragma unroll
  for (int i = 0; i < 48; ++i)
    asm volatile("buffer_load_dwordx4 %0, %1, %2, %3 offen" : "=a"(wf[i]) : "v"(voff), "s"(x.ws), "s"(i * 1024));
}
template <int PAR> DI void conv_mfma(const Ctx& x, const u4 (&a)[32], f4 (&acc)[8]) {
  const int al = x.lane & 15, kq = x.lane >> 4, ch = x.w >> 1;
  unsigned bo = LD_U + ch * U_CH + 2 * (256 + 16 * al - 16 * (kq >> 1) + 8 * (kq & 1)) - 64 * 7;
  asm volatile("" : "+v"(bo));
  lds_t* base = x.L + bo;
  constexpr int NT = 8 - Mu<PAR>::v(0);
  u4 b[8];
#pragma unroll
  for (int i = 0; i < 8; ++i) b[i] = *(LAS u4*)(base + 64 * (7 - i));
  SB();
#pragma unroll
  for (int tau = 0; tau < 8; ++tau) {
    if (tau < NT) {
      int ng = 0;
#pragma unroll
      for (int k = 0; k < 4; ++k) ng += (Mu<PAR>::v(k) + tau <= 7);
#pragma unroll
      for (int i = 0; i < 8; ++i) {
#pragma unroll
        for (int k = 0; k < 4; ++k)
          if (Mu<PAR>::v(k) + tau <= 7) acc[Mu<PAR>::v(k) + tau] = mfma32(a[8 * k + i], b[i], acc[Mu<PAR>::v(k) + tau]);
        if (tau + 1 < NT) b[i] = *(LAS u4*)(base + 64 * (8 * (tau + 1) - i + 7));
      }
      if (tau + 1 < NT) {
#pragma unroll
        for (int i = 0; i < 8; ++i) {
          if (ng == 4) SGB(SG_MFMA, 4); else if (ng == 3) SGB(SG_MFMA, 3); else if (ng == 2) SGB(SG_MFMA, 2); else SGB(SG_MFMA, 1);
          SGB(SG_DSR, 1);
        }
      }
    }
    SB();
  }
}

struct ConvIn { u4 x0, x1, s[4]; };
DI void conv_load_in(const Ctx& x, int layer, ConvIn& in) {
  const unsigned xo = WS_XT + layer * XT_BYTES + (2 * x.c * XT_STRIDE + 8 * x.tid) * 2;
  in.x0 = ld16(x.ws, xo); in.x1 = ld16(x.ws, xo + XT_STRIDE * 2);
  const unsigned so = WS_ST + layer * 2048 * 8 + x.tid * 64;
#pragma unroll
  for (int i = 0; i < 4; ++i) in.s[i] = ld16(x.ws, so + 16 * i);
}
DI void conv_build_u(const Ctx& x, const ConvIn& in) {
  float m[8], r[8];
#pragma unroll
  for (int i = 0; i < 4; ++i) {
    const f4 s = __builtin_bit_cast(f4, in.s[i]);
    m[2 * i] = s[0] * (1.f / DM); m[2 * i + 1] = s[2] * (1.f / DM);
    r[2 * i] = __builtin_amdgcn_rsqf(fmaxf(s[1] * (1.f / DM) - m[2 * i] * m[2 * i], 0.f) + LN_EPS);
    r[2 * i + 1] = __builtin_amdgcn_rsqf(fmaxf(s[3] * (1.f / DM) - m[2 * i + 1] * m[2 * i + 1], 0.f) + LN_EPS);
  }
  u4 u0, u1;
#pragma unroll
  for (int i = 0; i < 4; ++i) {
    u0[i] = pack2((blo(in.x0[i]) - m[2 * i]) * r[2 * i], (bhi(in.x0[i]) - m[2 * i + 1]) * r[2 * i + 1]);
    u1[i] = pack2((blo(in.x1[i]) - m[2 * i]) * r[2 * i], (bhi(in.x1[i]) - m[2 * i + 1]) * r[2 * i + 1]);
  }
  *(LAS u4*)(x.L + LD_U + 512 + 16 * x.tid) = u0; *(LAS u4*)(x.L + LD_U + U_CH + 512 + 16 * x.tid) = u1;
  *(LAS u4*)(x.L + LD_X + 16 * x.tid) = in.x0; *(LAS u4*)(x.L + LD_X + 4096 + 16 * x.tid) = in.x1;
}
template <int PAR> DI void conv_wave(const Ctx& x, float dsk, const u4 (&a)[32]) {
  const int ch = x.w >> 1;
  f4 acc[8];
#pragma unroll
  for (int t = 0; t < 8; ++t) acc[t] = f4{0, 0, 0, 0};
  conv_mfma<PAR>(x, a, acc);
  STAMP(x.wsp, 6 + 16 * x.layer);
  lds_t* mine = x.L + LD_EX + x.w * 4096 + x.lane * 16;
  lds_t* other = x.L + LD_EX + (x.w ^ 1) * 4096 + x.lane * 16;
#pragma unroll
  for (int t = 0; t < 4; ++t) *(LAS f4*)(mine + t * 1024) = acc[PAR ? t : 4 + t];
  const int al = x.lane & 15, g = x.lane >> 4;
  u2 uu[4], xx[4];
#pragma unroll
  for (int t = 0; t < 4; ++t) {
    const int pos = 256 * (4 * PAR + t) + 16 * al + 4 * g;
    uu[t] = *(LAS u2*)(x.L + LD_U + ch * U_CH + 512 + 2 * pos);
    xx[t] = *(LAS u2*)(x.L + LD_X + ch * 4096 + 2 * pos);
  }
  __syncthreads();
#pragma unroll
  for (int t = 0; t < 4; ++t) {
    const f4 y = acc[PAR ? 4 + t : t] + *(LAS f4*)(other + t * 1024);
    const int pos = 256 * (4 * PAR + t) + 16 * al + 4 * g;
    *(LAS u2*)(x.L + LD_X + ch * 4096 + 2 * pos) =
        u2{pack2(blo(xx[t].x) + (y[0] + blo(uu[t].x) * dsk), bhi(xx[t].x) + (y[1] + bhi(uu[t].x) * dsk)),
           pack2(blo(xx[t].y) + (y[2] + blo(uu[t].y) * dsk), bhi(xx[t].y) + (y[3] + bhi(uu[t].y) * dsk))};
  }
}
DI void conv_store(const Ctx& x) {
  __syncthreads();
  STAMP(x.wsp, 7 + 16 * x.layer);
#pragma unroll
  for (int i = 0; i < 2; ++i) {
    const int k = i * 256 + x.tid, rb = k >> 5, cl = (k >> 4) & 1, q = k & 15;
    const u4 v = *(LAS u4*)(x.L + LD_X + cl * 4096 + (rb * 128 + 8 * q) * 2);
    st16(x.ws, WS_X1 + x.layer * X1_BYTES + ((rb * 512 + 2 * x.c + cl) * 128 + 8 * q) * 2, v);
  }
}

DI void q_wsum(const u4 (&wf)[48], f4 (&ws)[3]) {
  const u4 ones = {0x3f803f80u, 0x3f803f80u, 0x3f803f80u, 0x3f803f80u};
  ws[0] = f4{0, 0, 0, 0}; ws[1] = ws[0]; ws[2] = ws[0];
#pragma unroll
  for (int ks = 0; ks < 16; ++ks)
#pragma unroll
    for (int m = 0; m < 3; ++m) ws[m] = mfma32(wf[16 * m + ks], ones, ws[m]);
}
DI void q_dma_x(const Ctx& x) {
  const int row = x.lane >> 4, pc = x.lane & 15;
  const unsigned v0 = row * 256 + ((pc ^ (row << 1)) * 16), v1 = row * 256 + ((pc ^ ((4 + row) << 1)) * 16);
  const unsigned base = WS_X1 + x.layer * X1_BYTES + x.rb * (512 * 256) + x.w * 4096;
#pragma unroll
  for (int k = 0; k < 8; ++k) dma4_sc1(x.ws, v0, v1, base + k * 16384, LD_XT + x.w * 4096 + k * 16384);
}
struct QAddr { lds_t* lo[4]; lds_t* hi[4]; };
DI void q_frag_addr(const Ctx& x, QAddr& ba) {
  const int g = x.lane >> 4, q = (x.lane & 15) >> 2, pp = x.lane & 3, ph = x.w >> 1;
  const int r7 = 4 * (g & 1) + q;
#pragma unroll
  for (int pt = 0; pt < 4; ++pt) {
    const int lc = 8 * ph + 2 * (pt ^ (2 * (x.w & 1))) + (pp >> 1);
    ba.lo[pt] = x.L + LD_XT + (4 * g + q) * 256 + ((lc ^ (r7 << 1)) * 16) + 8 * (pp & 1);
    ba.hi[pt] = ba.lo[pt] + 65536;
  }
}
template <int KS> DI void q_ldb(const QAddr& ba, u4 (&b)[4]) {
#pragma unroll
  for (int pt = 0; pt < 4; ++pt) {
    lds_t* p = (KS < 8 ? ba.lo[pt] : ba.hi[pt]) + (KS & 7) * 8192;
    b[pt] = cat(lds_tr(p), lds_tr(p + 4096));
  }
}
template <int N> DI void wait_vm_barrier() { asm volatile("s_waitcnt vmcnt(%0)\n\ts_barrier" :: "n"(N) : "memory"); }
DI u4 softmax_pm(f4 acc, f4 wsum, float mean, float rs) {
  const float cs = rs * LOG2E;
  f4 t;
#pragma unroll
  for (int r = 0; r < 4; ++r) t[r] = acc[r] - mean * wsum[r];
  const float mx = fmaxf(fmaxf(t[0], t[1]), fmaxf(t[2], t[3])), mn = fminf(fminf(t[0], t[1]), fminf(t[2], t[3]));
  const f2 mm = xrow_max2(mx, -mn);
  const float bp = -cs * mm.x, bn = -cs * mm.y;
  f4 ep, en;
#pragma unroll
  for (int r = 0; r < 4; ++r) { ep[r] = __builtin_amdgcn_exp2f(fmaf(cs, t[r], bp)); en[r] = __builtin_amdgcn_exp2f(fmaf(-cs, t[r], bn)); }
  const f2 ss = xrow_sum2((ep[0] + ep[1]) + (ep[2] + ep[3]), (en[0] + en[1]) + (en[2] + en[3]));
  const float ip = __builtin_amdgcn_rcpf(ss.x), in = __builtin_amdgcn_rcpf(ss.y);
  return u4{pack2(ep[0] * ip, ep[1] * ip), pack2(ep[2] * ip, ep[3] * ip), pack2(en[0] * in, en[1] * in), pack2(en[2] * in, en[3] * in)};
}

struct QState { u4 pq[4]; u2 res[4]; };

template <int KS> DI u4 q_ldb1(const QAddr& ba, int pt) {
  lds_t* p = (KS < 8 ? ba.lo[pt] : ba.hi[pt]) + (KS & 7) * 8192;
  return cat(lds_tr(p), lds_tr(p + 4096));
}
template <int KS> struct QPass1 {
  static DI void run(const QAddr& ba, const u4 (&wf)[48], u4 (&b)[4], f4 (&ak)[4], f4 (&av)[4], f4 (&ag)[2]) {
    if (KS < 15 && (KS & 1)) wait_vm_barrier<(KS < 15 ? 28 - 2 * (KS + 1) : 0)>();
#pragma unroll
    for (int pt = 0; pt < 4; ++pt) {
      ak[pt] = mfma32(wf[16 + KS], b[pt], ak[pt]); av[pt] = mfma32(wf[32 + KS], b[pt], av[pt]);
      if (pt < 2) ag[pt] = mfma32(b[pt], b[pt], ag[pt]);
      if (KS < 15) b[pt] = q_ldb1<(KS < 15 ? KS + 1 : 15)>(ba, pt);
    }
    if (KS < 15) {
#pragma unroll
      for (int pt = 0; pt < 4; ++pt) { if (pt < 2) SGB(SG_MFMA, 3); else SGB(SG_MFMA, 2); SGB(SG_DSR, 2); }
    }
    SB();
    QPass1<KS + 1>::run(ba, wf, b, ak, av, ag);
  }
};
template <> struct QPass1<16> { static DI void run(const QAddr&, const u4 (&)[48], u4 (&)[4], f4 (&)[4], f4 (&)[4], f4 (&)[2]) {} };
template <int KS> struct QPass2 {
  static DI void run(const QAddr& ba, const u4 (&wf)[48], u4 (&b)[3][4], f4 (&aq)[4]) {
    if (KS < 14) q_ldb<(KS < 14 ? KS + 2 : 15)>(ba, b[(KS + 2) % 3]);
#pragma unroll
    for (int pt = 0; pt < 4; ++pt) aq[pt] = mfma32(wf[KS], b[KS % 3][pt], aq[pt]);
    if (KS < 14) {
#pragma unroll
      for (int i = 0; i < 4; ++i) { SGB(SG_MFMA, 1); SGB(SG_DSR, 2); }
    }
    SB();
    QPass2<KS + 1>::run(ba, wf, b, aq);
  }
};
template <> struct QPass2<16> { static DI void run(const QAddr&, const u4 (&)[48], u4 (&)[3][4], f4 (&)[4]) {} };

DI void o_dma_wo(const Ctx& x, const Params& p) {
  const rsrc_t r = make_rsrc(p.wo, 512 * 512 * 2);
  const int row = x.lane >> 3, pc = x.lane & 7, cb = x.hp >> 1;
  const int b1 = (row >> 1) & 1;
  const unsigned v0 = row * 1024 + ((pc ^ (b1 << 1)) * 16), v1 = row * 1024 + ((pc ^ ((b1 | 2) << 1)) * 16) + 7168;
#pragma unroll
  for (int i = 0; i < 4; ++i) {
    const unsigned k0 = 16 * x.w + 4 * i;
    dma4(r, v0, v1, v0 + 2 * 7168, v1 + 2 * 7168, k0 * 8192 + cb * 128, LD_XT + k0 * 1024);
  }
}

DI void q_phase(const Ctx& x, const Params& p, const u4 (&wf)[48], const f4 (&wsum)[3], QState& st) {
  QAddr ba;
  q_frag_addr(x, ba);
  const int h = x.w & 1, ph = x.w >> 1, g = x.lane >> 4, cl = x.lane & 15;
  f4 ak[4], av[4], ag[2];
#pragma unroll
  for (int i = 0; i < 4; ++i) { ak[i] = f4{0, 0, 0, 0}; av[i] = f4{0, 0, 0, 0}; }
  ag[0] = f4{0, 0, 0, 0}; ag[1] = f4{0, 0, 0, 0};
  STAMP(x.wsp, 9 + 16 * x.layer);
  {
    u4 b[4];
    wait_vm_barrier<28>();
    q_ldb<0>(ba, b);
    SB();
    QPass1<0>::run(ba, wf, b, ak, av, ag);
  }
  STAMP(x.wsp, 10 + 16 * x.layer);
  LAS float* S = (LAS float*)(x.L + LD_STAT);
#pragma unroll
  for (int i = 0; i < 2; ++i) {
    const int pos = 64 * ph + 16 * (2 * h + i) + cl;
    const int r = cl & 3;
    const float dq = r == 0 ? ag[i][0] : r == 1 ? ag[i][1] : r == 2 ? ag[i][2] : ag[i][3];
    if (g == 0) S[pos] = ak[i][0];
    if (g == (cl >> 2)) S[128 + pos] = dq;
  }
  __syncthreads();
  float mean[4], rs[4];
#pragma unroll
  for (int pt = 0; pt < 4; ++pt) {
    const int pos = 64 * ph + 16 * (pt ^ (2 * h)) + cl;
    mean[pt] = S[pos] * (1.f / DM);
    rs[pt] = __builtin_amdgcn_rsqf(fmaxf(S[128 + pos] * (1.f / DM) - mean[pt] * mean[pt], 0.f) + LN_EPS);
  }
  lds_t* pv = x.L + LD_PV + x.w * PV_WAVE;
#pragma unroll
  for (int pt = 0; pt < 4; ++pt) {
    const u4 pk = softmax_pm(ak[pt], wsum[1], mean[pt], rs[pt]);
    f4 v;
#pragma unroll
    for (int r = 0; r < 4; ++r) v[r] = rs[pt] * (av[pt][r] - mean[pt] * wsum[2][r]);
    lds_t* row = pv + (16 * pt + cl) * PV_ROW + 8 * g;
    *(LAS u2*)(row) = u2{pk.x, pk.y}; *(LAS u2*)(row + 32) = u2{pk.z, pk.w}; *(LAS u2*)(row + 64) = u2{pack2(v[0], v[1]), pack2(v[2], v[3])};
  }
  f4 kv0 = {0, 0, 0, 0}, kv1 = kv0, z0 = kv0, z1 = kv0;
  {
    const u4 ones = {0x3f803f80u, 0x3f803f80u, 0x3f803f80u, 0x3f803f80u};
    const int q = cl >> 2, pp = cl & 3;
#pragma unroll
    for (int k2 = 0; k2 < 2; ++k2) {
      lds_t* a = pv + (32 * k2 + 8 * g + q) * PV_ROW + 8 * pp;
      const u4 a0 = cat(lds_tr(a), lds_tr(a + 4 * PV_ROW));
      const u4 a1 = cat(lds_tr(a + 32), lds_tr(a + 4 * PV_ROW + 32));
      const u4 bv = cat(lds_tr(a + 64), lds_tr(a + 4 * PV_ROW + 64));
      kv0 = mfma32(a0, bv, kv0); kv1 = mfma32(a1, bv, kv1); z0 = mfma32(a0, ones, z0); z1 = mfma32(a1, ones, z1);
    }
  }
  STAMP(x.wsp, 11 + 16 * x.layer);
  {
    lds_t* mine = x.L + LD_PV + x.w * PV_WAVE + x.lane * 16;
    if (ph == 1) { *(LAS f4*)(mine) = kv0; *(LAS f4*)(mine + 1024) = kv1; *(LAS f4*)(mine + 2048) = z0; *(LAS f4*)(mine + 3072) = z1; }
    __syncthreads();
    if (ph == 0) {
      lds_t* o = x.L + LD_PV + (x.w + 2) * PV_WAVE + x.lane * 16;
      kv0 += *(LAS f4*)(o); kv1 += *(LAS f4*)(o + 1024); z0 += *(LAS f4*)(o + 2048); z1 += *(LAS f4*)(o + 3072);
      const int hh = 2 * x.hp + h;
      const unsigned ko = WS_KVP + x.layer * KVP_BYTES + (hh * 16 + x.rb) * 2048 + x.lane * 16;
      st16(x.ws, ko, __builtin_bit_cast(u4, kv0)); st16(x.ws, ko + 1024, __builtin_bit_cast(u4, kv1));
      if (cl < 2) st16(x.ws, WS_ZP + x.layer * ZP_BYTES + (hh * 16 + x.rb) * 128 + cl * 64 + g * 16, __builtin_bit_cast(u4, cl ? z1 : z0));
    }
  }
  grp_signal(x, (x.layer * 2 + 0) * 16 + x.hp);
  STAMP(x.wsp, 12 + 16 * x.layer);
  {
    f4 aq[4];
#pragma unroll
    for (int i = 0; i < 4; ++i) aq[i] = f4{0, 0, 0, 0};
    u4 b[3][4];
    q_ldb<0>(ba, b[0]); q_ldb<1>(ba, b[1]);
    SB();
    QPass2<0>::run(ba, wf, b, aq);
    const int cb = x.hp >> 1, p0 = 64 * (x.hp & 1) + 16 * x.w;
#pragma unroll
    for (int ct = 0; ct < 4; ++ct) {
      const int q = cl >> 2, pp = cl & 3;
      const int row = 64 * cb + 16 * ct + 4 * g + q, pos = p0 + 4 * pp;
      st.res[ct] = lds_tr(x.L + LD_XT + row * 256 + (((pos >> 3) ^ ((row & 7) << 1)) * 16) + (pos & 4) * 2);
    }
    __syncthreads();
    o_dma_wo(x, p);
#pragma unroll
    for (int pt = 0; pt < 4; ++pt) st.pq[pt] = softmax_pm(aq[pt], wsum[0], mean[pt], rs[pt]);
  }
  STAMP(x.wsp, 13 + 16 * x.layer);
}

DI void o_compute(const Ctx& x, const QState& st) {
  const int h = x.w & 1, ph = x.w >> 1, g = x.lane >> 4, cl = x.lane & 15, hh = 2 * x.hp + h;
  STAMP(x.wsp, 14 + 16 * x.layer);
  u4 pk[16];
  const unsigned ko = WS_KVP + x.layer * KVP_BYTES + (hh * 16 + 8 * ph) * 2048 + x.lane * 16;
  const unsigned zo = WS_ZP + x.layer * ZP_BYTES + (hh * 16 + cl) * 128 + g * 16;
  const u4 zq0 = ld16(x.ws, zo), zq1 = ld16(x.ws, zo + 64);
#pragma unroll
  for (int i = 0; i < 16; ++i) pk[i] = ld16(x.ws, ko + i * 1024);
  SB();
  f4 z0 = __builtin_bit_cast(f4, zq0), z1 = __builtin_bit_cast(f4, zq1);
#pragma unroll
  for (int r = 0; r < 4; ++r) { z0[r] = row_sum(z0[r]); z1[r] = row_sum(z1[r]); }
  f4 k0 = {0, 0, 0, 0}, k1 = k0;
#pragma unroll
  for (int i = 0; i < 8; ++i) { k0 += __builtin_bit_cast(f4, pk[2 * i]); k1 += __builtin_bit_cast(f4, pk[2 * i + 1]); }
  lds_t* mine = x.L + LD_PV + 8192 + x.w * 2048 + x.lane * 16;
  *(LAS f4*)(mine) = k0; *(LAS f4*)(mine + 1024) = k1;
  __syncthreads();
  STAMP(x.wsp, 15 + 16 * x.layer);
  lds_t* o = x.L + LD_PV + 8192 + (x.w ^ 2) * 2048 + x.lane * 16;
  k0 += *(LAS f4*)(o); k1 += *(LAS f4*)(o + 1024);
  const u4 akv = {pack2(k0[0], k0[1]), pack2(k0[2], k0[3]), pack2(k1[0], k1[1]), pack2(k1[2], k1[3])};
  const u4 az = {pack2(z0[0], z0[1]), pack2(z0[2], z0[3]), pack2(z1[0], z1[1]), pack2(z1[2], z1[3])};
#pragma unroll
  for (int pt = 0; pt < 4; ++pt) {
    const f4 num = mfma32(akv, st.pq[pt], f4{0, 0, 0, 0}), den = mfma32(az, st.pq[pt], f4{0, 0, 0, 0});
    const float id = __builtin_amdgcn_rcpf(den[0] + 1e-6f);
    *(LAS u2*)(x.L + LD_OST + (64 * ph + 16 * (pt ^ (2 * h)) + cl) * 64 + h * 32 + g * 8) = u2{pack2(num[0] * id, num[1] * id), pack2(num[2] * id, num[3] * id)};
  }
  __syncthreads();
#pragma unroll
  for (int i = 0; i < 2; ++i) {
    const u4 v = *(LAS u4*)(x.L + LD_OST + i * 4096 + x.tid * 16);
    st16(x.ws, WS_OB + x.layer * OB_BYTES + ((2 * x.rb + i) * 16 + x.hp) * 4096 + x.tid * 16, v);
  }
}

struct OAddr { lds_t* p[4]; };
template <int KS> DI void o_ldw(const OAddr& wb, u4 (&b)[4]) {
#pragma unroll
  for (int ct = 0; ct < 4; ++ct) b[ct] = cat(lds_tr(wb.p[ct] + KS * 4096), lds_tr(wb.p[ct] + KS * 4096 + 512));
}
template <int KS> struct OGemm {
  static DI void run(const OAddr& wb, const u4 (&of)[16], u4 (&b)[3][4], f4 (&acc)[4]) {
    if (KS < 14) o_ldw<(KS < 14 ? KS + 2 : 15)>(wb, b[(KS + 2) % 3]);
#pragma unroll
    for (int ct = 0; ct < 4; ++ct) acc[ct] = mfma32(b[KS % 3][ct], of[KS], acc[ct]);
    if (KS < 14) {
#pragma unroll
      for (int i = 0; i < 4; ++i) { SGB(SG_MFMA, 1); SGB(SG_DSR, 2); }
    }
    SB();
    OGemm<KS + 1>::run(wb, of, b, acc);
  }
};
template <> struct OGemm<16> { static DI void run(const OAddr&, const u4 (&)[16], u4 (&)[3][4], f4 (&)[4]) {} };

DI void o_gemm(const Ctx& x, const Params& p, const QState& st) {
  const int g = x.lane >> 4, cl = x.lane & 15, pb = 2 * x.rb + (x.hp & 1), cb = x.hp >> 1;
  STAMP(x.wsp, 17 + 16 * x.layer);
  u4 of[16];
  const unsigned oo = WS_OB + x.layer * OB_BYTES + pb * 65536 + (16 * x.w + cl) * 64 + g * 16;
#pragma unroll
  for (int ks = 0; ks < 16; ++ks) of[ks] = ld16(x.ws, oo + ks * 4096);
  OAddr wb;
  {
    const int q = cl >> 2, pp = cl & 3, f = ((q >> 1) & 1) | ((g & 1) << 1);
#pragma unroll
    for (int ct = 0; ct < 4; ++ct) wb.p[ct] = x.L + LD_XT + (8 * g + q) * 128 + (((2 * ct + (pp >> 1)) ^ (f << 1)) * 16) + 8 * (pp & 1);
  }
  f4 acc[4];
#pragma unroll
  for (int i = 0; i < 4; ++i) acc[i] = f4{0, 0, 0, 0};
  u4 b[3][4];
  o_ldw<0>(wb, b[0]); o_ldw<1>(wb, b[1]);
  SB();
  OGemm<0>::run(wb, of, b, acc);
  STAMP(x.wsp, 18 + 16 * x.layer);
  float s = 0.f, q = 0.f;
#pragma unroll
  for (int ct = 0; ct < 4; ++ct) {
    const unsigned a = pack2(blo(st.res[ct].x) + acc[ct][0], bhi(st.res[ct].x) + acc[ct][1]);
    const unsigned b2 = pack2(blo(st.res[ct].y) + acc[ct][2], bhi(st.res[ct].y) + acc[ct][3]);
    const float v0 = blo(a), v1 = bhi(a), v2 = blo(b2), v3 = bhi(b2);
    s += (v0 + v1) + (v2 + v3); q += (v0 * v0 + v1 * v1) + (v2 * v2 + v3 * v3);
    *(LAS u2*)(x.L + LD_OST + (16 * x.w + cl) * 144 + (16 * ct + 4 * g) * 2) = u2{a, b2};
  }
  if (x.layer == 0) {
    const f2 sq = xrow_sum2(s, q);
    if (g < 2) {
      GAS float* d = (GAS float*)(p.ws + WS_ST + 2048 * 8) + (64 * pb + 16 * x.w + cl) * 2 + g;
      __hip_atomic_fetch_add(d, g ? sq.y : sq.x, RLX_AGENT);
    }
  }
  __syncthreads();
  if (x.layer == 0) {
    const int qq = cl >> 2, pp = cl & 3;
#pragma unroll
    for (int j = 0; j < 2; ++j) {
      lds_t* a = x.L + LD_OST + (8 * (g + 4 * j) + qq) * 144 + (16 * x.w + 4 * pp) * 2;
      const u4 v = cat(lds_tr(a), lds_tr(a + 4 * 144));
      st16(x.ws, WS_XT + XT_BYTES + ((64 * cb + 16 * x.w + cl) * XT_STRIDE + 64 * pb + 8 * (g + 4 * j)) * 2, v);
    }
  } else {
#pragma unroll
    for (int i = 0; i < 2; ++i) {
      const int k = i * 256 + x.tid, pos = k >> 3, q8 = k & 7;
      *(GAS u4*)(p.out + (64 * pb + pos) * DM + 64 * cb + 8 * q8) = *(LAS u4*)(x.L + LD_OST + pos * 144 + q8 * 16);
    }
  }
  STAMP(x.wsp, 19 + 16 * x.layer);
}

DI Ctx make_ctx(const Params& p, unsigned char* smem) {
  Ctx x;
  x.L = (lds_t*)smem; x.ws = make_rsrc(p.ws, WS_END); x.cw = (GAS unsigned*)(p.ws + WS_CTRL); x.wsp = p.ws;
  x.tid = threadIdx.x; x.lane = x.tid & 63; x.w = __builtin_amdgcn_readfirstlane(x.tid >> 6); x.c = blockIdx.x;
  x.rb = 2 * (x.c & 7) + (x.c >> 7); x.hp = (x.c >> 3) & 15; x.layer = 0;
  return x;
}

DI Ctx fresh(const Ctx& x, int layer) { Ctx y = x; int t = x.tid; asm volatile("" : "+v"(t)); y.tid = t; y.lane = t & 63; y.layer = layer; return y; }

__global__ void __launch_bounds__(256, 1) k_main(Params p) {
  extern __shared__ __attribute__((aligned(16))) unsigned char smem[];
  const Ctx x0 = make_ctx(p, smem);
  STAMP(p.ws, 4);
  u4 wf[48], a[32];
  f4 wsum[3] = {f4{0, 0, 0, 0}, f4{0, 0, 0, 0}, f4{0, 0, 0, 0}};
  const float dsk = bf2f(p.dsk[2 * x0.c + (x0.w >> 1)]);
  {
    const Ctx x = fresh(x0, 0);
    u4 kd[3];
    ConvIn in;
    conv_load_k(x, kd);
    conv_load_in(x, 0, in);
    conv_build_k(x, kd);
    conv_build_u(x, in);
    __syncthreads();
    if (x.w & 1) conv_load_a<1>(x, a); else conv_load_a<0>(x, a);
    SB();
    q_load_w(x, wf);
  }
#pragma unroll 1
  for (int layer = 0; layer < 2; ++layer) {
    Ctx x = fresh(x0, layer);
    STAMP(p.ws, 5 + 16 * layer);
    if (x.w & 1) conv_wave<1>(x, dsk, a); else conv_wave<0>(x, dsk, a);
    x = fresh(x0, layer);
    conv_store(x);
    a2a_signal(x, 2 * layer);
    if (layer == 0) q_wsum(wf, wsum);
    a2a_wait(x, 2 * layer);
    STAMP(p.ws, 8 + 16 * layer);
    q_dma_x(x);
    QState st;
    x = fresh(x0, layer);
    q_phase(x, p, wf, wsum, st);
    x = fresh(x0, layer);
    grp_wait(x, (layer * 2 + 0) * 16 + x.hp);
    o_compute(x, st);
    grp_signal(x, (layer * 2 + 1) * 16 + x.rb);
    if (layer == 0) { u4 kd[3]; conv_load_k(x, kd); conv_build_k(x, kd); }
    STAMP(p.ws, 16 + 16 * layer);
    x = fresh(x0, layer);
    grp_wait(x, (layer * 2 + 1) * 16 + x.rb);
    o_gemm(x, p, st);
    if (layer == 0) {
      a2a_signal(x, 1);
      x = fresh(x0, layer);
      if (x.w & 1) conv_load_a<1>(x, a); else conv_load_a<0>(x, a);
      SB();
      a2a_wait(x, 1);
      STAMP(p.ws, 20);
      ConvIn in;
      conv_load_in(x, 1, in);
      conv_build_u(x, in);
      __syncthreads();
    }
  }
}

extern "C" void kernel_launch(void* const* d_in, const int* in_sizes, int n_in, void* d_out, int out_size, void* d_ws, size_t ws_size,
                              hipStream_t stream) {
  Params p;
  p.u = (const unsigned short*)d_in[0]; p.lar = (const unsigned short*)d_in[1]; p.aim = (const unsigned short*)d_in[2];
  p.cre = (const unsigned short*)d_in[3]; p.cim = (const unsigned short*)d_in[4]; p.ldt = (const unsigned short*)d_in[5];
  p.dsk = (const unsigned short*)d_in[6]; p.wqkv = (const unsigned short*)d_in[8]; p.wphi = (const unsigned short*)d_in[9];
  p.wo = (const unsigned short*)d_in[10]; p.out = (unsigned short*)d_out; p.ws = (unsigned char*)d_ws;
  (void)hipFuncSetAttribute((const void*)k_prep, hipFuncAttributeMaxDynamicSharedMemorySize, KG_LDS);
  (void)hipFuncSetAttribute((const void*)k_main, hipFuncAttributeMaxDynamicSharedMemorySize, LDS_BYTES);
  k_prep<<<256, 512, KG_LDS, stream>>>(p);
  k_main<<<256, 256, LDS_BYTES, stream>>>(p);
}
```

```cpp
#include <hip/hip_runtime.h>
#include <hip/hip_bf16.h>
#include <stdint.h>

#define MK_STAMP 0
#define STAMP(ws, i) do { } while (0)
#define STAMP2(x, i) do { } while (0)
#define STAMP3(x, i) do { } while (0)
typedef short s4 __attribute__((ext_vector_type(4)));
typedef short s8 __attribute__((ext_vector_type(8)));
typedef float f2 __attribute__((ext_vector_type(2)));
typedef float f4 __attribute__((ext_vector_type(4)));
typedef unsigned u2 __attribute__((ext_vector_type(2)));
typedef unsigned u4 __attribute__((ext_vector_type(4)));
typedef __bf16 bf2 __attribute__((ext_vector_type(2)));
#define GAS __attribute__((address_space(1)))
#define LAS __attribute__((address_space(3)))
#define DI __device__ __forceinline__
#define RLX_AGENT __ATOMIC_RELAXED, __HIP_MEMORY_SCOPE_AGENT
typedef GAS unsigned gu32;
typedef LAS unsigned char lds_t;
typedef __amdgpu_buffer_rsrc_t rsrc_t;

constexpr int SEQ = 2048, DM = 512, NST = 128, NH = 32;
constexpr float LOG2E = 1.4426950408889634f;
constexpr float LN_EPS = 1e-5f;
constexpr int AUX_SC1 = 16;

constexpr int XT_STRIDE = 2112;
constexpr unsigned WS_CTRL = 0;
constexpr unsigned WS_KREV = 262144;
constexpr unsigned WS_WFRAG = WS_KREV + 512u * 2048 * 2;
constexpr unsigned WS_XT = WS_WFRAG + 32u * 3 * 16 * 1024;
constexpr unsigned XT_BYTES = 512u * XT_STRIDE * 2;
constexpr unsigned WS_ST = WS_XT + 2 * XT_BYTES;
constexpr unsigned WS_X1 = WS_ST + 2u * 2048 * 8;
constexpr unsigned X1_BYTES = 2048u * 512 * 2;
constexpr unsigned WS_KVP = WS_X1 + 2 * X1_BYTES;
constexpr unsigned KVP_BYTES = 32u * 16 * 1024;
constexpr unsigned WS_ZP = WS_KVP + 2 * KVP_BYTES;
constexpr unsigned ZP_BYTES = 32u * 16 * 64;
constexpr unsigned WS_OB = WS_ZP + 2 * ZP_BYTES;
constexpr unsigned OB_BYTES = 2048u * 512 * 2;
constexpr unsigned WS_END = WS_OB + 2 * OB_BYTES;
constexpr int CW_TMO = 0;
constexpr int NCW_A2A = 3 * 8 * 16, NCW_GRP = 2 * 2 * 16;
constexpr int CW_STRIDE = 64;
constexpr int CWI_A2A = 1, CWI_GRP = CWI_A2A + NCW_A2A, CWI_END = CWI_GRP + NCW_GRP;
constexpr unsigned WS_EPOCH = 131072, WS_FLAG = WS_EPOCH + 256, WS_FLAG2 = WS_FLAG + 256 * 64;
static_assert(CWI_END * CW_STRIDE * 4 <= WS_EPOCH && WS_FLAG2 + 256 * 64 <= WS_KREV, "control block");

struct Params {
  const unsigned short* u;
  const unsigned short* lar;
  const unsigned short* aim;
  const unsigned short* cre;
  const unsigned short* cim;
  const unsigned short* ldt;
  const unsigned short* dsk;
  const unsigned short* wqkv;
  const unsigned short* wphi;
  const unsigned short* wo;
  unsigned short* out;
  unsigned char* ws;
};

DI float bf2f(unsigned short b) { return __builtin_bit_cast(float, (unsigned)b << 16); }
DI float blo(unsigned x) { return __builtin_bit_cast(float, x << 16); }
DI float bhi(unsigned x) { return __builtin_bit_cast(float, x & 0xffff0000u); }
DI unsigned pack2(float a, float b) { bf2 v = {(__bf16)a, (__bf16)b}; return __builtin_bit_cast(unsigned, v); }
DI float rbf(float a) { return blo(pack2(a, a)); }
DI f4 mfma32(u4 a, u4 b, f4 c) { return __builtin_amdgcn_mfma_f32_16x16x32_bf16(__builtin_bit_cast(s8, a), __builtin_bit_cast(s8, b), c, 0, 0, 0); }
DI f4 mfma16(u2 a, u2 b, f4 c) { return __builtin_amdgcn_mfma_f32_16x16x16bf16_1k(__builtin_bit_cast(s4, a), __builtin_bit_cast(s4, b), c, 0, 0, 0); }
DI u2 lds_tr(lds_t* p) { return __builtin_bit_cast(u2, __builtin_amdgcn_ds_read_tr16_b64_v4i16((LAS s4*)p)); }
DI u4 cat(u2 a, u2 b) { return u4{a.x, a.y, b.x, b.y}; }
DI rsrc_t make_rsrc(const void* p, unsigned bytes) { return __builtin_amdgcn_make_buffer_rsrc((void*)p, 0, (int)bytes, 0x00020000); }
DI u4 ld16(rsrc_t r, unsigned off) { return __builtin_amdgcn_raw_buffer_load_b128(r, off, 0, AUX_SC1); }
DI void st16(rsrc_t r, unsigned off, u4 v) { __builtin_amdgcn_raw_buffer_store_b128(v, r, off, 0, AUX_SC1); }
DI void drain() { asm volatile("s_waitcnt vmcnt(0)" ::: "memory"); }
DI void dma4_sc1(rsrc_t r, unsigned v0, unsigned v1, unsigned soff, unsigned lds) {
  asm volatile("s_mov_b32 m0, %4\n\ts_nop 0\n\t"
               "buffer_load_dwordx4 %0, %2, %3 offen sc1 lds\n\tbuffer_load_dwordx4 %1, %2, %3 offen offset:1024 sc1 lds\n\t"
               "buffer_load_dwordx4 %0, %2, %3 offen offset:2048 sc1 lds\n\tbuffer_load_dwordx4 %1, %2, %3 offen offset:3072 sc1 lds"
               :: "v"(v0), "v"(v1), "s"(r), "s"(soff), "s"(lds) : "memory");
}
DI void dma4(rsrc_t r, unsigned v0, unsigned v1, unsigned v2, unsigned v3, unsigned soff, unsigned lds) {
  asm volatile("s_mov_b32 m0, %6\n\ts_nop 0\n\t"
               "buffer_load_dwordx4 %0, %4, %5 offen lds\n\tbuffer_load_dwordx4 %1, %4, %5 offen offset:1024 lds\n\t"
               "buffer_load_dwordx4 %2, %4, %5 offen offset:2048 lds\n\tbuffer_load_dwordx4 %3, %4, %5 offen offset:3072 lds"
               :: "v"(v0), "v"(v1), "v"(v2), "v"(v3), "s"(r), "s"(soff), "s"(lds) : "memory");
}
#define SB() __builtin_amdgcn_sched_barrier(0)
#define SGB(mask, n) __builtin_amdgcn_sched_group_barrier(mask, n, 0)
constexpr int SG_MFMA = 0x8, SG_DSR = 0x100;
template <int CTRL> DI float dpp_f(float v) { return __builtin_bit_cast(float, __builtin_amdgcn_mov_dpp(__builtin_bit_cast(int, v), CTRL, 0xf, 0xf, true)); }
DI float row_sum(float v) { v += dpp_f<0x128>(v); v += dpp_f<0x124>(v); v += dpp_f<0x122>(v); v += dpp_f<0x121>(v); return v; }
DI f2 swap32(float a, float b) { auto r = __builtin_amdgcn_permlane32_swap(__builtin_bit_cast(unsigned, a), __builtin_bit_cast(unsigned, b), false, false);
  const unsigned r0 = r[0], r1 = r[1];
  return f2{__builtin_bit_cast(float, r0), __builtin_bit_cast(float, r1)}; }
DI f2 swap16(float a, float b) { auto r = __builtin_amdgcn_permlane16_swap(__builtin_bit_cast(unsigned, a), __builtin_bit_cast(unsigned, b), false, false);
  const unsigned r0 = r[0], r1 = r[1];
  return f2{__builtin_bit_cast(float, r0), __builtin_bit_cast(float, r1)}; }
DI float fmax2(float a, float b) { return __builtin_fmaxf(a, b); }
DI float fmin2(float a, float b) { return __builtin_fminf(a, b); }
template <int OP> DI float op2(float a, float b) { return OP == 0 ? fmax2(a, b) : OP == 1 ? fmin2(a, b) : a + b; }
template <int OP> DI float xrow4(float a, float b, float c, float d) {
  f2 s = swap32(a, b); const float ab = op2<OP>(s.x, s.y);
  s = swap32(c, d); const float cd = op2<OP>(s.x, s.y);
  s = swap16(ab, cd); return op2<OP>(s.x, s.y);
}
DI f2 xrow_max2(float a, float b) { f2 s = swap32(a, b); float c = fmaxf(s.x, s.y); s = swap16(c, c); c = fmaxf(s.x, s.y); return swap32(c, c); }
DI f2 xrow_sum2(float a, float b) { f2 s = swap32(a, b); float c = s.x + s.y; s = swap16(c, c); c = s.x + s.y; return swap32(c, c); }
DI float wave_sum(float v) { v = row_sum(v); f2 s = swap16(v, v); v = s.x + s.y; s = swap32(v, v); return s.x + s.y; }

constexpr int LD_XT = 0;
constexpr int LD_KC = 65536;
constexpr int KC_COPY = 4160, KC_CH = 4 * KC_COPY;
constexpr int LD_U = LD_KC + 2 * KC_CH;
constexpr int U_CH = 4608;
constexpr int LD_X = LD_U + 2 * U_CH;
constexpr int LD_EX = 0;
constexpr int LD_CONV_END = LD_X + 8192;
constexpr int LD_MISC = 131072;
constexpr int LD_PV = LD_MISC;
constexpr int PV_ROW = 104, PV_WAVE = 64 * PV_ROW;
constexpr int LD_STAT = LD_PV + 4 * PV_WAVE;
constexpr int LD_SCR = LD_STAT + 1024 + 256;
constexpr int LD_FLAG = LD_SCR + 2048;
constexpr int LD_OST = LD_PV;
constexpr int LDS_BYTES = 163840;
static_assert(LD_CONV_END <= LD_MISC && LD_FLAG + 64 <= LDS_BYTES, "LDS map");

struct Ctx {
  lds_t* L; rsrc_t ws; GAS unsigned* cw; unsigned char* wsp;
  int tid, lane, w, c, cp, rb, hp, layer;
};

DI void spin_fail(const Ctx& x) { __hip_atomic_store(x.cw + CW_TMO, 1u, RLX_AGENT); }
constexpr unsigned SPIN_CAP = 1u << 18;
DI void a2a_post(const Ctx& x, int e) {
  __syncthreads();
  if (x.tid < 8) __hip_atomic_fetch_add(x.cw + (CWI_A2A + (e * 8 + x.tid) * 16 + (x.c >> 4)) * CW_STRIDE, 1u, RLX_AGENT);
}
DI void a2a_signal(const Ctx& x, int e) { drain(); a2a_post(x, e); }
struct Poll { unsigned r0, r1, r2, r3; };
DI void poll16(const Ctx& x, unsigned word, Poll& q) {
  if (x.w == 0) {
    const rsrc_t rc = make_rsrc(x.wsp + WS_CTRL, CWI_END * CW_STRIDE * 4);
    const unsigned off = word * (CW_STRIDE * 4);
    unsigned n;
    asm volatile(
        "s_mov_b32 %4, 0\n\t"
        "buffer_load_dword %0, %5, %6, 0 offen sc1\n\ts_sleep 2\n\t"
        "buffer_load_dword %1, %5, %6, 0 offen sc1\n\ts_sleep 2\n\t"
        "buffer_load_dword %2, %5, %6, 0 offen sc1\n\ts_sleep 2\n\t"
        "buffer_load_dword %3, %5, %6, 0 offen sc1\n"
        "1:\n\t"
        "s_waitcnt vmcnt(3)\n\tv_cmp_gt_u32 vcc, 16, %0\n\ts_cbranch_vccz 2f\n\tbuffer_load_dword %0, %5, %6, 0 offen sc1\n\t"
        "s_waitcnt vmcnt(3)\n\tv_cmp_gt_u32 vcc, 16, %1\n\ts_cbranch_vccz 2f\n\tbuffer_load_dword %1, %5, %6, 0 offen sc1\n\t"
        "s_waitcnt vmcnt(3)\n\tv_cmp_gt_u32 vcc, 16, %2\n\ts_cbranch_vccz 2f\n\tbuffer_load_dword %2, %5, %6, 0 offen sc1\n\t"
        "s_waitcnt vmcnt(3)\n\tv_cmp_gt_u32 vcc, 16, %3\n\ts_cbranch_vccz 2f\n\tbuffer_load_dword %3, %5, %6, 0 offen sc1\n\t"
        "s_add_u32 %4, %4, 1\n\ts_cmp_lt_u32 %4, 0x20000\n\ts_cbranch_scc1 1b\n"
        "2:"
        : "=&v"(q.r0), "=&v"(q.r1), "=&v"(q.r2), "=&v"(q.r3), "=&s"(n) : "v"(off), "s"(rc) : "vcc", "scc", "memory");
    if (n >= 0x20000u) spin_fail(x);
  }
  __syncthreads();
}
DI void poll_retire(const Poll& q) { asm volatile("" :: "v"(q.r0), "v"(q.r1), "v"(q.r2), "v"(q.r3)); }
DI void a2a_wait(const Ctx& x, int e, Poll& q) { poll16(x, CWI_A2A + (e * 8 + (x.c & 7)) * 16 + (x.lane & 15), q); }
DI void grp_signal(const Ctx& x, int e) {
  drain(); __syncthreads();
  if (x.tid == 0) __hip_atomic_fetch_add(x.cw + (CWI_GRP + e) * CW_STRIDE, 1u, RLX_AGENT);
}
DI void grp_wait(const Ctx& x, int e, Poll& q) { poll16(x, CWI_GRP + e, q); }

DI void conv_load_k(const Ctx& x, u4 (&kd)[3]) {
  const int ch = x.tid >> 7, q = x.tid & 127;
  const unsigned off = WS_KREV + ((2 * x.cp + ch) * 2048 + 16 * q) * 2;
  kd[0] = __builtin_amdgcn_raw_buffer_load_b128(x.ws, off, 0, 0);
  kd[1] = __builtin_amdgcn_raw_buffer_load_b128(x.ws, off + 16, 0, 0);
  kd[2] = __builtin_amdgcn_raw_buffer_load_b128(x.ws, off + 32, 0, 0);
}
DI void conv_build_k(const Ctx& x, const u4 (&kd)[3]) {
  const int ch = x.tid >> 7, q = x.tid & 127;
  const unsigned d8 = q == 127 ? 0u : kd[2].x, d9 = q == 127 ? 0u : kd[2].y;
  const unsigned d[10] = {kd[0].x, kd[0].y, kd[0].z, kd[0].w, kd[1].x, kd[1].y, kd[1].z, kd[1].w, d8, d9};
  lds_t* b = x.L + LD_KC + ch * KC_CH + 32 * q;
  *(LAS u4*)(b) = u4{d[0], d[1], d[2], d[3]}; *(LAS u4*)(b + 16) = u4{d[4], d[5], d[6], d[7]};
  *(LAS u4*)(b + 2 * KC_COPY) = u4{d[1], d[2], d[3], d[4]}; *(LAS u4*)(b + 2 * KC_COPY + 16) = u4{d[5], d[6], d[7], d[8]};
  unsigned e[9];
#pragma unroll
  for (int i = 0; i < 9; ++i) e[i] = __builtin_amdgcn_alignbit(d[i + 1], d[i], 16);
  *(LAS u4*)(b + KC_COPY) = u4{e[0], e[1], e[2], e[3]}; *(LAS u4*)(b + KC_COPY + 16) = u4{e[4], e[5], e[6], e[7]};
  *(LAS u4*)(b + 3 * KC_COPY) = u4{e[1], e[2], e[3], e[4]}; *(LAS u4*)(b + 3 * KC_COPY + 16) = u4{e[5], e[6], e[7], e[8]};
  if (x.tid < 16) {
    *(LAS u4*)(x.L + LD_KC + (x.tid >> 1) * KC_COPY + 4096 + (x.tid & 1) * 16) = u4{0, 0, 0, 0};
  }
  if (x.tid >= 64 && x.tid < 128) {
    const int i = x.tid - 64;
    *(LAS u4*)(x.L + LD_U + (i >> 5) * U_CH + (i & 31) * 16) = u4{0, 0, 0, 0};
  }
}
template <int PAR> struct Mu { static constexpr int v(int k) { return PAR ? (k == 0 ? 1 : k == 1 ? 2 : k == 2 ? 5 : 6) : (k == 0 ? 0 : k == 1 ? 3 : k == 2 ? 4 : 7); } };
template <int PAR> DI void conv_load_a(const Ctx& x, u4 (&a)[32]) {
  const int b = x.lane & 15, kq = x.lane >> 4, ch = x.w >> 1;
  const int s0 = 2047 - 16 * (kq >> 1) - b + 8 * (kq & 1), cc = s0 & 3;
  unsigned bo = LD_KC + ch * KC_CH + cc * KC_COPY + 2 * (s0 - cc) - 64 * 63;
  asm volatile("" : "+v"(bo));
  lds_t* base = x.L + bo;
#pragma unroll
  for (int k = 0; k < 4; ++k)
#pragma unroll
    for (int i = 0; i < 8; ++i) {
      const int m = 8 * Mu<PAR>::v(k) + i;
      a[8 * k + i] = cat(*(LAS u2*)(base + 64 * (63 - m)), *(LAS u2*)(base + 64 * (63 - m) + 8));
    }
}
DI void q_load_w1(const Ctx& x, u4& wf, unsigned voff, unsigned soff) {
  asm volatile("buffer_load_dwordx4 %0, %1, %2, %3 offen" : "=a"(wf) : "v"(voff), "s"(x.ws), "s"(soff));
}
DI unsigned q_w_voff(const Ctx& x) { return WS_WFRAG + (2 * x.hp + (x.w & 1)) * 48 * 1024 + x.lane * 16; }
template <int PAR, bool LOADW> DI void conv_mfma(const Ctx& x, const u4 (&a)[32], f4 (&acc)[8], u4 (&wf)[48]) {
  const unsigned woff = q_w_voff(x);
  const int al = x.lane & 15, kq = x.lane >> 4, ch = x.w >> 1;
  unsigned bo = LD_U + ch * U_CH + 2 * (256 + 16 * al - 16 * (kq >> 1) + 8 * (kq & 1)) - 64 * 7;
  asm volatile("" : "+v"(bo));
  lds_t* base = x.L + bo;
  constexpr int NT = 8 - Mu<PAR>::v(0);
  u4 b[8];
#pragma unroll
  for (int i = 0; i < 8; ++i) b[i] = *(LAS u4*)(base + 64 * (7 - i));
  SB();
#pragma unroll
  for (int tau = 0; tau < 8; ++tau) {
    if (tau < NT) {
      int ng = 0;
#pragma unroll
      for (int k = 0; k < 4; ++k) ng += (Mu<PAR>::v(k) + tau <= 7);
#pragma unroll
      for (int i = 0; i < 8; ++i) {
#pragma unroll
        for (int k = 0; k < 4; ++k)
          if (Mu<PAR>::v(k) + tau <= 7) acc[Mu<PAR>::v(k) + tau] = mfma32(a[8 * k + i], b[i], acc[Mu<PAR>::v(k) + tau]);
        if (tau + 1 < NT) {
          b[i] = *(LAS u4*)(base + 64 * (8 * (tau + 1) - i + 7));
          if (ng == 4) SGB(SG_MFMA, 4); else if (ng == 3) SGB(SG_MFMA, 3); else if (ng == 2) SGB(SG_MFMA, 2); else SGB(SG_MFMA, 1);
          SGB(SG_DSR, 1);
        }
        SB();
        if (LOADW && tau < 4) { q_load_w1(x, wf[8 * tau + i], woff, (8 * tau + i) * 1024); SB(); }
      }
    }
  }
}

struct ConvIn { u4 x0, x1, s[4]; };
DI void conv_load_in(const Ctx& x, int layer, ConvIn& in) {
  const unsigned xo = WS_XT + layer * XT_BYTES + (2 * x.cp * XT_STRIDE + 8 * x.tid) * 2;
  const unsigned so = WS_ST + layer * 2048 * 8 + x.tid * 32;
  in.s[0] = ld16(x.ws, so); in.s[1] = ld16(x.ws, so + 16); in.s[2] = ld16(x.ws, so + 8192); in.s[3] = ld16(x.ws, so + 8192 + 16);
  in.x0 = ld16(x.ws, xo); in.x1 = ld16(x.ws, xo + XT_STRIDE * 2);
}
DI f2 unpk(unsigned v) { return f2{blo(v), bhi(v)}; }
DI unsigned pk(f2 v) { return pack2(v.x, v.y); }
DI void conv_build_u(const Ctx& x, const ConvIn& in) {
  const f4 sa = __builtin_bit_cast(f4, in.s[0]), sb = __builtin_bit_cast(f4, in.s[1]), qa = __builtin_bit_cast(f4, in.s[2]), qb = __builtin_bit_cast(f4, in.s[3]);
  const f2 sv[4] = {f2{sa[0], sa[1]}, f2{sa[2], sa[3]}, f2{sb[0], sb[1]}, f2{sb[2], sb[3]}};
  const f2 qv[4] = {f2{qa[0], qa[1]}, f2{qa[2], qa[3]}, f2{qb[0], qb[1]}, f2{qb[2], qb[3]}};
  u4 u0, u1;
#pragma unroll
  for (int i = 0; i < 4; ++i) {
    const f2 m = sv[i] * (1.f / DM);
    const f2 ve = (qv[i] * (1.f / DM) + LN_EPS) - m * m;
    const f2 r = {__builtin_amdgcn_rsqf(__builtin_fabsf(ve.x)), __builtin_amdgcn_rsqf(__builtin_fabsf(ve.y))};
    const f2 nmr = -m * r;
    u0[i] = pk(unpk(in.x0[i]) * r + nmr);
    u1[i] = pk(unpk(in.x1[i]) * r + nmr);
  }
  *(LAS u4*)(x.L + LD_U + 512 + 16 * x.tid) = u0; *(LAS u4*)(x.L + LD_U + U_CH + 512 + 16 * x.tid) = u1;
  *(LAS u4*)(x.L + LD_X + 16 * x.tid) = in.x0; *(LAS u4*)(x.L + LD_X + 4096 + 16 * x.tid) = in.x1;
}
template <int PAR, bool LOADW> DI void conv_wave(const Ctx& x, float dsk, const u4 (&a)[32], u4 (&wf)[48]) {
  const int ch = x.w >> 1;
  f4 acc[8];
#pragma unroll
  for (int t = 0; t < 8; ++t) acc[t] = f4{0, 0, 0, 0};
  conv_mfma<PAR, LOADW>(x, a, acc, wf);
  STAMP(x.wsp, 6 + 16 * x.layer);
  const int al = x.lane & 15, g = x.lane >> 4;
  unsigned eo = LD_EX + x.lane * 16, uo = LD_U + ch * U_CH + 512 + 2 * (1024 * PAR + 16 * al + 4 * g);
  asm volatile("" : "+v"(eo), "+v"(uo));
  lds_t* mine = x.L + eo + x.w * 4096;
  lds_t* other = x.L + eo + (x.w ^ 1) * 4096;
#pragma unroll
  for (int t = 0; t < 4; ++t) *(LAS f4*)(mine + t * 1024) = acc[PAR ? t : 4 + t];
  f2 v[4][2];
#pragma unroll
  for (int t = 0; t < 4; ++t) {
    const u2 uu = *(LAS u2*)(x.L + uo + 512 * t), xx = *(LAS u2*)(x.L + uo + (LD_X - LD_U - 512) + ch * (4096 - U_CH) + 512 * t);
    const f4 own = acc[PAR ? 4 + t : t];
    v[t][0] = unpk(uu.x) * dsk + (unpk(xx.x) + f2{own[0], own[1]});
    v[t][1] = unpk(uu.y) * dsk + (unpk(xx.y) + f2{own[2], own[3]});
  }
  __syncthreads();
  STAMP(x.wsp, 7 + 16 * x.layer);
  const unsigned so = WS_X1 + x.layer * X1_BYTES + (((8 * PAR + 2 * (g & 1) + (al >> 3)) * 512 + 2 * x.cp + ch) * 128 + 16 * (al & 7) + 8 * (g >> 1)) * 2;
#pragma unroll
  for (int t = 0; t < 4; t += 2) {
    const f4 o0 = *(LAS f4*)(other + t * 1024), o1 = *(LAS f4*)(other + (t + 1) * 1024);
    const unsigned a0 = pk(v[t][0] + f2{o0[0], o0[1]}), a1 = pk(v[t][1] + f2{o0[2], o0[3]});
    const unsigned b0 = pk(v[t + 1][0] + f2{o1[0], o1[1]}), b1 = pk(v[t + 1][1] + f2{o1[2], o1[3]});
    const auto s0 = __builtin_amdgcn_permlane16_swap(a0, b0, false, false), s1 = __builtin_amdgcn_permlane16_swap(a1, b1, false, false);
    st16(x.ws, so + t * (2 * 512 * 256), u4{s0[0], s1[0], s0[1], s1[1]});
  }
  if (LOADW) {
    SB();
    const unsigned woff = q_w_voff(x);
#pragma unroll
    for (int i = 32; i < 48; ++i) q_load_w1(x, wf[i], woff, i * 1024);
    asm volatile("s_waitcnt vmcnt(16)" ::: "memory");
  } else {
    drain();
  }
}

DI f2 cmul(f2 a, f2 b) { return f2{a.x, a.x} * b + f2{a.y, a.y} * f2{-b.y, b.x}; }
DI f2 cexp_p(float wr2, float wrev, float p) {
  const float e = __builtin_amdgcn_exp2f(wr2 * p);
  const float a = __builtin_amdgcn_fractf(wrev * p);
  return f2{e * __builtin_amdgcn_cosf(a), e * __builtin_amdgcn_sinf(a)};
}
constexpr int KG_ROW = 528;
constexpr int KG_A = 0;
constexpr int KG_X = 2 * 48 * KG_ROW;
constexpr int KG_T = LD_CONV_END;
constexpr int KG_B = KG_T + 2 * 2056 * 4;
static_assert(KG_X + 64 * 136 <= LD_KC && KG_B + 2 * 16 * KG_ROW <= LDS_BYTES, "prologue LDS map");

struct ProW { u2 a0, a1; unsigned short b[4]; };
struct ProIn { unsigned short lar, aim, cre, cim, ldt; u4 xt[2], xr[2]; ProW w[2]; unsigned epoch; };
DI void pro_load_w(const Params& p, int F, int lane, ProW& w) {
  const int fh = F / 48, fm = (F % 48) >> 4, fks = F & 15, row = lane & 15, kq = lane >> 4;
  w.a0 = *(const u2*)(p.wqkv + (32 * fks + row) * 1536 + fm * 512 + 16 * fh + 4 * kq);
  w.a1 = *(const u2*)(p.wqkv + (32 * fks + 16 + row) * 1536 + fm * 512 + 16 * fh + 4 * kq);
  const unsigned short* q = p.wphi + fh * 256 + (4 * kq) * 16 + row;
#pragma unroll
  for (int j = 0; j < 4; ++j) w.b[j] = q[16 * j];
}
DI void pro_fold_w(const Ctx& x, int F, const ProW& w) {
  const bool fold = ((F % 48) >> 4) < 2;
  const int dd = (x.lane & 15) - 4 * (x.lane >> 4);
  const u2 wb = fold ? u2{w.b[0] | ((unsigned)w.b[1] << 16), w.b[2] | ((unsigned)w.b[3] << 16)}
                     : u2{(dd == 0 ? 0x3f80u : 0u) | (dd == 1 ? 0x3f800000u : 0u), (dd == 2 ? 0x3f80u : 0u) | (dd == 3 ? 0x3f800000u : 0u)};
  const f4 d0 = mfma16(w.a0, wb, f4{0, 0, 0, 0}), d1 = mfma16(w.a1, wb, f4{0, 0, 0, 0});
  float e[8];
#pragma unroll
  for (int r = 0; r < 4; ++r) { e[r] = d0[r] - (fold ? dpp_f<0x150>(d0[r]) : 0.f); e[4 + r] = d1[r] - (fold ? dpp_f<0x150>(d1[r]) : 0.f); }
  u4 o = {pack2(e[0], e[1]), pack2(e[2], e[3]), pack2(e[4], e[5]), pack2(e[6], e[7])};
  if (fold && (x.lane & 15) == 0) o = u4{0x3f803f80u, 0x3f803f80u, 0x3f803f80u, 0x3f803f80u};
  st16(x.ws, WS_WFRAG + F * 1024 + x.lane * 16, o);
}
DI void pro_load(const Ctx& x, const Params& p, ProIn& in) {
#pragma unroll
  for (int i = 0; i < 2; ++i) {
    in.xt[i] = *(const u4*)(p.u + (64 * (x.c >> 3) + 32 * i + (x.tid >> 3)) * DM + 64 * (x.c & 7) + 8 * (x.tid & 7));
    in.xr[i] = *(const u4*)(p.u + (8 * x.c + 2 * x.w + i) * DM + 8 * x.lane);
  }
  in.epoch = __builtin_amdgcn_raw_buffer_load_b32(x.ws, WS_EPOCH, 0, AUX_SC1);
  const int d = 2 * x.cp + (x.tid >> 7), n = x.tid & 127;
  in.lar = p.lar[d * NST + n]; in.aim = p.aim[d * NST + n]; in.cre = p.cre[d * NST + n]; in.cim = p.cim[d * NST + n]; in.ldt = p.ldt[d];
  pro_load_w(p, 6 * x.c + x.w, x.lane, in.w[0]);
  pro_load_w(p, 6 * x.c + 4 + (x.w & 1), x.lane, in.w[1]);
  SB();
}
DI void pro_init(const Ctx& x) {
  const rsrc_t ws = x.ws;
  if (x.c < 2) { const int i = x.c * 256 + x.tid; if (i < CWI_END) __builtin_amdgcn_raw_buffer_store_b32(0u, ws, i * (CW_STRIDE * 4), 0, AUX_SC1); }
  if (x.c >= 8 && x.c < 12) st16(ws, WS_ST + 2048 * 8 + ((x.c - 8) * 256 + x.tid) * 16, u4{0, 0, 0, 0});
  static_assert(WS_ZP == WS_KVP + 2 * KVP_BYTES && (2 * KVP_BYTES + 2 * ZP_BYTES) == 256 * 272 * 16, "poisoned block");
  st16(ws, WS_KVP + (x.c * 272 + x.tid) * 16, u4{~0u, ~0u, ~0u, ~0u});
  if (x.tid < 16) st16(ws, WS_KVP + (x.c * 272 + 256 + x.tid) * 16, u4{~0u, ~0u, ~0u, ~0u});
}
DI void pro_publish_x(const Ctx& x, const ProIn& in) {
#pragma unroll
  for (int i = 0; i < 2; ++i) {
    lds_t* a = x.L + KG_X + (32 * i + (x.tid >> 3)) * 136 + (x.tid & 7) * 16;
    *(LAS u2*)a = u2{in.xt[i].x, in.xt[i].y}; *(LAS u2*)(a + 8) = u2{in.xt[i].z, in.xt[i].w};
  }
  {
    f2 a[2], q[2];
#pragma unroll
    for (int i = 0; i < 2; ++i) {
      a[i] = unpk(in.xr[i][0]); q[i] = a[i] * a[i];
#pragma unroll
      for (int k = 1; k < 4; ++k) { const f2 v = unpk(in.xr[i][k]); a[i] += v; q[i] += v * v; }
    }
    const float t = xrow4<2>(row_sum(a[0].x + a[0].y), row_sum(q[0].x + q[0].y), row_sum(a[1].x + a[1].y), row_sum(q[1].x + q[1].y));
    const int g = x.lane >> 4;
    if ((x.lane & 15) == 0)
      __builtin_amdgcn_raw_buffer_store_b32(__builtin_bit_cast(unsigned, t), x.ws, WS_ST + (g >> 1) * 8192 + (8 * x.c + 2 * x.w + (g & 1)) * 4, 0, AUX_SC1);
  }
  __syncthreads();
  const int g = x.lane >> 4, cl = x.lane & 15;
#pragma unroll
  for (int j = 0; j < 2; ++j) {
    lds_t* a = x.L + KG_X + (8 * (g + 4 * j) + (cl >> 2)) * 136 + (16 * x.w + 4 * (cl & 3)) * 2;
    st16(x.ws, WS_XT + ((64 * (x.c & 7) + 16 * x.w + cl) * XT_STRIDE + 64 * (x.c >> 3) + 8 * (g + 4 * j)) * 2, cat(lds_tr(a), lds_tr(a + 4 * 136)));
  }
}
DI void pro_post(const Ctx& x, unsigned flags, unsigned tag) {
  drain(); __syncthreads();
  if (x.tid == 0) __builtin_amdgcn_raw_buffer_store_b32(tag, x.ws, flags + x.c * 64, 0, AUX_SC1);
}
DI void pro_tables(const Ctx& x, const ProIn& in) {
  const float dt = rbf(__expf(bf2f(in.ldt)));
  const float ar = -rbf(__expf(bf2f(in.lar)));
  const float wr2 = (ar * dt) * LOG2E, wrev = (bf2f(in.aim) * dt) * 0.15915494309189535f;
  const f2 c2 = {2.0f * (bf2f(in.cre) * dt), -2.0f * (bf2f(in.cim) * dt)};
  const int chl = x.tid >> 7, n = x.tid & 127;
  LAS unsigned* TA = (LAS unsigned*)(x.L + KG_A + chl * 48 * KG_ROW) + n;
  LAS unsigned* TB = (LAS unsigned*)(x.L + KG_B + chl * 16 * KG_ROW) + n;
  constexpr int RS = KG_ROW / 4;
  const f2 e8 = cexp_p(wr2, wrev, 8.f), e128 = cexp_p(wr2, -wrev, 128.f);
  f2 e[8]; e[1] = cexp_p(wr2, -wrev, 1.f);
  f2 y = e8, h = cmul(c2, cexp_p(wr2, -wrev, 1024.f)), g = c2;
  e[2] = cmul(e[1], e[1]); e[3] = cmul(e[2], e[1]); e[4] = cmul(e[2], e[2]); e[5] = cmul(e[4], e[1]); e[6] = cmul(e[3], e[3]); e[7] = cmul(e[4], e[3]);
  TB[0] = 0x00003f80u;
#pragma unroll
  for (int j = 1; j < 16; ++j) { TB[j * RS] = pk(y); if (j < 15) y = cmul(y, e8); }
#pragma unroll
  for (int a = 0; a < 9; ++a) { TA[(32 + a) * RS] = pk(h); if (a < 8) h = cmul(h, e128); }
#pragma unroll
  for (int a = 0; a < 2; ++a) {
#pragma unroll
    for (int k = 0; k < 8; ++k) TA[(8 * a + k) * RS] = pk(k ? cmul(g, e[k]) : g);
    g = cmul(g, e128);
  }
#pragma unroll
  for (int a = 0; a < 2; ++a) {
#pragma unroll
    for (int k = 0; k < 4; ++k) TA[(16 + 4 * a + k) * RS] = pk(k ? cmul(g, e[2 * k]) : g);
    g = cmul(g, e128);
  }
#pragma unroll
  for (int a = 0; a < 4; ++a) {
#pragma unroll
    for (int k = 0; k < 2; ++k) TA[(24 + 2 * a + k) * RS] = pk(k ? cmul(g, e[4]) : g);
    if (a < 3) g = cmul(g, e128);
  }
}
DI void pro_kgemm(const Ctx& x) {
  const int ch = x.w >> 1, j = x.lane & 15, g = x.lane >> 4, odd = x.w & 1;
  unsigned ao = KG_A + (ch * 48 + 16 * odd + j) * KG_ROW + g * 16, a2o = KG_A + (ch * 48 + 32 + j) * KG_ROW + g * 16;
  unsigned bo = KG_B + (ch * 16 + j) * KG_ROW + g * 16;
  asm volatile("" : "+v"(ao), "+v"(a2o), "+v"(bo));
  u4 a0[8], a2[8], b[8];
#pragma unroll
  for (int ks = 0; ks < 8; ++ks) { b[ks] = *(LAS u4*)(x.L + bo + ks * 64); a0[ks] = *(LAS u4*)(x.L + ao + ks * 64); a2[ks] = *(LAS u4*)(x.L + a2o + ks * 64); }
  SB();
  f4 acc0 = {0, 0, 0, 0}, acc2 = acc0;
#pragma unroll
  for (int ks = 0; ks < 8; ++ks) { acc0 = mfma32(a0[ks], b[ks], acc0); acc2 = mfma32(a2[ks], b[ks], acc2); }
  const int pe = 128 * (g >> 1) + 4 * (g & 1), po = g < 2 ? 256 + 128 * g : 512 + 256 * (g - 2);
  const unsigned t0 = KG_T + (ch * 2056 + 8 * j + (odd ? po : pe)) * 4;
  const int s1 = odd ? (g < 2 ? 8 : 16) : 4, s2 = odd ? (g < 2 ? 16 : 512) : 8;
  *(LAS float*)(x.L + t0) = acc0[0]; *(LAS float*)(x.L + t0 + s1) = acc0[1];
  *(LAS float*)(x.L + t0 + s2) = acc0[2]; *(LAS float*)(x.L + t0 + s1 + s2) = acc0[3];
  const unsigned t2 = KG_T + (ch * 2056 + 1024 + 512 * g + 8 * j) * 4, dummy = KG_T + 2052 * 4;
#pragma unroll
  for (int r = 0; r < 4; ++r) *(LAS float*)(x.L + ((4 * g + r < 8 || (4 * g + r == 8 && j == 0)) ? t2 + 512 * r : dummy)) = acc2[r];
}
constexpr int KG_D = 0, KG_DCH = 4096 + 64;
DI void pro_expand(const Ctx& x) {
  unsigned tb = KG_T, db = KG_D + x.tid * 4;
  f2 l0 = {(float)(2047 - 2 * x.tid), (float)(2046 - 2 * x.tid)};
  asm volatile("" : "+v"(tb), "+v"(db), "+v"(l0));
#pragma unroll
  for (int i = 0; i < 4; ++i) {
    unsigned r = pk(l0 - (float)(512 * i));
    asm("" : "+v"(r));
    const unsigned a0 = tb + 4 * (int)blo(r), a1 = tb + 4 * (int)bhi(r);
#pragma unroll
    for (int ch = 0; ch < 2; ++ch)
      *(LAS unsigned*)(x.L + db + ch * KG_DCH + i * 1024) = pack2(*(LAS float*)(x.L + a0 + ch * (2056 * 4)), *(LAS float*)(x.L + a1 + ch * (2056 * 4)));
  }
}
DI void pro_take_k(const Ctx& x, u4 (&kd)[3]) {
  const int ch = x.tid >> 7, q = x.tid & 127;
  lds_t* a = x.L + KG_D + ch * KG_DCH + 32 * q;
  kd[0] = *(LAS u4*)a; kd[1] = *(LAS u4*)(a + 16); kd[2] = *(LAS u4*)(a + 32);
  const unsigned off = WS_KREV + ((2 * x.cp + ch) * 2048 + 16 * q) * 2;
  __builtin_amdgcn_raw_buffer_store_b128(kd[0], x.ws, off, 0, 0);
  __builtin_amdgcn_raw_buffer_store_b128(kd[1], x.ws, off + 16, 0, 0);
}
DI void pro_wait(const Ctx& x, unsigned flags, unsigned tag, unsigned first) {
  unsigned v = first;
  for (unsigned s = 0;; ++s) {
    const unsigned ok = __all(v == tag);
    if (x.lane == 0) ((LAS unsigned*)(x.L + LD_FLAG))[x.w] = ok;
    __syncthreads();
    const u4 r = *(LAS u4*)(x.L + LD_FLAG);
    if (r.x & r.y & r.z & r.w) break;
    if (s > SPIN_CAP) { if (x.tid == 0) spin_fail(x); break; }
    v = __builtin_amdgcn_raw_buffer_load_b32(x.ws, flags + x.tid * 64, 0, AUX_SC1);
    __syncthreads();
  }
}

DI void q_wsum(const u4 (&wf)[48], f4 (&ws)[3]) {
  const u4 ones = {0x3f803f80u, 0x3f803f80u, 0x3f803f80u, 0x3f803f80u};
  ws[0] = f4{0, 0, 0, 0}; ws[1] = ws[0]; ws[2] = ws[0];
#pragma unroll
  for (int ks = 0; ks < 16; ++ks)
#pragma unroll
    for (int m = 0; m < 3; ++m) ws[m] = mfma32(wf[16 * m + ks], ones, ws[m]);
}
struct QDma { unsigned v0, v1, base, lds; int layer; unsigned char* wsp; };
DI QDma q_dma_setup(const Ctx& x) {
  const int row = x.lane >> 4, pc = x.lane & 15;
  return QDma{(unsigned)(row * 256 + ((pc ^ (row << 1)) * 16)), (unsigned)(row * 256 + ((pc ^ ((4 + row) << 1)) * 16)),
              WS_X1 + x.layer * X1_BYTES + x.rb * (512 * 256) + x.w * 4096, (unsigned)(LD_XT + x.w * 4096), x.layer, x.wsp};
}
template <int OFF> DI void dma2_sc1(rsrc_t r, const QDma& d) {
  unsigned t;
  asm volatile("s_add_u32 m0, %5, %6\n\ts_add_u32 %0, %4, %6\n\t"
               "buffer_load_dwordx4 %1, %3, %0 offen sc1 lds\n\tbuffer_load_dwordx4 %2, %3, %0 offen offset:1024 sc1 lds"
               : "=&s"(t) : "v"(d.v0), "v"(d.v1), "s"(r), "s"(d.base), "s"(d.lds), "n"(OFF) : "memory", "scc");
}
template <int OFF> DI void dma1_sc1(rsrc_t r, const QDma& d) {
  unsigned t;
  asm volatile("s_add_u32 m0, %4, %5\n\ts_add_u32 %0, %3, %5\n\tbuffer_load_dwordx4 %1, %2, %0 offen sc1 lds"
               : "=&s"(t) : "v"((OFF & 1024) ? d.v1 : d.v0), "s"(r), "s"(d.base), "s"(d.lds), "n"(OFF) : "memory", "scc");
}
struct QAddr { lds_t* lo[4]; lds_t* hi[4]; };
DI void q_frag_addr(const Ctx& x, QAddr& ba) {
  const int g = x.lane >> 4, q = (x.lane & 15) >> 2, pp = x.lane & 3, ph = x.w >> 1;
  const int r7 = 4 * (g & 1) + q;
#pragma unroll
  for (int pt = 0; pt < 4; ++pt) {
    const int lc = 8 * ph + 2 * (pt ^ (2 * (x.w & 1))) + (pp >> 1);
    unsigned o = LD_XT + (4 * g + q) * 256 + ((lc ^ (r7 << 1)) * 16) + 8 * (pp & 1), o2 = o + 65536;
    asm volatile("" : "+v"(o), "+v"(o2));
    ba.lo[pt] = x.L + o; ba.hi[pt] = x.L + o2;
  }
}
template <int KS> DI u4 q_ldb1(const QAddr& ba, int pt) {
  lds_t* p = (KS < 8 ? ba.lo[pt] : ba.hi[pt]) + (KS & 7) * 8192;
  return cat(lds_tr(p), lds_tr(p + 4096));
}
template <int KS> DI void q_ldb(const QAddr& ba, u4 (&b)[4]) {
#pragma unroll
  for (int pt = 0; pt < 4; ++pt) b[pt] = q_ldb1<KS>(ba, pt);
}
template <int N> DI void wait_vm_barrier() { asm volatile("s_waitcnt vmcnt(%0)\n\ts_barrier" :: "n"(N) : "memory"); }

DI void softmax4(const f4 (&t)[4], const float (&cs)[4], float csr, lds_t* scr_w, lds_t* scr_r, u4 (&out)[4]) {
  float mx[4], mn[4];
#pragma unroll
  for (int pt = 0; pt < 4; ++pt) {
    mx[pt] = fmax2(fmax2(fmax2(t[pt][0], t[pt][1]), t[pt][2]), t[pt][3]);
    mn[pt] = fmin2(fmin2(fmin2(t[pt][0], t[pt][1]), t[pt][2]), t[pt][3]);
  }
  const float MX = xrow4<0>(mx[0], mx[1], mx[2], mx[3]), MN = xrow4<1>(mn[0], mn[1], mn[2], mn[3]);
  *(LAS f2*)scr_w = f2{-csr * MX, csr * MN};
  f2 ep[4][2], en[4][2];
  float sp[4], sn[4];
#pragma unroll
  for (int pt = 0; pt < 4; ++pt) {
    const f2 b = *(LAS f2*)(scr_r + 128 * pt);
    const f2 t0 = {t[pt][0], t[pt][1]}, t1 = {t[pt][2], t[pt][3]};
    const f2 p0 = t0 * cs[pt] + b.x, p1 = t1 * cs[pt] + b.x, n0 = b.y - t0 * cs[pt], n1 = b.y - t1 * cs[pt];
    ep[pt][0] = f2{__builtin_amdgcn_exp2f(p0.x), __builtin_amdgcn_exp2f(p0.y)}; ep[pt][1] = f2{__builtin_amdgcn_exp2f(p1.x), __builtin_amdgcn_exp2f(p1.y)};
    en[pt][0] = f2{__builtin_amdgcn_exp2f(n0.x), __builtin_amdgcn_exp2f(n0.y)}; en[pt][1] = f2{__builtin_amdgcn_exp2f(n1.x), __builtin_amdgcn_exp2f(n1.y)};
    const f2 a = ep[pt][0] + ep[pt][1], c = en[pt][0] + en[pt][1];
    sp[pt] = a.x + a.y; sn[pt] = c.x + c.y;
  }
  const float SP = xrow4<2>(sp[0], sp[1], sp[2], sp[3]), SN = xrow4<2>(sn[0], sn[1], sn[2], sn[3]);
  *(LAS f2*)scr_w = f2{__builtin_amdgcn_rcpf(SP), __builtin_amdgcn_rcpf(SN)};
#pragma unroll
  for (int pt = 0; pt < 4; ++pt) {
    const f2 iv = *(LAS f2*)(scr_r + 128 * pt);
    out[pt] = u4{pk(ep[pt][0] * iv.x), pk(ep[pt][1] * iv.x), pk(en[pt][0] * iv.y), pk(en[pt][1] * iv.y)};
  }
}

struct QState { u4 pq[4]; u2 res[4]; u4 pk8[8], az; };
DI void o_load_partials(const Ctx& x, QState& st) {
  const int h = x.w & 1, ph = x.w >> 1, hh = 2 * x.hp + h;
  const unsigned ko = WS_KVP + x.layer * KVP_BYTES + (hh * 16 + 8 * ph) * 1024 + x.lane * 16;
#pragma unroll
  for (int i = 0; i < 8; ++i) st.pk8[i] = ld16(x.ws, ko + i * 1024);
  st.az = ld16(x.ws, WS_ZP + x.layer * ZP_BYTES + (hh * 16 + (x.lane & 15)) * 64 + (x.lane >> 4) * 16);
}

template <int KS> struct QPass1 {
  static DI void run(rsrc_t ws, const QDma& d, const QAddr& ba, const u4 (&wf)[48], u4 (&b)[4], f4 (&ak)[4], f4 (&av)[4], f4 (&ag)[2]) {
    if (KS < 15 && (KS & 1)) wait_vm_barrier<(KS < 15 ? 8 + 2 * (KS < 10 ? KS : 10) - 4 * ((KS + 1) / 2) : 0)>();
    const u4 g0 = b[0], g1 = b[1];
#pragma unroll
    for (int pt = 0; pt < 4; ++pt) {
      ak[pt] = mfma32(wf[16 + KS], b[pt], ak[pt]); av[pt] = mfma32(wf[32 + KS], b[pt], av[pt]);
      if (KS < 15) {
        b[pt] = q_ldb1<(KS < 15 ? KS + 1 : 15)>(ba, pt);
        SGB(SG_MFMA, 1); SGB(SG_DSR, 1); SGB(SG_MFMA, 1); SGB(SG_DSR, 1);
      }
      SB();
    }
    ag[0] = mfma32(g0, g0, ag[0]);
    SB();
    if (KS < 10) dma1_sc1<(3 + KS / 2) * 16384 + (KS & 1) * 2048>(ws, d);
    SB();
    ag[1] = mfma32(g1, g1, ag[1]);
    SB();
    if (KS < 10) dma1_sc1<(3 + KS / 2) * 16384 + (KS & 1) * 2048 + 1024>(ws, d);
    SB();
    QPass1<KS + 1>::run(ws, d, ba, wf, b, ak, av, ag);
  }
};
template <> struct QPass1<16> { static DI void run(rsrc_t, const QDma&, const QAddr&, const u4 (&)[48], u4 (&)[4], f4 (&)[4], f4 (&)[4], f4 (&)[2]) {} };
template <int KS> struct QPass2 {
  static DI void run(const QAddr& ba, const u4 (&wf)[48], u4 (&b)[3][4], f4 (&aq)[4]) {
    if (KS < 14) q_ldb<(KS < 14 ? KS + 2 : 15)>(ba, b[(KS + 2) % 3]);
#pragma unroll
    for (int pt = 0; pt < 4; ++pt) aq[pt] = mfma32(wf[KS], b[KS % 3][pt], aq[pt]);
    if (KS < 14) {
#pragma unroll
      for (int i = 0; i < 4; ++i) { SGB(SG_MFMA, 1); SGB(SG_DSR, 2); }
    }
    SB();
    QPass2<KS + 1>::run(ba, wf, b, aq);
  }
};
template <> struct QPass2<16> { static DI void run(const QAddr&, const u4 (&)[48], u4 (&)[3][4], f4 (&)[4]) {} };

DI void o_dma_wo(const Ctx& x, const Params& p) {
  if (x.w == 0) return;
  const rsrc_t r = make_rsrc(p.wo, 512 * 512 * 2);
  const int row = x.lane >> 3, pc = x.lane & 7, cb = x.hp >> 1;
  const int b1 = (row >> 1) & 1;
  const unsigned v0 = row * 1024 + ((pc ^ (b1 << 1)) * 16), v1 = row * 1024 + ((pc ^ ((b1 | 2) << 1)) * 16) + 7168;
  const int g0 = x.w == 1 ? 0 : x.w == 2 ? 6 : 11, g1 = x.w == 1 ? 6 : x.w == 2 ? 11 : 16;
#pragma unroll 1
  for (int i = g0; i < g1; ++i) dma4(r, v0, v1, v0 + 2 * 7168, v1 + 2 * 7168, i * 32768 + cb * 128, LD_XT + i * 4096);
}

DI void q_phase(const Ctx& x, const Params& p, const u4 (&wf)[48], const f4 (&wsum)[3], QState& st, const Poll& pl) {
  const QDma dm = q_dma_setup(x);
  dma2_sc1<0>(x.ws, dm); dma2_sc1<2048>(x.ws, dm); dma2_sc1<16384>(x.ws, dm); dma2_sc1<16384 + 2048>(x.ws, dm);
  dma2_sc1<32768>(x.ws, dm); dma2_sc1<32768 + 2048>(x.ws, dm);
  QAddr ba;
  q_frag_addr(x, ba);
  const int h = x.w & 1, ph = x.w >> 1, g = x.lane >> 4, cl = x.lane & 15;
  f4 ak[4], av[4], ag[2];
#pragma unroll
  for (int i = 0; i < 4; ++i) { ak[i] = f4{0, 0, 0, 0}; av[i] = f4{0, 0, 0, 0}; }
  ag[0] = f4{0, 0, 0, 0}; ag[1] = f4{0, 0, 0, 0};
  STAMP(x.wsp, 9 + 16 * x.layer);
  {
    u4 b[4];
    wait_vm_barrier<8>();
    poll_retire(pl);
    q_ldb<0>(ba, b);
    SB();
    QPass1<0>::run(x.ws, dm, ba, wf, b, ak, av, ag);
  }
  STAMP(x.wsp, 10 + 16 * x.layer);
  {
    const int r = cl & 3;
    const unsigned so = LD_STAT + 4 * (64 * ph + 32 * h + cl);
    const unsigned a0 = g == 0 ? so : LD_STAT + 1024, a1 = g == (cl >> 2) ? so + 512 : LD_STAT + 1024;
#pragma unroll
    for (int i = 0; i < 2; ++i) {
      const float dq = r == 0 ? ag[i][0] : r == 1 ? ag[i][1] : r == 2 ? ag[i][2] : ag[i][3];
      *(LAS float*)(x.L + a0 + 64 * i) = ak[i][0];
      *(LAS float*)(x.L + a1 + 64 * i) = dq;
    }
  }
  __syncthreads();
  float mean[4], rs[4], cs[4];
#pragma unroll
  for (int pt = 0; pt < 4; ++pt) {
    LAS float* S = (LAS float*)(x.L + LD_STAT) + 64 * ph + 16 * (pt ^ (2 * h)) + cl;
    mean[pt] = S[0] * (1.f / DM);
    rs[pt] = __builtin_amdgcn_rsqf(__builtin_fabsf((S[128] * (1.f / DM) + LN_EPS) - mean[pt] * mean[pt]));
    cs[pt] = rs[pt] * LOG2E;
  }
  const float csr = g == 0 ? cs[0] : g == 1 ? cs[2] : g == 2 ? cs[1] : cs[3];
  lds_t* scr_r = x.L + LD_SCR + x.w * 512 + cl * 8;
  lds_t* scr_w = scr_r + 128 * ((g >> 1) | ((g & 1) << 1));
  lds_t* pv = x.L + LD_PV + x.w * PV_WAVE;
  {
    f4 t[4];
#pragma unroll
    for (int pt = 0; pt < 4; ++pt) t[pt] = ak[pt] - mean[pt] * wsum[1];
    u4 pk4[4];
    softmax4(t, cs, csr, scr_w, scr_r, pk4);
#pragma unroll
    for (int pt = 0; pt < 4; ++pt) {
      const f4 v = (av[pt] - mean[pt] * wsum[2]) * rs[pt];
      lds_t* row = pv + (16 * pt + cl) * PV_ROW + 8 * g;
      *(LAS u2*)(row) = u2{pk4[pt].x, pk4[pt].y}; *(LAS u2*)(row + 32) = u2{pk4[pt].z, pk4[pt].w};
      *(LAS u2*)(row + 64) = u2{pack2(v[0], v[1]), pack2(v[2], v[3])};
    }
  }
  f4 kv0 = {0, 0, 0, 0}, kv1 = kv0, z0 = kv0, z1 = kv0;
  {
    const u4 ones = {0x3f803f80u, 0x3f803f80u, 0x3f803f80u, 0x3f803f80u};
    const int q = cl >> 2, pp = cl & 3;
#pragma unroll
    for (int k2 = 0; k2 < 2; ++k2) {
      lds_t* a = pv + (32 * k2 + 8 * g + q) * PV_ROW + 8 * pp;
      const u4 a0 = cat(lds_tr(a), lds_tr(a + 4 * PV_ROW));
      const u4 a1 = cat(lds_tr(a + 32), lds_tr(a + 4 * PV_ROW + 32));
      const u4 bv = cat(lds_tr(a + 64), lds_tr(a + 4 * PV_ROW + 64));
      kv0 = mfma32(a0, bv, kv0); kv1 = mfma32(a1, bv, kv1); z0 = mfma32(a0, ones, z0); z1 = mfma32(a1, ones, z1);
    }
  }
  STAMP(x.wsp, 11 + 16 * x.layer);
  {
    lds_t* mine = x.L + LD_PV + x.w * PV_WAVE + x.lane * 16;
    if (ph == 1) { *(LAS f4*)(mine) = kv0; *(LAS f4*)(mine + 1024) = kv1; *(LAS f4*)(mine + 2048) = z0; *(LAS f4*)(mine + 3072) = z1; }
    __syncthreads();
    if (ph == 0) {
      lds_t* o = x.L + LD_PV + (x.w + 2) * PV_WAVE + x.lane * 16;
      kv0 += *(LAS f4*)(o); kv1 += *(LAS f4*)(o + 1024); z0 += *(LAS f4*)(o + 2048); z1 += *(LAS f4*)(o + 3072);
      const int hr = (2 * x.hp + h) * 16 + x.rb;
      st16(x.ws, WS_KVP + x.layer * KVP_BYTES + hr * 1024 + x.lane * 16,
           u4{pack2(kv0[0], kv0[1]), pack2(kv0[2], kv0[3]), pack2(kv1[0], kv1[1]), pack2(kv1[2], kv1[3])});
      if (cl == 0) st16(x.ws, WS_ZP + x.layer * ZP_BYTES + hr * 64 + g * 16,
                        u4{pack2(z0[0], z0[1]), pack2(z0[2], z0[3]), pack2(z1[0], z1[1]), pack2(z1[2], z1[3])});
    }
  }
  STAMP(x.wsp, 12 + 16 * x.layer);
  {
    f4 aq[4];
#pragma unroll
    for (int i = 0; i < 4; ++i) aq[i] = f4{0, 0, 0, 0};
    u4 b[3][4];
    q_ldb<0>(ba, b[0]); q_ldb<1>(ba, b[1]);
    SB();
    QPass2<0>::run(ba, wf, b, aq);
    {
      const int cb = x.hp >> 1, q = cl >> 2, pos = 64 * (x.hp & 1) + 16 * x.w + 4 * (cl & 3);
      const int row = 64 * cb + 4 * g + q;
      unsigned ro = LD_XT + row * 256 + (((pos >> 3) ^ ((row & 7) << 1)) * 16) + (pos & 4) * 2;
      asm volatile("" : "+v"(ro));
#pragma unroll
      for (int ct = 0; ct < 4; ++ct) st.res[ct] = lds_tr(x.L + ro + ct * 4096);
    }
    o_load_partials(x, st);
    f4 t[4];
#pragma unroll
    for (int pt = 0; pt < 4; ++pt) t[pt] = aq[pt] - mean[pt] * wsum[0];
    softmax4(t, cs, csr, scr_w, scr_r, st.pq);
  }
  STAMP(x.wsp, 13 + 16 * x.layer);
}

DI void o_compute(const Ctx& x, QState& st) {
  const int h = x.w & 1, ph = x.w >> 1, g = x.lane >> 4, cl = x.lane & 15, hh = 2 * x.hp + h;
  STAMP(x.wsp, 14 + 16 * x.layer);
  const unsigned one = g == (cl >> 2) ? ((cl & 1) ? 0x3f800000u : 0x3f80u) : 0u;
  const unsigned e0 = (cl & 2) ? 0u : one, e1 = (cl & 2) ? one : 0u;
  const u4 sel0 = {e0, e1, 0u, 0u}, sel1 = {0u, 0u, e0, e1};
  f4 k0, k1, den[4];
  for (unsigned spin = 0;; ++spin) {
    k0 = f4{0, 0, 0, 0}; k1 = k0;
#pragma unroll
    for (int i = 0; i < 8; ++i) { k0 = mfma32(sel0, st.pk8[i], k0); k1 = mfma32(sel1, st.pk8[i], k1); }
    const u2 ones = {0x3f803f80u, 0x3f803f80u};
    f4 dp[4];
#pragma unroll
    for (int pt = 0; pt < 4; ++pt) dp[pt] = mfma32(st.az, st.pq[pt], f4{0, 0, 0, 0});
#pragma unroll
    for (int pt = 0; pt < 4; ++pt) den[pt] = mfma16(ones, u2{pack2(dp[pt][0], dp[pt][1]), pack2(dp[pt][2], dp[pt][3])}, f4{1e-6f, 1e-6f, 1e-6f, 1e-6f});
    const float chk = (k0[0] + k1[0]) + den[0][0];
    if (!__any(chk != chk)) break;
    if (spin > 4096u) { spin_fail(x); break; }
    o_load_partials(x, st);
  }
  lds_t* mine = x.L + LD_PV + 8192 + x.w * 2048 + x.lane * 16;
  *(LAS f4*)(mine) = k0; *(LAS f4*)(mine + 1024) = k1;
  __syncthreads();
  STAMP(x.wsp, 15 + 16 * x.layer);
  lds_t* o = x.L + LD_PV + 8192 + (x.w ^ 2) * 2048 + x.lane * 16;
  k0 += *(LAS f4*)(o); k1 += *(LAS f4*)(o + 1024);
  const u4 akv = {pack2(k0[0], k0[1]), pack2(k0[2], k0[3]), pack2(k1[0], k1[1]), pack2(k1[2], k1[3])};
  f4 num[4];
#pragma unroll
  for (int pt = 0; pt < 4; ++pt) num[pt] = mfma32(akv, st.pq[pt], f4{0, 0, 0, 0});
  const unsigned oo = WS_OB + x.layer * OB_BYTES + ((2 * x.rb + ph) * 32 + hh) * 2048 + (16 * ((g & 1) ^ (2 * h)) + cl) * 32 + 16 * (g >> 1);
#pragma unroll
  for (int pt = 0; pt < 4; pt += 2) {
    const f4 va = num[pt] * __builtin_amdgcn_rcpf(den[pt][0]), vb = num[pt + 1] * __builtin_amdgcn_rcpf(den[pt + 1][0]);
    const auto s0 = __builtin_amdgcn_permlane16_swap(pack2(va[0], va[1]), pack2(vb[0], vb[1]), false, false);
    const auto s1 = __builtin_amdgcn_permlane16_swap(pack2(va[2], va[3]), pack2(vb[2], vb[3]), false, false);
    st16(x.ws, oo ^ (pt * 512), u4{s0[0], s1[0], s0[1], s1[1]});
  }
}

struct OAddr { lds_t* p[4]; };
template <int KS> DI void o_ldw(const OAddr& wb, u4 (&b)[4]) {
#pragma unroll
  for (int ct = 0; ct < 4; ++ct) b[ct] = cat(lds_tr(wb.p[ct] + KS * 4096), lds_tr(wb.p[ct] + KS * 4096 + 512));
}
template <int KS> struct OGemm {
  static DI void run(const OAddr& wb, const u4 (&of)[16], u4 (&b)[3][4], f4 (&acc)[4]) {
    if (KS < 14) o_ldw<(KS < 14 ? KS + 2 : 15)>(wb, b[(KS + 2) % 3]);
#pragma unroll
    for (int ct = 0; ct < 4; ++ct) acc[ct] = mfma32(b[KS % 3][ct], of[KS], acc[ct]);
    if (KS < 14) {
#pragma unroll
      for (int i = 0; i < 4; ++i) { SGB(SG_MFMA, 1); SGB(SG_DSR, 2); }
    }
    SB();
    OGemm<KS + 1>::run(wb, of, b, acc);
  }
};
template <> struct OGemm<16> { static DI void run(const OAddr&, const u4 (&)[16], u4 (&)[3][4], f4 (&)[4]) {} };

DI void o_gemm(const Ctx& x, const Params& p, const QState& st, const Poll& pl) {
  const int g = x.lane >> 4, cl = x.lane & 15, pb = 2 * x.rb + (x.hp & 1), cb = x.hp >> 1;
  STAMP(x.wsp, 17 + 16 * x.layer);
  u4 of[16];
  const unsigned oo = WS_OB + x.layer * OB_BYTES + ((pb * 32 + (g >> 1)) * 64 + 16 * x.w + cl) * 32 + 16 * (g & 1);
#pragma unroll
  for (int ks = 0; ks < 16; ++ks) of[ks] = ld16(x.ws, oo + ks * 4096);
  OAddr wb;
  {
    const int q = cl >> 2, pp = cl & 3, f = ((q >> 1) & 1) | ((g & 1) << 1);
#pragma unroll
    for (int ct = 0; ct < 4; ++ct) wb.p[ct] = x.L + LD_XT + (8 * g + q) * 128 + (((2 * ct + (pp >> 1)) ^ (f << 1)) * 16) + 8 * (pp & 1);
  }
  f4 acc[4];
#pragma unroll
  for (int i = 0; i < 4; ++i) acc[i] = f4{0, 0, 0, 0};
  u4 b[3][4];
  o_ldw<0>(wb, b[0]); o_ldw<1>(wb, b[1]);
  SB();
  OGemm<0>::run(wb, of, b, acc);
  poll_retire(pl);
  STAMP(x.wsp, 18 + 16 * x.layer);
  float s = 0.f, q = 0.f;
#pragma unroll
  for (int ct = 0; ct < 4; ++ct) {
    const unsigned a = pack2(blo(st.res[ct].x) + acc[ct][0], bhi(st.res[ct].x) + acc[ct][1]);
    const unsigned b2 = pack2(blo(st.res[ct].y) + acc[ct][2], bhi(st.res[ct].y) + acc[ct][3]);
    const float v0 = blo(a), v1 = bhi(a), v2 = blo(b2), v3 = bhi(b2);
    s += (v0 + v1) + (v2 + v3); q += (v0 * v0 + v1 * v1) + (v2 * v2 + v3 * v3);
    *(LAS u2*)(x.L + LD_OST + (16 * x.w + cl) * 144 + (16 * ct + 4 * g) * 2) = u2{a, b2};
  }
  if (x.layer == 0) {
    const f2 sq = xrow_sum2(s, q);
    if (g < 2) {
      GAS float* d = (GAS float*)(p.ws + WS_ST + 2048 * 8) + g * 2048 + 64 * pb + 16 * x.w + cl;
      __hip_atomic_fetch_add(d, g ? sq.y : sq.x, RLX_AGENT);
    }
  }
  __syncthreads();
  if (x.layer == 0) {
    const int qq = cl >> 2, pp = cl & 3;
#pragma unroll
    for (int j = 0; j < 2; ++j) {
      lds_t* a = x.L + LD_OST + (8 * (g + 4 * j) + qq) * 144 + (16 * x.w + 4 * pp) * 2;
      const u4 v = cat(lds_tr(a), lds_tr(a + 4 * 144));
      st16(x.ws, WS_XT + XT_BYTES + ((64 * cb + 16 * x.w + cl) * XT_STRIDE + 64 * pb + 8 * (g + 4 * j)) * 2, v);
    }
  } else {
#pragma unroll
    for (int i = 0; i < 2; ++i) {
      const int k = i * 256 + x.tid, pos = k >> 3, q8 = k & 7;
      *(GAS u4*)(p.out + (64 * pb + pos) * DM + 64 * cb + 8 * q8) = *(LAS u4*)(x.L + LD_OST + pos * 144 + q8 * 16);
    }
  }
  STAMP(x.wsp, 19 + 16 * x.layer);
}

DI Ctx make_ctx(const Params& p, unsigned char* smem) {
  Ctx x;
  x.L = (lds_t*)smem; x.ws = make_rsrc(p.ws, WS_END); x.cw = (GAS unsigned*)(p.ws + WS_CTRL); x.wsp = p.ws;
  x.tid = threadIdx.x; x.lane = x.tid & 63; x.w = __builtin_amdgcn_readfirstlane(x.tid >> 6); x.c = blockIdx.x;
  x.cp = (x.c & 7) * 32 + (x.c >> 3);
  x.rb = 2 * (x.c & 7) + (x.c >> 7); x.hp = (x.c >> 3) & 15; x.layer = 0;
  return x;
}

DI Ctx fresh(const Ctx& x, int layer) { Ctx y = x; int t = x.tid; asm volatile("" : "+v"(t)); y.tid = t; y.lane = t & 63; y.layer = layer; return y; }

__global__ void __launch_bounds__(256, 1) k_main(Params p) {
  extern __shared__ __attribute__((aligned(16))) unsigned char smem[];
  const Ctx x0 = make_ctx(p, smem);
  STAMP(p.ws, 0);
  u4 wf[48];
  f4 wsum[3];
  const float dsk = bf2f(p.dsk[2 * x0.cp + (x0.w >> 1)]);
  unsigned tag;
  {
    const Ctx x = fresh(x0, 0);
    u4 kd[3], a[32];
    ProIn pin;
    pro_load(x, p, pin);
    pro_init(x);
    tag = pin.epoch + 1u;
    pro_publish_x(x, pin);
    pro_post(x, WS_FLAG, tag);
    STAMP(p.ws, 1);
    pro_fold_w(x, 6 * x.c + x.w, pin.w[0]);
    if (x.w < 2) pro_fold_w(x, 6 * x.c + 4 + x.w, pin.w[1]);
    pro_tables(x, pin);
    pro_post(x, WS_FLAG2, tag);
    STAMP(p.ws, 2);
    const unsigned fl = __builtin_amdgcn_raw_buffer_load_b32(x.ws, WS_FLAG + x.tid * 64, 0, AUX_SC1);
    pro_kgemm(x);
    pro_wait(x, WS_FLAG, tag, fl);
    STAMP(p.ws, 3);
    ConvIn in;
    conv_load_in(x, 0, in);
    const unsigned fl2 = __builtin_amdgcn_raw_buffer_load_b32(x.ws, WS_FLAG2 + x.tid * 64, 0, AUX_SC1);
    SB();
    pro_expand(x);
    __syncthreads();
    pro_take_k(x, kd);
    conv_build_k(x, kd);
    STAMP(p.ws, 4);
    conv_build_u(x, in);
    pro_wait(x, WS_FLAG2, tag, fl2);
    STAMP(p.ws, 5);
    if (x.w & 1) { conv_load_a<1>(x, a); conv_wave<1, true>(x, dsk, a, wf); } else { conv_load_a<0>(x, a); conv_wave<0, true>(x, dsk, a, wf); }
    a2a_post(fresh(x0, 0), 0);
    drain();
    SB();
    q_wsum(wf, wsum);
  }
#pragma unroll 1
  for (int layer = 0; layer < 2; ++layer) {
    Ctx x = fresh(x0, layer);
    Poll pl;
    a2a_wait(x, 2 * layer, pl);
    STAMP(p.ws, 8 + 16 * layer);
    QState st;
    x = fresh(x0, layer);
    q_phase(x, p, wf, wsum, st, pl);
    x = fresh(x0, layer);
    o_compute(x, st);
    grp_signal(x, (layer * 2 + 1) * 16 + x.rb);
    x = fresh(x0, layer);
    if (layer == 0) {
      u4 kd[3];
      conv_load_k(x, kd);
      o_dma_wo(x, p);
      conv_build_k(x, kd);
    } else {
      o_dma_wo(x, p);
    }
    if (x.w) drain();
    STAMP(p.ws, 16 + 16 * layer);
    x = fresh(x0, layer);
    grp_wait(x, (layer * 2 + 1) * 16 + x.rb, pl);
    o_gemm(x, p, st, pl);
    if (layer == 0) {
      a2a_signal(x, 1);
      x = fresh(x0, 1);
      u4 a[32];
      if (x.w & 1) conv_load_a<1>(x, a); else conv_load_a<0>(x, a);
      SB();
      a2a_wait(x, 1, pl);
      STAMP(p.ws, 20);
      ConvIn in;
      conv_load_in(x, 1, in);
      conv_build_u(x, in);
      poll_retire(pl);
      __syncthreads();
      STAMP(p.ws, 21);
      if (x.w & 1) conv_wave<1, false>(x, dsk, a, wf); else conv_wave<0, false>(x, dsk, a, wf);
      a2a_post(fresh(x0, 1), 2);
    }
  }
  if (x0.c == 0 && x0.tid == 0) __builtin_amdgcn_raw_buffer_store_b32(tag, x0.ws, WS_EPOCH, 0, AUX_SC1);
}

extern "C" void kernel_launch(void* const* d_in, const int* in_sizes, int n_in, void* d_out, int out_size, void* d_ws, size_t ws_size,
                              hipStream_t stream) {
  Params p;
  p.u = (const unsigned short*)d_in[0]; p.lar = (const unsigned short*)d_in[1]; p.aim = (const unsigned short*)d_in[2];
  p.cre = (const unsigned short*)d_in[3]; p.cim = (const unsigned short*)d_in[4]; p.ldt = (const unsigned short*)d_in[5];
  p.dsk = (const unsigned short*)d_in[6]; p.wqkv = (const unsigned short*)d_in[8]; p.wphi = (const unsigned short*)d_in[9];
  p.wo = (const unsigned short*)d_in[10]; p.out = (unsigned short*)d_out; p.ws = (unsigned char*)d_ws;
  (void)hipFuncSetAttribute((const void*)k_main, hipFuncAttributeMaxDynamicSharedMemorySize, LDS_BYTES);
  k_main<<<256, 256, LDS_BYTES, stream>>>(p);
}
```

```cpp
#include <hip/hip_runtime.h>
#include <hip/hip_bf16.h>
#include <stdint.h>

#define MK_STAMP 0
#define STAMP(ws, i) do { } while (0)
#define STAMP2(x, i) do { } while (0)
#define STAMP3(x, i) do { } while (0)
typedef short s4 __attribute__((ext_vector_type(4)));
typedef short s8 __attribute__((ext_vector_type(8)));
typedef float f2 __attribute__((ext_vector_type(2)));
typedef float f4 __attribute__((ext_vector_type(4)));
typedef unsigned u2 __attribute__((ext_vector_type(2)));
typedef unsigned u4 __attribute__((ext_vector_type(4)));
typedef __bf16 bf2 __attribute__((ext_vector_type(2)));
#define GAS __attribute__((address_space(1)))
#define LAS __attribute__((address_space(3)))
#define DI __device__ __forceinline__
#define RLX_AGENT __ATOMIC_RELAXED, __HIP_MEMORY_SCOPE_AGENT
typedef GAS unsigned gu32;
typedef LAS unsigned char lds_t;
typedef __amdgpu_buffer_rsrc_t rsrc_t;

constexpr int SEQ = 2048, DM = 512, NST = 128, NH = 32;
constexpr float LOG2E = 1.4426950408889634f;
constexpr float LN_EPS = 1e-5f;
constexpr int AUX_SC1 = 16;

constexpr int XT_STRIDE = 2112;
constexpr unsigned WS_CTRL = 0;
constexpr unsigned WS_KREV = 262144;
constexpr unsigned WS_WFRAG = WS_KREV + 512u * 2048 * 2;
constexpr unsigned WS_XT = WS_WFRAG + 32u * 3 * 16 * 1024;
constexpr unsigned XT_BYTES = 512u * XT_STRIDE * 2;
constexpr unsigned WS_ST = WS_XT + 2 * XT_BYTES;
constexpr unsigned WS_X1 = WS_ST + 2u * 2048 * 8;
constexpr unsigned X1_BYTES = 2048u * 512 * 2;
constexpr unsigned WS_KVP = WS_X1 + 2 * X1_BYTES;
constexpr unsigned KVP_BYTES = 32u * 16 * 1024;
constexpr unsigned WS_ZP = WS_KVP + 2 * KVP_BYTES;
constexpr unsigned ZP_BYTES = 32u * 16 * 64;
constexpr unsigned WS_OB = WS_ZP + 2 * ZP_BYTES;
constexpr unsigned OB_BYTES = 2048u * 512 * 2;
constexpr unsigned WS_END = WS_OB + 2 * OB_BYTES;
constexpr int CW_TMO = 0;
constexpr int NCW_A2A = 3 * 8 * 16;
constexpr int CW_STRIDE = 64;
constexpr int CWI_A2A = 1, CWI_END = CWI_A2A + NCW_A2A;
constexpr unsigned WS_EPOCH = 131072, WS_FLAG = WS_EPOCH + 256, WS_FLAG2 = WS_FLAG + 256 * 64;
static_assert(CWI_END * CW_STRIDE * 4 <= WS_EPOCH && WS_FLAG2 + 256 * 64 <= WS_KREV, "control block");

struct Params {
  const unsigned short* u;
  const unsigned short* lar;
  const unsigned short* aim;
  const unsigned short* cre;
  const unsigned short* cim;
  const unsigned short* ldt;
  const unsigned short* dsk;
  const unsigned short* wqkv;
  const unsigned short* wphi;
  const unsigned short* wo;
  unsigned short* out;
  unsigned char* ws;
};

DI float bf2f(unsigned short b) { return __builtin_bit_cast(float, (unsigned)b << 16); }
DI float blo(unsigned x) { return __builtin_bit_cast(float, x << 16); }
DI float bhi(unsigned x) { return __builtin_bit_cast(float, x & 0xffff0000u); }
DI unsigned pack2(float a, float b) { bf2 v = {(__bf16)a, (__bf16)b}; return __builtin_bit_cast(unsigned, v); }
DI float rbf(float a) { return blo(pack2(a, a)); }
DI f4 mfma32(u4 a, u4 b, f4 c) { return __builtin_amdgcn_mfma_f32_16x16x32_bf16(__builtin_bit_cast(s8, a), __builtin_bit_cast(s8, b), c, 0, 0, 0); }
DI f4 mfma16(u2 a, u2 b, f4 c) { return __builtin_amdgcn_mfma_f32_16x16x16bf16_1k(__builtin_bit_cast(s4, a), __builtin_bit_cast(s4, b), c, 0, 0, 0); }
DI u2 lds_tr(lds_t* p) { return __builtin_bit_cast(u2, __builtin_amdgcn_ds_read_tr16_b64_v4i16((LAS s4*)p)); }
DI u4 cat(u2 a, u2 b) { return u4{a.x, a.y, b.x, b.y}; }
DI rsrc_t make_rsrc(const void* p, unsigned bytes) { return __builtin_amdgcn_make_buffer_rsrc((void*)p, 0, (int)bytes, 0x00020000); }
DI u4 ld16(rsrc_t r, unsigned off) { return __builtin_amdgcn_raw_buffer_load_b128(r, off, 0, AUX_SC1); }
DI void st16(rsrc_t r, unsigned off, u4 v) { __builtin_amdgcn_raw_buffer_store_b128(v, r, off, 0, AUX_SC1); }
DI void drain() { asm volatile("s_waitcnt vmcnt(0)" ::: "memory"); }
DI void dma4_sc1(rsrc_t r, unsigned v0, unsigned v1, unsigned soff, unsigned lds) {
  asm volatile("s_mov_b32 m0, %4\n\ts_nop 0\n\t"
               "buffer_load_dwordx4 %0, %2, %3 offen sc1 lds\n\tbuffer_load_dwordx4 %1, %2, %3 offen offset:1024 sc1 lds\n\t"
               "buffer_load_dwordx4 %0, %2, %3 offen offset:2048 sc1 lds\n\tbuffer_load_dwordx4 %1, %2, %3 offen offset:3072 sc1 lds"
               :: "v"(v0), "v"(v1), "s"(r), "s"(soff), "s"(lds) : "memory");
}
DI void dma4(rsrc_t r, unsigned v0, unsigned v1, unsigned v2, unsigned v3, unsigned soff, unsigned lds) {
  asm volatile("s_mov_b32 m0, %6\n\ts_nop 0\n\t"
               "buffer_load_dwordx4 %0, %4, %5 offen lds\n\tbuffer_load_dwordx4 %1, %4, %5 offen offset:1024 lds\n\t"
               "buffer_load_dwordx4 %2, %4, %5 offen offset:2048 lds\n\tbuffer_load_dwordx4 %3, %4, %5 offen offset:3072 lds"
               :: "v"(v0), "v"(v1), "v"(v2), "v"(v3), "s"(r), "s"(soff), "s"(lds) : "memory");
}
#define SB() __builtin_amdgcn_sched_barrier(0)
#define SGB(mask, n) __builtin_amdgcn_sched_group_barrier(mask, n, 0)
constexpr int SG_MFMA = 0x8, SG_DSR = 0x100;
template <int CTRL> DI float dpp_f(float v) { return __builtin_bit_cast(float, __builtin_amdgcn_mov_dpp(__builtin_bit_cast(int, v), CTRL, 0xf, 0xf, true)); }
DI float row_sum(float v) { v += dpp_f<0x128>(v); v += dpp_f<0x124>(v); v += dpp_f<0x122>(v); v += dpp_f<0x121>(v); return v; }
DI f2 swap32(float a, float b) { auto r = __builtin_amdgcn_permlane32_swap(__builtin_bit_cast(unsigned, a), __builtin_bit_cast(unsigned, b), false, false);
  const unsigned r0 = r[0], r1 = r[1];
  return f2{__builtin_bit_cast(float, r0), __builtin_bit_cast(float, r1)}; }
DI f2 swap16(float a, float b) { auto r = __builtin_amdgcn_permlane16_swap(__builtin_bit_cast(unsigned, a), __builtin_bit_cast(unsigned, b), false, false);
  const unsigned r0 = r[0], r1 = r[1];
  return f2{__builtin_bit_cast(float, r0), __builtin_bit_cast(float, r1)}; }
DI float fmax2(float a, float b) { return __builtin_fmaxf(a, b); }
DI float fmin2(float a, float b) { return __builtin_fminf(a, b); }
template <int OP> DI float op2(float a, float b) { return OP == 0 ? fmax2(a, b) : OP == 1 ? fmin2(a, b) : a + b; }
template <int OP> DI float xrow4(float a, float b, float c, float d) {
  f2 s = swap32(a, b); const float ab = op2<OP>(s.x, s.y);
  s = swap32(c, d); const float cd = op2<OP>(s.x, s.y);
  s = swap16(ab, cd); return op2<OP>(s.x, s.y);
}
DI f2 xrow_max2(float a, float b) { f2 s = swap32(a, b); float c = fmaxf(s.x, s.y); s = swap16(c, c); c = fmaxf(s.x, s.y); return swap32(c, c); }
DI f2 xrow_sum2(float a, float b) { f2 s = swap32(a, b); float c = s.x + s.y; s = swap16(c, c); c = s.x + s.y; return swap32(c, c); }
DI float wave_sum(float v) { v = row_sum(v); f2 s = swap16(v, v); v = s.x + s.y; s = swap32(v, v); return s.x + s.y; }

constexpr int LD_XT = 0;
constexpr int LD_KC = 65536;
constexpr int KC_COPY = 4160, KC_CH = 4 * KC_COPY;
constexpr int LD_U = LD_KC + 2 * KC_CH;
constexpr int U_CH = 4608;
constexpr int LD_X = LD_U + 2 * U_CH;
constexpr int LD_EX = LD_KC;
constexpr int LD_CONV_END = LD_X + 8192;
constexpr int LD_WA = 0, LD_WB = LD_CONV_END;
constexpr int LD_MISC = 131072;
constexpr int LD_PV = LD_MISC;
constexpr int PV_ROW = 104, PV_WAVE = 64 * PV_ROW;
constexpr int LD_STAT = LD_PV + 4 * PV_WAVE;
constexpr int LD_SCR = LD_STAT + 1024 + 256;
constexpr int LD_FLAG = LD_SCR + 2048;
constexpr int LD_OST = LD_PV;
constexpr int LDS_BYTES = 163840;
static_assert(LD_CONV_END <= LD_MISC && LD_FLAG + 64 <= LDS_BYTES, "LDS map");

struct Ctx {
  lds_t* L; rsrc_t ws; GAS unsigned* cw; unsigned char* wsp;
  int tid, lane, w, c, cp, rb, hp, layer;
};

DI void spin_fail(const Ctx& x) { __hip_atomic_store(x.cw + CW_TMO, 1u, RLX_AGENT); }
constexpr unsigned SPIN_CAP = 1u << 18;
DI void a2a_post(const Ctx& x, int e) {
  __syncthreads();
  if (x.tid < 8) __hip_atomic_fetch_add(x.cw + (CWI_A2A + (e * 8 + x.tid) * 16 + (x.c >> 4)) * CW_STRIDE, 1u, RLX_AGENT);
}
DI void a2a_signal(const Ctx& x, int e) { drain(); a2a_post(x, e); }
struct Poll { unsigned r0, r1, r2, r3; };
DI void poll16(const Ctx& x, unsigned word, Poll& q) {
  if (x.w == 0) {
    const rsrc_t rc = make_rsrc(x.wsp + WS_CTRL, CWI_END * CW_STRIDE * 4);
    const unsigned off = word * (CW_STRIDE * 4);
    unsigned n;
    asm volatile(
        "s_mov_b32 %4, 0\n\t"
        "buffer_load_dword %0, %5, %6, 0 offen sc1\n\ts_sleep 2\n\t"
        "buffer_load_dword %1, %5, %6, 0 offen sc1\n\ts_sleep 2\n\t"
        "buffer_load_dword %2, %5, %6, 0 offen sc1\n\ts_sleep 2\n\t"
        "buffer_load_dword %3, %5, %6, 0 offen sc1\n"
        "1:\n\t"
        "s_waitcnt vmcnt(3)\n\tv_cmp_gt_u32 vcc, 16, %0\n\ts_cbranch_vccz 2f\n\tbuffer_load_dword %0, %5, %6, 0 offen sc1\n\t"
        "s_waitcnt vmcnt(3)\n\tv_cmp_gt_u32 vcc, 16, %1\n\ts_cbranch_vccz 2f\n\tbuffer_load_dword %1, %5, %6, 0 offen sc1\n\t"
        "s_waitcnt vmcnt(3)\n\tv_cmp_gt_u32 vcc, 16, %2\n\ts_cbranch_vccz 2f\n\tbuffer_load_dword %2, %5, %6, 0 offen sc1\n\t"
        "s_waitcnt vmcnt(3)\n\tv_cmp_gt_u32 vcc, 16, %3\n\ts_cbranch_vccz 2f\n\tbuffer_load_dword %3, %5, %6, 0 offen sc1\n\t"
        "s_add_u32 %4, %4, 1\n\ts_cmp_lt_u32 %4, 0x20000\n\ts_cbranch_scc1 1b\n"
        "2:"
        : "=&v"(q.r0), "=&v"(q.r1), "=&v"(q.r2), "=&v"(q.r3), "=&s"(n) : "v"(off), "s"(rc) : "vcc", "scc", "memory");
    if (n >= 0x20000u) spin_fail(x);
  }
  __syncthreads();
}
DI void poll_retire(const Poll& q) { asm volatile("" :: "v"(q.r0), "v"(q.r1), "v"(q.r2), "v"(q.r3)); }
DI void a2a_wait(const Ctx& x, int e, Poll& q) { poll16(x, CWI_A2A + (e * 8 + (x.c & 7)) * 16 + (x.lane & 15), q); }

DI void conv_load_k(const Ctx& x, u4 (&kd)[3]) {
  const int ch = x.tid >> 7, q = x.tid & 127;
  const unsigned off = WS_KREV + ((2 * x.cp + ch) * 2048 + 16 * q) * 2;
  kd[0] = __builtin_amdgcn_raw_buffer_load_b128(x.ws, off, 0, 0);
  kd[1] = __builtin_amdgcn_raw_buffer_load_b128(x.ws, off + 16, 0, 0);
  kd[2] = __builtin_amdgcn_raw_buffer_load_b128(x.ws, off + 32, 0, 0);
}
DI void conv_build_k(const Ctx& x, const u4 (&kd)[3]) {
  const int ch = x.tid >> 7, q = x.tid & 127;
  const unsigned d8 = q == 127 ? 0u : kd[2].x, d9 = q == 127 ? 0u : kd[2].y;
  const unsigned d[10] = {kd[0].x, kd[0].y, kd[0].z, kd[0].w, kd[1].x, kd[1].y, kd[1].z, kd[1].w, d8, d9};
  lds_t* b = x.L + LD_KC + ch * KC_CH + 32 * q;
  *(LAS u4*)(b) = u4{d[0], d[1], d[2], d[3]}; *(LAS u4*)(b + 16) = u4{d[4], d[5], d[6], d[7]};
  *(LAS u4*)(b + 2 * KC_COPY) = u4{d[1], d[2], d[3], d[4]}; *(LAS u4*)(b + 2 * KC_COPY + 16) = u4{d[5], d[6], d[7], d[8]};
  unsigned e[9];
#pragma unroll
  for (int i = 0; i < 9; ++i) e[i] = __builtin_amdgcn_alignbit(d[i + 1], d[i], 16);
  *(LAS u4*)(b + KC_COPY) = u4{e[0], e[1], e[2], e[3]}; *(LAS u4*)(b + KC_COPY + 16) = u4{e[4], e[5], e[6], e[7]};
  *(LAS u4*)(b + 3 * KC_COPY) = u4{e[1], e[2], e[3], e[4]}; *(LAS u4*)(b + 3 * KC_COPY + 16) = u4{e[5], e[6], e[7], e[8]};
  if (x.tid < 16) {
    *(LAS u4*)(x.L + LD_KC + (x.tid >> 1) * KC_COPY + 4096 + (x.tid & 1) * 16) = u4{0, 0, 0, 0};
  }
  if (x.tid >= 64 && x.tid < 128) {
    const int i = x.tid - 64;
    *(LAS u4*)(x.L + LD_U + (i >> 5) * U_CH + (i & 31) * 16) = u4{0, 0, 0, 0};
  }
}
template <int PAR> struct Mu { static constexpr int v(int k) { return PAR ? (k == 0 ? 1 : k == 1 ? 2 : k == 2 ? 5 : 6) : (k == 0 ? 0 : k == 1 ? 3 : k == 2 ? 4 : 7); } };
template <int PAR> DI lds_t* conv_a_base(const Ctx& x) {
  const int b = x.lane & 15, kq = x.lane >> 4, ch = x.w >> 1;
  const int s0 = 2047 - 16 * (kq >> 1) - b + 8 * (kq & 1), cc = s0 & 3;
  unsigned bo = LD_KC + ch * KC_CH + cc * KC_COPY + 2 * (s0 - cc) - 64 * 63;
  asm volatile("" : "+v"(bo));
  return x.L + bo;
}
template <int PAR> DI u4 conv_a_frag(lds_t* base, int k, int i) {
  const int m = 8 * Mu<PAR>::v(k) + i;
  const u2 lo = *(volatile LAS u2*)(base + 64 * (63 - m)), hi = *(volatile LAS u2*)(base + 64 * (63 - m) + 8);
  return cat(lo, hi);
}
template <int PAR> DI void conv_load_a(const Ctx& x, u4 (&a)[32]) {
  lds_t* base = conv_a_base<PAR>(x);
#pragma unroll
  for (int k = 0; k < 4; ++k)
#pragma unroll
    for (int i = 0; i < 8; ++i) a[8 * k + i] = conv_a_frag<PAR>(base, k, i);
}
struct WDma { unsigned lane16, soff, lo, hi; };
DI WDma q_dma_w_setup(const Ctx& x) {
  const unsigned lo = x.w < 3 ? LD_WA + x.w * 24576 : LD_WB + 8192, hi = x.w < 2 ? lo : x.w == 2 ? LD_WB - 16384 : lo;
  return WDma{(unsigned)x.lane * 16, WS_WFRAG + x.hp * (96 * 1024) + x.w * 24576, lo, hi};
}
template <int I> DI void q_dma_w1(const Ctx& x, const WDma& d) {
  unsigned t;
  asm volatile("s_add_u32 m0, %4, %5\n\ts_add_u32 %0, %3, %5\n\tbuffer_load_dwordx4 %1, %2, %0 offen lds"
               : "=&s"(t) : "v"(d.lane16), "s"(x.ws), "s"(d.soff), "s"(I < 16 ? d.lo : d.hi), "n"(I * 1024) : "memory", "scc");
}
DI void q_take_w(const Ctx& x, u4 (&wf)[48]) {
  const int h = x.w & 1;
  const unsigned a0 = LD_WA + h * 49152 + x.lane * 16, a1 = (h ? LD_WB - 16384 : LD_WA) + x.lane * 16;
#pragma unroll
  for (int i = 0; i < 48; ++i)
    asm volatile("ds_read_b128 %0, %1 offset:%2" : "=a"(wf[i]) : "v"(i < 16 ? a0 : a1), "n"(i * 1024));
  asm volatile("s_waitcnt lgkmcnt(0)" ::: "memory");
  SB();
}
template <int TAU, int I> DI void conv_dma_w(const Ctx& x, const WDma& d) { if ((I & 1) && TAU < 6) { q_dma_w1<(TAU < 6 ? 4 * TAU + (I >> 1) : 0)>(x, d); SB(); } }
template <int PAR, bool FIRST> DI void conv_mfma(const Ctx& x, u4 (&a)[32], f4 (&acc)[8]) {
  const WDma wd = q_dma_w_setup(x);
  const int al = x.lane & 15, kq = x.lane >> 4, ch = x.w >> 1;
  unsigned bo = LD_U + ch * U_CH + 2 * (256 + 16 * al - 16 * (kq >> 1) + 8 * (kq & 1)) - 64 * 7;
  asm volatile("" : "+v"(bo));
  lds_t* base = x.L + bo;
  constexpr int NT = 8 - Mu<PAR>::v(0);
  u4 b[8];
  if (FIRST) {
    STAMP3(x, 37);
    lds_t* abase = conv_a_base<PAR>(x);
#pragma unroll
    for (int i = 0; i < 8; ++i) { b[i] = *(LAS u4*)(base + 64 * (7 - i)); a[i] = conv_a_frag<PAR>(abase, 0, i); }
    SB();
#pragma unroll
    for (int k = 0; k < 4; ++k) {
#pragma unroll
      for (int i = 0; i < 8; ++i) {
        acc[Mu<PAR>::v(k)] = mfma32(a[8 * k + i], b[i], acc[Mu<PAR>::v(k)]);
        if (k < 3) { a[8 * (k + 1) + i] = conv_a_frag<PAR>(abase, k + 1, i); SGB(SG_MFMA, 1); SGB(SG_DSR, 2); }
        else { b[i] = *(LAS u4*)(base + 64 * (8 - i + 7)); SGB(SG_MFMA, 1); SGB(SG_DSR, 1); }
        SB();
      }
      switch (k) {
        case 0: q_dma_w1<0>(x, wd); break; case 1: q_dma_w1<1>(x, wd); break;
        case 2: q_dma_w1<2>(x, wd); break; default: q_dma_w1<3>(x, wd); break;
      }
      SB();
    }
    STAMP3(x, 38);
  } else {
#pragma unroll
    for (int i = 0; i < 8; ++i) b[i] = *(LAS u4*)(base + 64 * (7 - i));
    SB();
  }
#pragma unroll
  for (int tau = FIRST ? 1 : 0; tau < 8; ++tau) {
    if (FIRST && tau == 4) STAMP3(x, 39);
    if (tau < NT) {
      int ng = 0;
#pragma unroll
      for (int k = 0; k < 4; ++k) ng += (Mu<PAR>::v(k) + tau <= 7);
#pragma unroll
      for (int i = 0; i < 8; ++i) {
#pragma unroll
        for (int k = 0; k < 4; ++k)
          if (Mu<PAR>::v(k) + tau <= 7) acc[Mu<PAR>::v(k) + tau] = mfma32(a[8 * k + i], b[i], acc[Mu<PAR>::v(k) + tau]);
        if (tau + 1 < NT) {
          b[i] = *(LAS u4*)(base + 64 * (8 * (tau + 1) - i + 7));
          if (ng == 4) SGB(SG_MFMA, 4); else if (ng == 3) SGB(SG_MFMA, 3); else if (ng == 2) SGB(SG_MFMA, 2); else SGB(SG_MFMA, 1);
          SGB(SG_DSR, 1);
        }
        SB();
        if (FIRST) {
          switch (8 * tau + i) {
#define W_(t, j) case 8 * t + j: conv_dma_w<t, j>(x, wd); break;
#define WT_(t) W_(t, 1) W_(t, 3) W_(t, 5) W_(t, 7)
            WT_(1) WT_(2) WT_(3) WT_(4) WT_(5)
#undef WT_
#undef W_
            default: break;
          }
        }
      }
    }
  }
}

struct ConvIn { u4 x0, x1, s[4]; };
DI void conv_load_in(const Ctx& x, int layer, ConvIn& in) {
  const unsigned xo = WS_XT + layer * XT_BYTES + (2 * x.cp * XT_STRIDE + 8 * x.tid) * 2;
  const unsigned so = WS_ST + layer * 2048 * 8 + x.tid * 32;
  in.s[0] = ld16(x.ws, so); in.s[1] = ld16(x.ws, so + 16); in.s[2] = ld16(x.ws, so + 8192); in.s[3] = ld16(x.ws, so + 8192 + 16);
  in.x0 = ld16(x.ws, xo); in.x1 = ld16(x.ws, xo + XT_STRIDE * 2);
}
DI f2 unpk(unsigned v) { return f2{blo(v), bhi(v)}; }
DI unsigned pk(f2 v) { return pack2(v.x, v.y); }
DI void conv_build_u(const Ctx& x, const ConvIn& in) {
  const f4 sa = __builtin_bit_cast(f4, in.s[0]), sb = __builtin_bit_cast(f4, in.s[1]), qa = __builtin_bit_cast(f4, in.s[2]), qb = __builtin_bit_cast(f4, in.s[3]);
  const f2 sv[4] = {f2{sa[0], sa[1]}, f2{sa[2], sa[3]}, f2{sb[0], sb[1]}, f2{sb[2], sb[3]}};
  const f2 qv[4] = {f2{qa[0], qa[1]}, f2{qa[2], qa[3]}, f2{qb[0], qb[1]}, f2{qb[2], qb[3]}};
  u4 u0, u1;
#pragma unroll
  for (int i = 0; i < 4; ++i) {
    const f2 m = sv[i] * (1.f / DM);
    const f2 ve = (qv[i] * (1.f / DM) + LN_EPS) - m * m;
    const f2 r = {__builtin_amdgcn_rsqf(__builtin_fabsf(ve.x)), __builtin_amdgcn_rsqf(__builtin_fabsf(ve.y))};
    const f2 nmr = -m * r;
    u0[i] = pk(unpk(in.x0[i]) * r + nmr);
    u1[i] = pk(unpk(in.x1[i]) * r + nmr);
  }
  *(LAS u4*)(x.L + LD_U + 512 + 16 * x.tid) = u0; *(LAS u4*)(x.L + LD_U + U_CH + 512 + 16 * x.tid) = u1;
  *(LAS u4*)(x.L + LD_X + 16 * x.tid) = in.x0; *(LAS u4*)(x.L + LD_X + 4096 + 16 * x.tid) = in.x1;
}
DI void poison_o(const Ctx& x, int layer) {
#pragma unroll
  for (int i = 0; i < 2; ++i) st16(x.ws, WS_OB + layer * OB_BYTES + (x.c * 2 + i) * 4096 + x.tid * 16, u4{~0u, ~0u, ~0u, ~0u});
}
template <int PAR, bool LOADW> DI void conv_wave(const Ctx& x, float dsk, u4 (&a)[32]) {
  const int ch = x.w >> 1;
  f4 acc[8];
#pragma unroll
  for (int t = 0; t < 8; ++t) acc[t] = f4{0, 0, 0, 0};
  conv_mfma<PAR, LOADW>(x, a, acc);
  STAMP(x.wsp, 6 + 16 * x.layer);
  const int al = x.lane & 15, g = x.lane >> 4;
  unsigned eo = LD_EX + x.lane * 16, uo = LD_U + ch * U_CH + 512 + 2 * (1024 * PAR + 16 * al + 4 * g);
  asm volatile("" : "+v"(eo), "+v"(uo));
  lds_t* mine = x.L + eo + x.w * 4096;
  lds_t* other = x.L + eo + (x.w ^ 1) * 4096;
#pragma unroll
  for (int t = 0; t < 4; ++t) *(LAS f4*)(mine + t * 1024) = acc[PAR ? t : 4 + t];
  f2 v[4][2];
#pragma unroll
  for (int t = 0; t < 4; ++t) {
    const u2 uu = *(LAS u2*)(x.L + uo + 512 * t), xx = *(LAS u2*)(x.L + uo + (LD_X - LD_U - 512) + ch * (4096 - U_CH) + 512 * t);
    const f4 own = acc[PAR ? 4 + t : t];
    v[t][0] = unpk(uu.x) * dsk + (unpk(xx.x) + f2{own[0], own[1]});
    v[t][1] = unpk(uu.y) * dsk + (unpk(xx.y) + f2{own[2], own[3]});
  }
  __syncthreads();
  STAMP(x.wsp, 7 + 16 * x.layer);
  const unsigned so = WS_X1 + x.layer * X1_BYTES + (((8 * PAR + 2 * (g & 1) + (al >> 3)) * 512 + 2 * x.cp + ch) * 128 + 16 * (al & 7) + 8 * (g >> 1)) * 2;
#pragma unroll
  for (int t = 0; t < 4; t += 2) {
    const f4 o0 = *(LAS f4*)(other + t * 1024), o1 = *(LAS f4*)(other + (t + 1) * 1024);
    const unsigned a0 = pk(v[t][0] + f2{o0[0], o0[1]}), a1 = pk(v[t][1] + f2{o0[2], o0[3]});
    const unsigned b0 = pk(v[t + 1][0] + f2{o1[0], o1[1]}), b1 = pk(v[t + 1][1] + f2{o1[2], o1[3]});
    const auto s0 = __builtin_amdgcn_permlane16_swap(a0, b0, false, false), s1 = __builtin_amdgcn_permlane16_swap(a1, b1, false, false);
    st16(x.ws, so + t * (2 * 512 * 256), u4{s0[0], s1[0], s0[1], s1[1]});
  }
  if (LOADW) poison_o(x, 0);
  drain();
}

DI f2 cmul(f2 a, f2 b) { return f2{a.x, a.x} * b + f2{a.y, a.y} * f2{-b.y, b.x}; }
DI f2 cexp_p(float wr2, float wrev, float p) {
  const float e = __builtin_amdgcn_exp2f(wr2 * p);
  const float a = __builtin_amdgcn_fractf(wrev * p);
  return f2{e * __builtin_amdgcn_cosf(a), e * __builtin_amdgcn_sinf(a)};
}
constexpr int KG_ROW = 528;
constexpr int KG_A = 0;
constexpr int KG_X = 2 * 48 * KG_ROW;
constexpr int KG_T = LD_CONV_END;
constexpr int KG_B = KG_T + 2 * 2056 * 4;
static_assert(KG_X + 64 * 136 <= LD_KC && KG_B + 2 * 16 * KG_ROW <= LDS_BYTES, "prologue LDS map");

struct ProW { u2 a0, a1; unsigned short b[4]; };
struct ProIn { unsigned short lar, aim, cre, cim, ldt; u4 xt[2], xr[2]; ProW w[2]; unsigned epoch; };
DI void pro_load_w(const Params& p, int F, int lane, ProW& w) {
  const int fh = F / 48, fm = (F % 48) >> 4, fks = F & 15, row = lane & 15, kq = lane >> 4;
  w.a0 = *(const u2*)(p.wqkv + (32 * fks + row) * 1536 + fm * 512 + 16 * fh + 4 * kq);
  w.a1 = *(const u2*)(p.wqkv + (32 * fks + 16 + row) * 1536 + fm * 512 + 16 * fh + 4 * kq);
  const unsigned short* q = p.wphi + fh * 256 + (4 * kq) * 16 + row;
#pragma unroll
  for (int j = 0; j < 4; ++j) w.b[j] = q[16 * j];
}
DI void pro_fold_w(const Ctx& x, int F, const ProW& w) {
  const bool fold = ((F % 48) >> 4) < 2;
  const int dd = (x.lane & 15) - 4 * (x.lane >> 4);
  const u2 wb = fold ? u2{w.b[0] | ((unsigned)w.b[1] << 16), w.b[2] | ((unsigned)w.b[3] << 16)}
                     : u2{(dd == 0 ? 0x3f80u : 0u) | (dd == 1 ? 0x3f800000u : 0u), (dd == 2 ? 0x3f80u : 0u) | (dd == 3 ? 0x3f800000u : 0u)};
  const f4 d0 = mfma16(w.a0, wb, f4{0, 0, 0, 0}), d1 = mfma16(w.a1, wb, f4{0, 0, 0, 0});
  float e[8];
#pragma unroll
  for (int r = 0; r < 4; ++r) {
    const float b0 = dpp_f<0x150>(d0[r]), b1 = dpp_f<0x150>(d1[r]);
    e[r] = d0[r] - (fold ? b0 : 0.f); e[4 + r] = d1[r] - (fold ? b1 : 0.f);
  }
  u4 o = {pack2(e[0], e[1]), pack2(e[2], e[3]), pack2(e[4], e[5]), pack2(e[6], e[7])};
  if (fold && (x.lane & 15) == 0) o = u4{0x3f803f80u, 0x3f803f80u, 0x3f803f80u, 0x3f803f80u};
  st16(x.ws, WS_WFRAG + F * 1024 + x.lane * 16, o);
}
DI void pro_load(const Ctx& x, const Params& p, ProIn& in) {
#pragma unroll
  for (int i = 0; i < 2; ++i) {
    in.xt[i] = *(const u4*)(p.u + (64 * (x.c >> 3) + 32 * i + (x.tid >> 3)) * DM + 64 * (x.c & 7) + 8 * (x.tid & 7));
    in.xr[i] = *(const u4*)(p.u + (8 * x.c + 2 * x.w + i) * DM + 8 * x.lane);
  }
  in.epoch = __builtin_amdgcn_raw_buffer_load_b32(x.ws, WS_EPOCH, 0, AUX_SC1);
  const int d = 2 * x.cp + (x.tid >> 7), n = x.tid & 127;
  in.lar = p.lar[d * NST + n]; in.aim = p.aim[d * NST + n]; in.cre = p.cre[d * NST + n]; in.cim = p.cim[d * NST + n]; in.ldt = p.ldt[d];
  pro_load_w(p, 6 * x.c + x.w, x.lane, in.w[0]);
  pro_load_w(p, 6 * x.c + 4 + (x.w & 1), x.lane, in.w[1]);
  SB();
}
constexpr int CODE_SKIP = 5, CODE_KB = 40;
struct Warm { unsigned a, b, c; };
DI void pro_warm(const Ctx& x, const Params& p, const unsigned char* code, Warm& wm) {
  const int j = x.c >> 3;
  static_assert(CODE_KB * 8 <= 32 * 64 / 4 * 4, "one line per lane");
  wm.a = wm.b = wm.c = 0;
  if (x.w == 3) {
    const int line = j * (CODE_KB * 8 / 32) + x.lane;
    if (x.lane < CODE_KB * 8 / 32) wm.a = *(const GAS unsigned*)(code + CODE_SKIP * 1024 + line * 128);
    wm.b = *(const GAS unsigned*)((const unsigned char*)p.wo + (j * 128 + x.lane) * 128);
    wm.c = *(const GAS unsigned*)((const unsigned char*)p.wo + (j * 128 + 64 + x.lane) * 128);
  }
}
DI void pro_warm_retire(const Warm& wm) { asm volatile("" :: "v"(wm.a), "v"(wm.b), "v"(wm.c)); }
DI void pro_init(const Ctx& x) {
  const rsrc_t ws = x.ws;
  if (x.c < 2) { const int i = x.c * 256 + x.tid; if (i < CWI_END) __builtin_amdgcn_raw_buffer_store_b32(0u, ws, i * (CW_STRIDE * 4), 0, AUX_SC1); }
  if (x.c >= 8 && x.c < 12) st16(ws, WS_ST + 2048 * 8 + ((x.c - 8) * 256 + x.tid) * 16, u4{0, 0, 0, 0});
  static_assert(WS_ZP == WS_KVP + 2 * KVP_BYTES && (2 * KVP_BYTES + 2 * ZP_BYTES) == 256 * 272 * 16, "poisoned block");
  st16(ws, WS_KVP + (x.c * 272 + x.tid) * 16, u4{~0u, ~0u, ~0u, ~0u});
  if (x.tid < 16) st16(ws, WS_KVP + (x.c * 272 + 256 + x.tid) * 16, u4{~0u, ~0u, ~0u, ~0u});
}
DI void pro_publish_x(const Ctx& x, const ProIn& in) {
#pragma unroll
  for (int i = 0; i < 2; ++i) {
    lds_t* a = x.L + KG_X + (32 * i + (x.tid >> 3)) * 136 + (x.tid & 7) * 16;
    *(LAS u2*)a = u2{in.xt[i].x, in.xt[i].y}; *(LAS u2*)(a + 8) = u2{in.xt[i].z, in.xt[i].w};
  }
  {
    f2 a[2], q[2];
#pragma unroll
    for (int i = 0; i < 2; ++i) {
      a[i] = unpk(in.xr[i][0]); q[i] = a[i] * a[i];
#pragma unroll
      for (int k = 1; k < 4; ++k) { const f2 v = unpk(in.xr[i][k]); a[i] += v; q[i] += v * v; }
    }
    const float t = xrow4<2>(row_sum(a[0].x + a[0].y), row_sum(q[0].x + q[0].y), row_sum(a[1].x + a[1].y), row_sum(q[1].x + q[1].y));
    const int g = x.lane >> 4;
    if ((x.lane & 15) == 0)
      __builtin_amdgcn_raw_buffer_store_b32(__builtin_bit_cast(unsigned, t), x.ws, WS_ST + (g >> 1) * 8192 + (8 * x.c + 2 * x.w + (g & 1)) * 4, 0, AUX_SC1);
  }
  __syncthreads();
  const int g = x.lane >> 4, cl = x.lane & 15;
#pragma unroll
  for (int j = 0; j < 2; ++j) {
    lds_t* a = x.L + KG_X + (8 * (g + 4 * j) + (cl >> 2)) * 136 + (16 * x.w + 4 * (cl & 3)) * 2;
    st16(x.ws, WS_XT + ((64 * (x.c & 7) + 16 * x.w + cl) * XT_STRIDE + 64 * (x.c >> 3) + 8 * (g + 4 * j)) * 2, cat(lds_tr(a), lds_tr(a + 4 * 136)));
  }
}
DI void pro_tables(const Ctx& x, const ProIn& in) {
  const float dt = rbf(__expf(bf2f(in.ldt)));
  const float ar = -rbf(__expf(bf2f(in.lar)));
  const float wr2 = (ar * dt) * LOG2E, wrev = (bf2f(in.aim) * dt) * 0.15915494309189535f;
  const f2 c2 = {2.0f * (bf2f(in.cre) * dt), -2.0f * (bf2f(in.cim) * dt)};
  const int chl = x.tid >> 7, n = x.tid & 127;
  LAS unsigned* TA = (LAS unsigned*)(x.L + KG_A + chl * 48 * KG_ROW) + n;
  LAS unsigned* TB = (LAS unsigned*)(x.L + KG_B + chl * 16 * KG_ROW) + n;
  constexpr int RS = KG_ROW / 4;
  const f2 e8 = cexp_p(wr2, wrev, 8.f), e128 = cexp_p(wr2, -wrev, 128.f), e1 = cexp_p(wr2, -wrev, 1.f);
  f2 y = e8, h = cmul(c2, cexp_p(wr2, -wrev, 1024.f)), g[8], e[8];
  g[0] = c2; e[1] = e1; e[2] = cmul(e1, e1);
  TB[0] = 0x00003f80u;
#pragma unroll
  for (int i = 0; i < 8; ++i) {
    TB[(2 * i + 1) * RS] = pk(y);
    TA[(32 + i) * RS] = pk(h);
    const f2 y1 = cmul(y, e8), h1 = cmul(h, e128);
    if (i < 7) g[i + 1] = cmul(g[i], e128);
    if (i == 0) { e[3] = cmul(e[2], e[1]); e[4] = cmul(e[2], e[2]); }
    if (i == 1) { e[5] = cmul(e[4], e[1]); e[6] = cmul(e[3], e[3]); e[7] = cmul(e[4], e[3]); }
    if (i < 7) { TB[(2 * i + 2) * RS] = pk(y1); y = cmul(y1, e8); }
    h = h1;
  }
  TA[40 * RS] = pk(h);
#pragma unroll
  for (int k = 0; k < 8; ++k) {
    TA[k * RS] = pk(k ? cmul(g[0], e[k]) : g[0]);
    TA[(8 + k) * RS] = pk(k ? cmul(g[1], e[k]) : g[1]);
    if (k < 4) { TA[(16 + k) * RS] = pk(k ? cmul(g[2], e[2 * k]) : g[2]); TA[(20 + k) * RS] = pk(k ? cmul(g[3], e[2 * k]) : g[3]); }
    else { TA[(24 + 2 * (k - 4)) * RS] = pk(g[k]); TA[(25 + 2 * (k - 4)) * RS] = pk(cmul(g[k], e[4])); }
  }
}
DI void pro_kgemm(const Ctx& x) {
  const int ch = x.w >> 1, j = x.lane & 15, g = x.lane >> 4, odd = x.w & 1;
  unsigned ao = KG_A + (ch * 48 + 16 * odd + j) * KG_ROW + g * 16, a2o = KG_A + (ch * 48 + 32 + j) * KG_ROW + g * 16;
  unsigned bo = KG_B + (ch * 16 + j) * KG_ROW + g * 16;
  asm volatile("" : "+v"(ao), "+v"(a2o), "+v"(bo));
  u4 a0[8], a2[8], b[8];
#pragma unroll
  for (int ks = 0; ks < 8; ++ks) { b[ks] = *(LAS u4*)(x.L + bo + ks * 64); a0[ks] = *(LAS u4*)(x.L + ao + ks * 64); a2[ks] = *(LAS u4*)(x.L + a2o + ks * 64); }
  SB();
  f4 acc0 = {0, 0, 0, 0}, acc2 = acc0;
#pragma unroll
  for (int ks = 0; ks < 8; ++ks) { acc0 = mfma32(a0[ks], b[ks], acc0); acc2 = mfma32(a2[ks], b[ks], acc2); }
  const int pe = 128 * (g >> 1) + 4 * (g & 1), po = g < 2 ? 256 + 128 * g : 512 + 256 * (g - 2);
  const unsigned t0 = KG_T + (ch * 2056 + 8 * j + (odd ? po : pe)) * 4;
  const int s1 = odd ? (g < 2 ? 8 : 16) : 4, s2 = odd ? (g < 2 ? 16 : 512) : 8;
  *(LAS float*)(x.L + t0) = acc0[0]; *(LAS float*)(x.L + t0 + s1) = acc0[1];
  *(LAS float*)(x.L + t0 + s2) = acc0[2]; *(LAS float*)(x.L + t0 + s1 + s2) = acc0[3];
  const unsigned t2 = KG_T + (ch * 2056 + 1024 + 512 * g + 8 * j) * 4, dummy = KG_T + 2052 * 4;
#pragma unroll
  for (int r = 0; r < 4; ++r) *(LAS float*)(x.L + ((4 * g + r < 8 || (4 * g + r == 8 && j == 0)) ? t2 + 512 * r : dummy)) = acc2[r];
}
constexpr int KG_D = 0, KG_DCH = 4096 + 64;
DI void pro_expand(const Ctx& x) {
  unsigned tb = KG_T, db = KG_D + x.tid * 4;
  f2 l0 = {(float)(2047 - 2 * x.tid), (float)(2046 - 2 * x.tid)};
  asm volatile("" : "+v"(tb), "+v"(db), "+v"(l0));
#pragma unroll
  for (int i = 0; i < 4; ++i) {
    unsigned r = pk(l0 - (float)(512 * i));
    asm("" : "+v"(r));
    const unsigned a0 = tb + 4 * (int)blo(r), a1 = tb + 4 * (int)bhi(r);
#pragma unroll
    for (int ch = 0; ch < 2; ++ch)
      *(LAS unsigned*)(x.L + db + ch * KG_DCH + i * 1024) = pack2(*(LAS float*)(x.L + a0 + ch * (2056 * 4)), *(LAS float*)(x.L + a1 + ch * (2056 * 4)));
  }
}
DI void pro_take_k(const Ctx& x, u4 (&kd)[3]) {
  const int ch = x.tid >> 7, q = x.tid & 127;
  lds_t* a = x.L + KG_D + ch * KG_DCH + 32 * q;
  kd[0] = *(LAS u4*)a; kd[1] = *(LAS u4*)(a + 16); kd[2] = *(LAS u4*)(a + 32);
  const unsigned off = WS_KREV + ((2 * x.cp + ch) * 2048 + 16 * q) * 2;
  __builtin_amdgcn_raw_buffer_store_b128(kd[0], x.ws, off, 0, 0);
  __builtin_amdgcn_raw_buffer_store_b128(kd[1], x.ws, off + 16, 0, 0);
}
DI void pro_wait(const Ctx& x, unsigned flags, unsigned tag, unsigned first) {
  unsigned v = first;
  for (unsigned s = 0;; ++s) {
    const unsigned ok = __all(v == tag);
    if (x.lane == 0) ((LAS unsigned*)(x.L + LD_FLAG))[x.w] = ok;
    __syncthreads();
    const u4 r = *(LAS u4*)(x.L + LD_FLAG);
    if (r.x & r.y & r.z & r.w) break;
    if (s > SPIN_CAP) { if (x.tid == 0) spin_fail(x); break; }
    v = __builtin_amdgcn_raw_buffer_load_b32(x.ws, flags + x.tid * 64, 0, AUX_SC1);
    __syncthreads();
  }
}

DI u4 pro_peek_wave(const Ctx& x, unsigned flags) {
  u4 v;
#pragma unroll
  for (int i = 0; i < 4; ++i) v[i] = __builtin_amdgcn_raw_buffer_load_b32(x.ws, flags + (64 * i + x.lane) * 64, 0, AUX_SC1);
  return v;
}
DI void pro_wait_wave(const Ctx& x, unsigned flags, unsigned tag, u4 v) {
  for (unsigned s = 0; !__all(v.x == tag && v.y == tag && v.z == tag && v.w == tag); ++s) {
    if (s > SPIN_CAP) { spin_fail(x); break; }
    asm volatile("" ::: "memory");
    v = pro_peek_wave(x, flags);
  }
}

DI void q_wsum(const u4 (&wf)[48], f4 (&ws)[3]) {
  const u4 ones = {0x3f803f80u, 0x3f803f80u, 0x3f803f80u, 0x3f803f80u};
  ws[0] = f4{0, 0, 0, 0}; ws[1] = ws[0]; ws[2] = ws[0];
#pragma unroll
  for (int ks = 0; ks < 16; ++ks)
#pragma unroll
    for (int m = 0; m < 3; ++m) ws[m] = mfma32(wf[16 * m + ks], ones, ws[m]);
}
struct QDma { unsigned v0, v1, base, lds; int layer; unsigned char* wsp; };
DI QDma q_dma_setup(const Ctx& x) {
  const int row = x.lane >> 4, pc = x.lane & 15;
  return QDma{(unsigned)(row * 256 + ((pc ^ (row << 1)) * 16)), (unsigned)(row * 256 + ((pc ^ ((4 + row) << 1)) * 16)),
              WS_X1 + x.layer * X1_BYTES + x.rb * (512 * 256) + x.w * 4096, (unsigned)(LD_XT + x.w * 4096), x.layer, x.wsp};
}
template <int OFF> DI void dma2_sc1(rsrc_t r, const QDma& d) {
  unsigned t;
  asm volatile("s_add_u32 m0, %5, %6\n\ts_add_u32 %0, %4, %6\n\t"
               "buffer_load_dwordx4 %1, %3, %0 offen sc1 lds\n\tbuffer_load_dwordx4 %2, %3, %0 offen offset:1024 sc1 lds"
               : "=&s"(t) : "v"(d.v0), "v"(d.v1), "s"(r), "s"(d.base), "s"(d.lds), "n"(OFF) : "memory", "scc");
}
template <int OFF> DI void dma1_sc1(rsrc_t r, const QDma& d) {
  unsigned t;
  asm volatile("s_add_u32 m0, %4, %5\n\ts_add_u32 %0, %3, %5\n\tbuffer_load_dwordx4 %1, %2, %0 offen sc1 lds"
               : "=&s"(t) : "v"((OFF & 1024) ? d.v1 : d.v0), "s"(r), "s"(d.base), "s"(d.lds), "n"(OFF) : "memory", "scc");
}
struct QAddr { lds_t* lo[4]; lds_t* hi[4]; };
DI void q_frag_addr(const Ctx& x, QAddr& ba) {
  const int g = x.lane >> 4, q = (x.lane & 15) >> 2, pp = x.lane & 3, ph = x.w >> 1;
  const int r7 = 4 * (g & 1) + q;
#pragma unroll
  for (int pt = 0; pt < 4; ++pt) {
    const int lc = 8 * ph + 2 * (pt ^ (2 * (x.w & 1))) + (pp >> 1);
    unsigned o = LD_XT + (4 * g + q) * 256 + ((lc ^ (r7 << 1)) * 16) + 8 * (pp & 1), o2 = o + 65536;
    asm volatile("" : "+v"(o), "+v"(o2));
    ba.lo[pt] = x.L + o; ba.hi[pt] = x.L + o2;
  }
}
template <int KS> DI u4 q_ldb1(const QAddr& ba, int pt) {
  lds_t* p = (KS < 8 ? ba.lo[pt] : ba.hi[pt]) + (KS & 7) * 8192;
  return cat(lds_tr(p), lds_tr(p + 4096));
}
template <int KS> DI void q_ldb(const QAddr& ba, u4 (&b)[4]) {
#pragma unroll
  for (int pt = 0; pt < 4; ++pt) b[pt] = q_ldb1<KS>(ba, pt);
}
template <int N> DI void wait_vm_barrier() { asm volatile("s_waitcnt vmcnt(%0)\n\ts_barrier" :: "n"(N) : "memory"); }

DI void softmax4(const f4 (&t)[4], const float (&cs)[4], float csr, lds_t* scr_w, lds_t* scr_r, u4 (&out)[4]) {
  float mx[4], mn[4];
#pragma unroll
  for (int pt = 0; pt < 4; ++pt) {
    mx[pt] = fmax2(fmax2(fmax2(t[pt][0], t[pt][1]), t[pt][2]), t[pt][3]);
    mn[pt] = fmin2(fmin2(fmin2(t[pt][0], t[pt][1]), t[pt][2]), t[pt][3]);
  }
  const float MX = xrow4<0>(mx[0], mx[1], mx[2], mx[3]), MN = xrow4<1>(mn[0], mn[1], mn[2], mn[3]);
  *(LAS f2*)scr_w = f2{-csr * MX, csr * MN};
  f2 ep[4][2], en[4][2];
  float sp[4], sn[4];
#pragma unroll
  for (int pt = 0; pt < 4; ++pt) {
    const f2 b = *(LAS f2*)(scr_r + 128 * pt);
    const f2 t0 = {t[pt][0], t[pt][1]}, t1 = {t[pt][2], t[pt][3]};
    const f2 p0 = t0 * cs[pt] + b.x, p1 = t1 * cs[pt] + b.x, n0 = b.y - t0 * cs[pt], n1 = b.y - t1 * cs[pt];
    ep[pt][0] = f2{__builtin_amdgcn_exp2f(p0.x), __builtin_amdgcn_exp2f(p0.y)}; ep[pt][1] = f2{__builtin_amdgcn_exp2f(p1.x), __builtin_amdgcn_exp2f(p1.y)};
    en[pt][0] = f2{__builtin_amdgcn_exp2f(n0.x), __builtin_amdgcn_exp2f(n0.y)}; en[pt][1] = f2{__builtin_amdgcn_exp2f(n1.x), __builtin_amdgcn_exp2f(n1.y)};
    const f2 a = ep[pt][0] + ep[pt][1], c = en[pt][0] + en[pt][1];
    sp[pt] = a.x + a.y; sn[pt] = c.x + c.y;
  }
  const float SP = xrow4<2>(sp[0], sp[1], sp[2], sp[3]), SN = xrow4<2>(sn[0], sn[1], sn[2], sn[3]);
  *(LAS f2*)scr_w = f2{__builtin_amdgcn_rcpf(SP), __builtin_amdgcn_rcpf(SN)};
#pragma unroll
  for (int pt = 0; pt < 4; ++pt) {
    const f2 iv = *(LAS f2*)(scr_r + 128 * pt);
    out[pt] = u4{pk(ep[pt][0] * iv.x), pk(ep[pt][1] * iv.x), pk(en[pt][0] * iv.y), pk(en[pt][1] * iv.y)};
  }
}

struct QState { u4 pq[4]; u2 res[4]; u4 pk8[8], az; };
DI void o_load_partials(const Ctx& x, QState& st) {
  const int h = x.w & 1, ph = x.w >> 1, hh = 2 * x.hp + h;
  const unsigned ko = WS_KVP + x.layer * KVP_BYTES + (hh * 16 + 8 * ph) * 1024 + x.lane * 16;
#pragma unroll
  for (int i = 0; i < 8; ++i) st.pk8[i] = ld16(x.ws, ko + i * 1024);
  st.az = ld16(x.ws, WS_ZP + x.layer * ZP_BYTES + (hh * 16 + (x.lane & 15)) * 64 + (x.lane >> 4) * 16);
}

template <int KS> struct QPass1 {
  static DI void run(rsrc_t ws, const QDma& d, const QAddr& ba, const u4 (&wf)[48], u4 (&b)[4], f4 (&ak)[4], f4 (&av)[4], f4 (&ag)[2]) {
    if (KS < 15 && (KS & 1)) wait_vm_barrier<(KS < 15 ? 8 + 2 * (KS < 10 ? KS : 10) - 4 * ((KS + 1) / 2) : 0)>();
    const u4 g0 = b[0], g1 = b[1];
#pragma unroll
    for (int pt = 0; pt < 4; ++pt) {
      ak[pt] = mfma32(wf[16 + KS], b[pt], ak[pt]); av[pt] = mfma32(wf[32 + KS], b[pt], av[pt]);
      if (KS < 15) {
        b[pt] = q_ldb1<(KS < 15 ? KS + 1 : 15)>(ba, pt);
        SGB(SG_MFMA, 1); SGB(SG_DSR, 1); SGB(SG_MFMA, 1); SGB(SG_DSR, 1);
      }
      SB();
    }
    ag[0] = mfma32(g0, g0, ag[0]);
    SB();
    if (KS < 10) dma1_sc1<(3 + KS / 2) * 16384 + (KS & 1) * 2048>(ws, d);
    SB();
    ag[1] = mfma32(g1, g1, ag[1]);
    SB();
    if (KS < 10) dma1_sc1<(3 + KS / 2) * 16384 + (KS & 1) * 2048 + 1024>(ws, d);
    SB();
    QPass1<KS + 1>::run(ws, d, ba, wf, b, ak, av, ag);
  }
};
template <> struct QPass1<16> { static DI void run(rsrc_t, const QDma&, const QAddr&, const u4 (&)[48], u4 (&)[4], f4 (&)[4], f4 (&)[4], f4 (&)[2]) {} };
template <int KS> struct QPass2 {
  static DI void run(const QAddr& ba, const u4 (&wf)[48], u4 (&b)[3][4], f4 (&aq)[4]) {
    if (KS < 14) q_ldb<(KS < 14 ? KS + 2 : 15)>(ba, b[(KS + 2) % 3]);
#pragma unroll
    for (int pt = 0; pt < 4; ++pt) aq[pt] = mfma32(wf[KS], b[KS % 3][pt], aq[pt]);
    if (KS < 14) {
#pragma unroll
      for (int i = 0; i < 4; ++i) { SGB(SG_MFMA, 1); SGB(SG_DSR, 2); }
    }
    SB();
    QPass2<KS + 1>::run(ba, wf, b, aq);
  }
};
template <> struct QPass2<16> { static DI void run(const QAddr&, const u4 (&)[48], u4 (&)[3][4], f4 (&)[4]) {} };

DI void o_dma_wo(const Ctx& x, const Params& p) {
  const rsrc_t r = make_rsrc(p.wo, 512 * 512 * 2);
  const int row = x.lane >> 3, pc = x.lane & 7, cb = x.hp >> 1;
  const int b1 = (row >> 1) & 1;
  const unsigned v0 = row * 1024 + ((pc ^ (b1 << 1)) * 16), v1 = row * 1024 + ((pc ^ ((b1 | 2) << 1)) * 16) + 7168;
#pragma unroll 1
  for (int i = 4 * x.w; i < 4 * x.w + 4; ++i) dma4(r, v0, v1, v0 + 2 * 7168, v1 + 2 * 7168, i * 32768 + cb * 128, LD_XT + i * 4096);
}

DI void q_phase(const Ctx& x, const Params& p, const u4 (&wf)[48], const f4 (&wsum)[3], QState& st, const Poll& pl) {
  const QDma dm = q_dma_setup(x);
  dma2_sc1<0>(x.ws, dm); dma2_sc1<2048>(x.ws, dm); dma2_sc1<16384>(x.ws, dm); dma2_sc1<16384 + 2048>(x.ws, dm);
  dma2_sc1<32768>(x.ws, dm); dma2_sc1<32768 + 2048>(x.ws, dm);
  QAddr ba;
  q_frag_addr(x, ba);
  const int h = x.w & 1, ph = x.w >> 1, g = x.lane >> 4, cl = x.lane & 15;
  f4 ak[4], av[4], ag[2];
#pragma unroll
  for (int i = 0; i < 4; ++i) { ak[i] = f4{0, 0, 0, 0}; av[i] = f4{0, 0, 0, 0}; }
  ag[0] = f4{0, 0, 0, 0}; ag[1] = f4{0, 0, 0, 0};
  STAMP(x.wsp, 9 + 16 * x.layer);
  {
    u4 b[4];
    wait_vm_barrier<8>();
    poll_retire(pl);
    q_ldb<0>(ba, b);
    SB();
    QPass1<0>::run(x.ws, dm, ba, wf, b, ak, av, ag);
  }
  STAMP(x.wsp, 10 + 16 * x.layer);
  {
    const int r = cl & 3;
    const unsigned so = LD_STAT + 4 * (64 * ph + 32 * h + cl);
    const unsigned a0 = g == 0 ? so : LD_STAT + 1024, a1 = g == (cl >> 2) ? so + 512 : LD_STAT + 1024;
#pragma unroll
    for (int i = 0; i < 2; ++i) {
      const float dq = r == 0 ? ag[i][0] : r == 1 ? ag[i][1] : r == 2 ? ag[i][2] : ag[i][3];
      *(LAS float*)(x.L + a0 + 64 * i) = ak[i][0];
      *(LAS float*)(x.L + a1 + 64 * i) = dq;
    }
  }
  __syncthreads();
  float mean[4], rs[4], cs[4];
#pragma unroll
  for (int pt = 0; pt < 4; ++pt) {
    LAS float* S = (LAS float*)(x.L + LD_STAT) + 64 * ph + 16 * (pt ^ (2 * h)) + cl;
    mean[pt] = S[0] * (1.f / DM);
    rs[pt] = __builtin_amdgcn_rsqf(__builtin_fabsf((S[128] * (1.f / DM) + LN_EPS) - mean[pt] * mean[pt]));
    cs[pt] = rs[pt] * LOG2E;
  }
  const float csr = g == 0 ? cs[0] : g == 1 ? cs[2] : g == 2 ? cs[1] : cs[3];
  lds_t* scr_r = x.L + LD_SCR + x.w * 512 + cl * 8;
  lds_t* scr_w = scr_r + 128 * ((g >> 1) | ((g & 1) << 1));
  lds_t* pv = x.L + LD_PV + x.w * PV_WAVE;
  {
    f4 t[4];
#pragma unroll
    for (int pt = 0; pt < 4; ++pt) t[pt] = ak[pt] - mean[pt] * wsum[1];
    u4 pk4[4];
    softmax4(t, cs, csr, scr_w, scr_r, pk4);
#pragma unroll
    for (int pt = 0; pt < 4; ++pt) {
      const f4 v = (av[pt] - mean[pt] * wsum[2]) * rs[pt];
      lds_t* row = pv + (16 * pt + cl) * PV_ROW + 8 * g;
      *(LAS u2*)(row) = u2{pk4[pt].x, pk4[pt].y}; *(LAS u2*)(row + 32) = u2{pk4[pt].z, pk4[pt].w};
      *(LAS u2*)(row + 64) = u2{pack2(v[0], v[1]), pack2(v[2], v[3])};
    }
  }
  f4 kv0 = {0, 0, 0, 0}, kv1 = kv0, z0 = kv0, z1 = kv0;
  {
    const u4 ones = {0x3f803f80u, 0x3f803f80u, 0x3f803f80u, 0x3f803f80u};
    const int q = cl >> 2, pp = cl & 3;
#pragma unroll
    for (int k2 = 0; k2 < 2; ++k2) {
      lds_t* a = pv + (32 * k2 + 8 * g + q) * PV_ROW + 8 * pp;
      const u4 a0 = cat(lds_tr(a), lds_tr(a + 4 * PV_ROW));
      const u4 a1 = cat(lds_tr(a + 32), lds_tr(a + 4 * PV_ROW + 32));
      const u4 bv = cat(lds_tr(a + 64), lds_tr(a + 4 * PV_ROW + 64));
      kv0 = mfma32(a0, bv, kv0); kv1 = mfma32(a1, bv, kv1); z0 = mfma32(a0, ones, z0); z1 = mfma32(a1, ones, z1);
    }
  }
  STAMP(x.wsp, 11 + 16 * x.layer);
  {
    lds_t* mine = x.L + LD_PV + x.w * PV_WAVE + x.lane * 16;
    if (ph == 1) { *(LAS f4*)(mine) = kv0; *(LAS f4*)(mine + 1024) = kv1; *(LAS f4*)(mine + 2048) = z0; *(LAS f4*)(mine + 3072) = z1; }
    __syncthreads();
    if (ph == 0) {
      lds_t* o = x.L + LD_PV + (x.w + 2) * PV_WAVE + x.lane * 16;
      kv0 += *(LAS f4*)(o); kv1 += *(LAS f4*)(o + 1024); z0 += *(LAS f4*)(o + 2048); z1 += *(LAS f4*)(o + 3072);
      const int hr = (2 * x.hp + h) * 16 + x.rb;
      st16(x.ws, WS_KVP + x.layer * KVP_BYTES + hr * 1024 + x.lane * 16,
           u4{pack2(kv0[0], kv0[1]), pack2(kv0[2], kv0[3]), pack2(kv1[0], kv1[1]), pack2(kv1[2], kv1[3])});
      if (cl == 0) st16(x.ws, WS_ZP + x.layer * ZP_BYTES + hr * 64 + g * 16,
                        u4{pack2(z0[0], z0[1]), pack2(z0[2], z0[3]), pack2(z1[0], z1[1]), pack2(z1[2], z1[3])});
    }
  }
  STAMP(x.wsp, 12 + 16 * x.layer);
  {
    f4 aq[4];
#pragma unroll
    for (int i = 0; i < 4; ++i) aq[i] = f4{0, 0, 0, 0};
    u4 b[3][4];
    q_ldb<0>(ba, b[0]); q_ldb<1>(ba, b[1]);
    SB();
    QPass2<0>::run(ba, wf, b, aq);
    {
      const int cb = x.hp >> 1, q = cl >> 2, pos = 64 * (x.hp & 1) + 16 * x.w + 4 * (cl & 3);
      const int row = 64 * cb + 4 * g + q;
      unsigned ro = LD_XT + row * 256 + (((pos >> 3) ^ ((row & 7) << 1)) * 16) + (pos & 4) * 2;
      asm volatile("" : "+v"(ro));
#pragma unroll
      for (int ct = 0; ct < 4; ++ct) st.res[ct] = lds_tr(x.L + ro + ct * 4096);
    }
    o_load_partials(x, st);
    f4 t[4];
#pragma unroll
    for (int pt = 0; pt < 4; ++pt) t[pt] = aq[pt] - mean[pt] * wsum[0];
    softmax4(t, cs, csr, scr_w, scr_r, st.pq);
  }
  STAMP(x.wsp, 13 + 16 * x.layer);
}

DI void o_compute(const Ctx& x, QState& st) {
  const int h = x.w & 1, ph = x.w >> 1, g = x.lane >> 4, cl = x.lane & 15, hh = 2 * x.hp + h;
  STAMP(x.wsp, 14 + 16 * x.layer);
  const unsigned one = g == (cl >> 2) ? ((cl & 1) ? 0x3f800000u : 0x3f80u) : 0u;
  const unsigned e0 = (cl & 2) ? 0u : one, e1 = (cl & 2) ? one : 0u;
  const u4 sel0 = {e0, e1, 0u, 0u}, sel1 = {0u, 0u, e0, e1};
  f4 k0, k1, den[4];
  for (unsigned spin = 0;; ++spin) {
    k0 = f4{0, 0, 0, 0}; k1 = k0;
#pragma unroll
    for (int i = 0; i < 8; ++i) { k0 = mfma32(sel0, st.pk8[i], k0); k1 = mfma32(sel1, st.pk8[i], k1); }
    const u2 ones = {0x3f803f80u, 0x3f803f80u};
    f4 dp[4];
#pragma unroll
    for (int pt = 0; pt < 4; ++pt) dp[pt] = mfma32(st.az, st.pq[pt], f4{0, 0, 0, 0});
#pragma unroll
    for (int pt = 0; pt < 4; ++pt) den[pt] = mfma16(ones, u2{pack2(dp[pt][0], dp[pt][1]), pack2(dp[pt][2], dp[pt][3])}, f4{1e-6f, 1e-6f, 1e-6f, 1e-6f});
    const float chk = (k0[0] + k1[0]) + den[0][0];
    if (!__any(chk != chk)) break;
    if (spin > 4096u) { spin_fail(x); break; }
    o_load_partials(x, st);
  }
  lds_t* mine = x.L + LD_PV + 8192 + x.w * 2048 + x.lane * 16;
  *(LAS f4*)(mine) = k0; *(LAS f4*)(mine + 1024) = k1;
  __syncthreads();
  STAMP(x.wsp, 15 + 16 * x.layer);
  lds_t* o = x.L + LD_PV + 8192 + (x.w ^ 2) * 2048 + x.lane * 16;
  k0 += *(LAS f4*)(o); k1 += *(LAS f4*)(o + 1024);
  const u4 akv = {pack2(k0[0], k0[1]), pack2(k0[2], k0[3]), pack2(k1[0], k1[1]), pack2(k1[2], k1[3])};
  f4 num[4];
#pragma unroll
  for (int pt = 0; pt < 4; ++pt) num[pt] = mfma32(akv, st.pq[pt], f4{0, 0, 0, 0});
  const unsigned oo = WS_OB + x.layer * OB_BYTES + ((2 * x.rb + ph) * 32 + hh) * 2048 + (16 * ((g & 1) ^ (2 * h)) + cl) * 32 + 16 * (g >> 1);
#pragma unroll
  for (int pt = 0; pt < 4; pt += 2) {
    const f4 va = num[pt] * __builtin_amdgcn_rcpf(den[pt][0]), vb = num[pt + 1] * __builtin_amdgcn_rcpf(den[pt + 1][0]);
    const auto s0 = __builtin_amdgcn_permlane16_swap(pack2(va[0], va[1]), pack2(vb[0], vb[1]), false, false);
    const auto s1 = __builtin_amdgcn_permlane16_swap(pack2(va[2], va[3]), pack2(vb[2], vb[3]), false, false);
    st16(x.ws, oo ^ (pt * 512), u4{s0[0], s1[0], s0[1], s1[1]});
  }
}

struct OAddr { lds_t* p[4]; };
template <int KS> DI void o_ldw(const OAddr& wb, u4 (&b)[4]) {
#pragma unroll
  for (int ct = 0; ct < 4; ++ct) b[ct] = cat(lds_tr(wb.p[ct] + KS * 4096), lds_tr(wb.p[ct] + KS * 4096 + 512));
}
DI unsigned umax3(unsigned a, unsigned b, unsigned c) { const unsigned m = a > b ? a : b; return m > c ? m : c; }
template <int KS> struct OGemm {
  static DI void run(const OAddr& wb, const u4 (&of)[16], u4 (&b)[3][4], f4 (&acc)[4], unsigned& top) {
    if (KS < 14) o_ldw<(KS < 14 ? KS + 2 : 15)>(wb, b[(KS + 2) % 3]);
#pragma unroll
    for (int ct = 0; ct < 4; ++ct) acc[ct] = mfma32(b[KS % 3][ct], of[KS], acc[ct]);
    top = umax3(umax3(top, of[KS].x, of[KS].y), of[KS].z, of[KS].w);
    if (KS < 14) {
#pragma unroll
      for (int i = 0; i < 4; ++i) { SGB(SG_MFMA, 1); SGB(SG_DSR, 2); }
    }
    SB();
    OGemm<KS + 1>::run(wb, of, b, acc, top);
  }
};
template <> struct OGemm<16> { static DI void run(const OAddr&, const u4 (&)[16], u4 (&)[3][4], f4 (&)[4], unsigned&) {} };

DI void o_gemm(const Ctx& x, const Params& p, const QState& st, u4 (&kd)[3]) {
  const int g = x.lane >> 4, cl = x.lane & 15, pb = 2 * x.rb + (x.hp & 1), cb = x.hp >> 1;
  const unsigned oo = WS_OB + x.layer * OB_BYTES + ((pb * 32 + (g >> 1)) * 64 + 16 * x.w + cl) * 32 + 16 * (g & 1);
  OAddr wb;
  {
    const int q = cl >> 2, pp = cl & 3, f = ((q >> 1) & 1) | ((g & 1) << 1);
#pragma unroll
    for (int ct = 0; ct < 4; ++ct) wb.p[ct] = x.L + LD_XT + (8 * g + q) * 128 + (((2 * ct + (pp >> 1)) ^ (f << 1)) * 16) + 8 * (pp & 1);
  }
  f4 acc[4];
  o_dma_wo(x, p);
  for (unsigned spin = 0;; ++spin) {
    u4 of[16];
#pragma unroll
    for (int ks = 0; ks < 16; ++ks) of[ks] = ld16(x.ws, oo + ks * 4096);
    if (spin == 0) wait_vm_barrier<16>();
    STAMP(x.wsp, 17 + 16 * x.layer);
#pragma unroll
    for (int i = 0; i < 4; ++i) acc[i] = f4{0, 0, 0, 0};
    u4 b[3][4];
    o_ldw<0>(wb, b[0]); o_ldw<1>(wb, b[1]);
    SB();
    unsigned top = 0;
    OGemm<0>::run(wb, of, b, acc, top);
    if (!__any(top == ~0u)) break;
    if (spin > 4096u) { spin_fail(x); break; }
  }
  STAMP(x.wsp, 18 + 16 * x.layer);
  if (x.layer == 0) conv_load_k(x, kd);
  float s = 0.f, q = 0.f;
#pragma unroll
  for (int ct = 0; ct < 4; ++ct) {
    const unsigned a = pack2(blo(st.res[ct].x) + acc[ct][0], bhi(st.res[ct].x) + acc[ct][1]);
    const unsigned b2 = pack2(blo(st.res[ct].y) + acc[ct][2], bhi(st.res[ct].y) + acc[ct][3]);
    const float v0 = blo(a), v1 = bhi(a), v2 = blo(b2), v3 = bhi(b2);
    s += (v0 + v1) + (v2 + v3); q += (v0 * v0 + v1 * v1) + (v2 * v2 + v3 * v3);
    *(LAS u2*)(x.L + LD_OST + (16 * x.w + cl) * 144 + (16 * ct + 4 * g) * 2) = u2{a, b2};
  }
  if (x.layer == 0) {
    const f2 sq = xrow_sum2(s, q);
    if (g < 2) {
      GAS float* d = (GAS float*)(p.ws + WS_ST + 2048 * 8) + g * 2048 + 64 * pb + 16 * x.w + cl;
      __hip_atomic_fetch_add(d, g ? sq.y : sq.x, RLX_AGENT);
    }
  }
  __syncthreads();
  if (x.layer == 0) {
    const int qq = cl >> 2, pp = cl & 3;
#pragma unroll
    for (int j = 0; j < 2; ++j) {
      lds_t* a = x.L + LD_OST + (8 * (g + 4 * j) + qq) * 144 + (16 * x.w + 4 * pp) * 2;
      const u4 v = cat(lds_tr(a), lds_tr(a + 4 * 144));
      st16(x.ws, WS_XT + XT_BYTES + ((64 * cb + 16 * x.w + cl) * XT_STRIDE + 64 * pb + 8 * (g + 4 * j)) * 2, v);
    }
  } else {
#pragma unroll
    for (int i = 0; i < 2; ++i) {
      const int k = i * 256 + x.tid, pos = k >> 3, q8 = k & 7;
      *(GAS u4*)(p.out + (64 * pb + pos) * DM + 64 * cb + 8 * q8) = *(LAS u4*)(x.L + LD_OST + pos * 144 + q8 * 16);
    }
  }
  STAMP(x.wsp, 19 + 16 * x.layer);
}

DI Ctx make_ctx(const Params& p, unsigned char* smem) {
  Ctx x;
  x.L = (lds_t*)smem; x.ws = make_rsrc(p.ws, WS_END); x.cw = (GAS unsigned*)(p.ws + WS_CTRL); x.wsp = p.ws;
  x.tid = threadIdx.x; x.lane = x.tid & 63; x.w = __builtin_amdgcn_readfirstlane(x.tid >> 6); x.c = blockIdx.x;
  x.cp = (x.c & 7) * 32 + (x.c >> 3);
  x.rb = 2 * (x.c & 7) + (x.c >> 7); x.hp = (x.c >> 3) & 15; x.layer = 0;
  return x;
}

DI Ctx fresh(const Ctx& x, int layer) { Ctx y = x; int t = x.tid; asm volatile("" : "+v"(t)); y.tid = t; y.lane = t & 63; y.layer = layer; return y; }

__global__ void __launch_bounds__(256, 1) k_main(Params p) {
  extern __shared__ __attribute__((aligned(16))) unsigned char smem[];
  const unsigned char* code0;
  asm volatile("s_getpc_b64 %0" : "=s"(code0));
  const Ctx x0 = make_ctx(p, smem);
  STAMP(p.ws, 0);
  u4 wf[48];
  f4 wsum[3];
  const float dsk = bf2f(p.dsk[2 * x0.cp + (x0.w >> 1)]);
  unsigned tag;
  {
    const Ctx x = fresh(x0, 0);
    u4 kd[3], a[32];
    ProIn pin;
    pro_load(x, p, pin);
    tag = pin.epoch + 1u;
    pro_publish_x(x, pin);
    SB();
    pro_fold_w(x, 6 * x.c + x.w, pin.w[0]);
    if (x.w < 2) { pro_fold_w(x, 6 * x.c + 4 + x.w, pin.w[1]); asm volatile("s_waitcnt vmcnt(2)" ::: "memory"); }
    else asm volatile("s_waitcnt vmcnt(1)" ::: "memory");
    __syncthreads();
    if (x.tid == 0) __builtin_amdgcn_raw_buffer_store_b32(tag, x.ws, WS_FLAG + x.c * 64, 0, AUX_SC1);
    pro_init(x);
    STAMP(p.ws, 1);
    pro_tables(x, pin);
    drain();
    __syncthreads();
    if (x.tid == 0) __builtin_amdgcn_raw_buffer_store_b32(tag, x.ws, WS_FLAG2 + x.c * 64, 0, AUX_SC1);
    STAMP(p.ws, 2);
    const unsigned fl = __builtin_amdgcn_raw_buffer_load_b32(x.ws, WS_FLAG + x.tid * 64, 0, AUX_SC1);
    pro_kgemm(x);
    pro_wait(x, WS_FLAG, tag, fl);
    STAMP(p.ws, 3);
    ConvIn in;
    conv_load_in(x, 0, in);
    SB();
    pro_expand(x);
    __syncthreads();
    pro_take_k(x, kd);
    conv_build_k(x, kd);
    STAMP(p.ws, 4);
    conv_build_u(x, in);
    const u4 fl2 = pro_peek_wave(x, WS_FLAG2);
    __syncthreads();
    STAMP(p.ws, 5);
    pro_wait_wave(x, WS_FLAG2, tag, fl2);
    STAMP3(x, 36);
    Warm wm;
    pro_warm(x, p, code0, wm);
    if (x.w & 1) conv_wave<1, true>(x, dsk, a); else conv_wave<0, true>(x, dsk, a);
    pro_warm_retire(wm);
    a2a_post(fresh(x0, 0), 0);
    poison_o(fresh(x0, 0), 1);
    q_take_w(fresh(x0, 0), wf);
    q_wsum(wf, wsum);
  }
#pragma unroll 1
  for (int layer = 0; layer < 2; ++layer) {
    Ctx x = fresh(x0, layer);
    Poll pl;
    a2a_wait(x, 2 * layer, pl);
    STAMP(p.ws, 8 + 16 * layer);
    QState st;
    x = fresh(x0, layer);
    q_phase(x, p, wf, wsum, st, pl);
    x = fresh(x0, layer);
    o_compute(x, st);
    STAMP(p.ws, 16 + 16 * layer);
    x = fresh(x0, layer);
    u4 kd[3];
    o_gemm(x, p, st, kd);
    if (layer == 0) {
      u4 a[32];
      conv_build_k(x, kd);
      a2a_signal(x, 1);
      x = fresh(x0, 1);
      if (x.w & 1) conv_load_a<1>(x, a); else conv_load_a<0>(x, a);
      SB();
      a2a_wait(x, 1, pl);
      STAMP(p.ws, 20);
      ConvIn in;
      conv_load_in(x, 1, in);
      conv_build_u(x, in);
      poll_retire(pl);
      __syncthreads();
      STAMP(p.ws, 21);
      if (x.w & 1) conv_wave<1, false>(x, dsk, a); else conv_wave<0, false>(x, dsk, a);
      a2a_post(fresh(x0, 1), 2);
    }
  }
  if (x0.c == 0 && x0.tid == 0) __builtin_amdgcn_raw_buffer_store_b32(tag, x0.ws, WS_EPOCH, 0, AUX_SC1);
}

extern "C" void kernel_launch(void* const* d_in, const int* in_sizes, int n_in, void* d_out, int out_size, void* d_ws, size_t ws_size,
                              hipStream_t stream) {
  Params p;
  p.u = (const unsigned short*)d_in[0]; p.lar = (const unsigned short*)d_in[1]; p.aim = (const unsigned short*)d_in[2];
  p.cre = (const unsigned short*)d_in[3]; p.cim = (const unsigned short*)d_in[4]; p.ldt = (const unsigned short*)d_in[5];
  p.dsk = (const unsigned short*)d_in[6]; p.wqkv = (const unsigned short*)d_in[8]; p.wphi = (const unsigned short*)d_in[9];
  p.wo = (const unsigned short*)d_in[10]; p.out = (unsigned short*)d_out; p.ws = (unsigned char*)d_ws;
  (void)hipFuncSetAttribute((const void*)k_main, hipFuncAttributeMaxDynamicSharedMemorySize, LDS_BYTES);
  k_main<<<256, 256, LDS_BYTES, stream>>>(p);
}
```

```cpp
#include <hip/hip_runtime.h>
#include <hip/hip_bf16.h>
#include <stdint.h>

#define MK_STAMP 0
#define STAMP(ws, i) do { } while (0)
#define STAMP2(x, i) do { } while (0)
#define STAMP3(x, i) do { } while (0)
typedef short s4 __attribute__((ext_vector_type(4)));
typedef short s8 __attribute__((ext_vector_type(8)));
typedef float f2 __attribute__((ext_vector_type(2)));
typedef float f4 __attribute__((ext_vector_type(4)));
typedef unsigned u2 __attribute__((ext_vector_type(2)));
typedef unsigned u4 __attribute__((ext_vector_type(4)));
typedef __bf16 bf2 __attribute__((ext_vector_type(2)));
#define GAS __attribute__((address_space(1)))
#define LAS __attribute__((address_space(3)))
#define DI __device__ __forceinline__
#define RLX_AGENT __ATOMIC_RELAXED, __HIP_MEMORY_SCOPE_AGENT
typedef GAS unsigned gu32;
typedef LAS unsigned char lds_t;
typedef __amdgpu_buffer_rsrc_t rsrc_t;

constexpr int SEQ = 2048, DM = 512, NST = 128, NH = 32;
constexpr float LOG2E = 1.4426950408889634f;
constexpr float LN_EPS = 1e-5f;
constexpr int AUX_SC1 = 16;

constexpr int XT_STRIDE = 2112;
constexpr unsigned WS_CTRL = 0;
constexpr unsigned WS_KREV = 262144;
constexpr unsigned WS_WFRAG = WS_KREV + 512u * 2048 * 2;
constexpr unsigned WS_XT = WS_WFRAG + 32u * 3 * 16 * 1024;
constexpr unsigned XT_BYTES = 512u * XT_STRIDE * 2;
constexpr unsigned WS_ST = WS_XT + 2 * XT_BYTES;
constexpr unsigned WS_X1 = WS_ST + 2u * 2048 * 8;
constexpr unsigned X1_BYTES = 2048u * 512 * 2;
constexpr unsigned WS_KVP = WS_X1 + 2 * X1_BYTES;
constexpr unsigned KVP_BYTES = 32u * 16 * 1024;
constexpr unsigned WS_ZP = WS_KVP + 2 * KVP_BYTES;
constexpr unsigned ZP_BYTES = 32u * 16 * 64;
constexpr unsigned WS_OB = WS_ZP + 2 * ZP_BYTES;
constexpr unsigned OB_BYTES = 2048u * 512 * 2;
constexpr unsigned WS_END = WS_OB + 2 * OB_BYTES;
constexpr int CW_TMO = 0;
constexpr int NCW_A2A = 3 * 8 * 16;
constexpr int CW_STRIDE = 64;
constexpr int CWI_A2A = 1, CWI_END = CWI_A2A + NCW_A2A;
constexpr unsigned WS_EPOCH = 131072, WS_FLAG = WS_EPOCH + 256, WS_FLAG2 = WS_FLAG + 256 * 64;
static_assert(CWI_END * CW_STRIDE * 4 <= WS_EPOCH && WS_FLAG2 + 256 * 64 <= WS_KREV, "control block");

struct Params {
  const unsigned short* u;
  const unsigned short* lar;
  const unsigned short* aim;
  const unsigned short* cre;
  const unsigned short* cim;
  const unsigned short* ldt;
  const unsigned short* dsk;
  const unsigned short* wqkv;
  const unsigned short* wphi;
  const unsigned short* wo;
  unsigned short* out;
  unsigned char* ws;
};

DI float bf2f(unsigned short b) { return __builtin_bit_cast(float, (unsigned)b << 16); }
DI float blo(unsigned x) { return __builtin_bit_cast(float, x << 16); }
DI float bhi(unsigned x) { return __builtin_bit_cast(float, x & 0xffff0000u); }
DI unsigned pack2(float a, float b) { bf2 v = {(__bf16)a, (__bf16)b}; return __builtin_bit_cast(unsigned, v); }
DI float rbf(float a) { return blo(pack2(a, a)); }
DI f4 mfma32(u4 a, u4 b, f4 c) { return __builtin_amdgcn_mfma_f32_16x16x32_bf16(__builtin_bit_cast(s8, a), __builtin_bit_cast(s8, b), c, 0, 0, 0); }
DI f4 mfma16(u2 a, u2 b, f4 c) { return __builtin_amdgcn_mfma_f32_16x16x16bf16_1k(__builtin_bit_cast(s4, a), __builtin_bit_cast(s4, b), c, 0, 0, 0); }
DI u2 lds_tr(lds_t* p) { return __builtin_bit_cast(u2, __builtin_amdgcn_ds_read_tr16_b64_v4i16((LAS s4*)p)); }
DI u4 cat(u2 a, u2 b) { return u4{a.x, a.y, b.x, b.y}; }
DI rsrc_t make_rsrc(const void* p, unsigned bytes) { return __builtin_amdgcn_make_buffer_rsrc((void*)p, 0, (int)bytes, 0x00020000); }
DI u4 ld16(rsrc_t r, unsigned off) { return __builtin_amdgcn_raw_buffer_load_b128(r, off, 0, AUX_SC1); }
DI void st16(rsrc_t r, unsigned off, u4 v) { __builtin_amdgcn_raw_buffer_store_b128(v, r, off, 0, AUX_SC1); }
DI void drain() { asm volatile("s_waitcnt vmcnt(0)" ::: "memory"); }
DI void dma4_sc1(rsrc_t r, unsigned v0, unsigned v1, unsigned soff, unsigned lds) {
  asm volatile("s_mov_b32 m0, %4\n\ts_nop 0\n\t"
               "buffer_load_dwordx4 %0, %2, %3 offen sc1 lds\n\tbuffer_load_dwordx4 %1, %2, %3 offen offset:1024 sc1 lds\n\t"
               "buffer_load_dwordx4 %0, %2, %3 offen offset:2048 sc1 lds\n\tbuffer_load_dwordx4 %1, %2, %3 offen offset:3072 sc1 lds"
               :: "v"(v0), "v"(v1), "s"(r), "s"(soff), "s"(lds) : "memory");
}
DI void dma4(rsrc_t r, unsigned v0, unsigned v1, unsigned v2, unsigned v3, unsigned soff, unsigned lds) {
  asm volatile("s_mov_b32 m0, %6\n\ts_nop 0\n\t"
               "buffer_load_dwordx4 %0, %4, %5 offen lds\n\tbuffer_load_dwordx4 %1, %4, %5 offen offset:1024 lds\n\t"
               "buffer_load_dwordx4 %2, %4, %5 offen offset:2048 lds\n\tbuffer_load_dwordx4 %3, %4, %5 offen offset:3072 lds"
               :: "v"(v0), "v"(v1), "v"(v2), "v"(v3), "s"(r), "s"(soff), "s"(lds) : "memory");
}
#define SB() __builtin_amdgcn_sched_barrier(0)
#define SGB(mask, n) __builtin_amdgcn_sched_group_barrier(mask, n, 0)
constexpr int SG_MFMA = 0x8, SG_DSR = 0x100;
template <int CTRL> DI float dpp_f(float v) { return __builtin_bit_cast(float, __builtin_amdgcn_mov_dpp(__builtin_bit_cast(int, v), CTRL, 0xf, 0xf, true)); }
DI float row_sum(float v) { v += dpp_f<0x128>(v); v += dpp_f<0x124>(v); v += dpp_f<0x122>(v); v += dpp_f<0x121>(v); return v; }
DI f2 swap32(float a, float b) { auto r = __builtin_amdgcn_permlane32_swap(__builtin_bit_cast(unsigned, a), __builtin_bit_cast(unsigned, b), false, false);
  const unsigned r0 = r[0], r1 = r[1];
  return f2{__builtin_bit_cast(float, r0), __builtin_bit_cast(float, r1)}; }
DI f2 swap16(float a, float b) { auto r = __builtin_amdgcn_permlane16_swap(__builtin_bit_cast(unsigned, a), __builtin_bit_cast(unsigned, b), false, false);
  const unsigned r0 = r[0], r1 = r[1];
  return f2{__builtin_bit_cast(float, r0), __builtin_bit_cast(float, r1)}; }
DI float fmax2(float a, float b) { return __builtin_fmaxf(a, b); }
DI float fmin2(float a, float b) { return __builtin_fminf(a, b); }
template <int OP> DI float op2(float a, float b) { return OP == 0 ? fmax2(a, b) : OP == 1 ? fmin2(a, b) : a + b; }
template <int OP> DI float xrow4(float a, float b, float c, float d) {
  f2 s = swap32(a, b); const float ab = op2<OP>(s.x, s.y);
  s = swap32(c, d); const float cd = op2<OP>(s.x, s.y);
  s = swap16(ab, cd); return op2<OP>(s.x, s.y);
}
DI f2 xrow_max2(float a, float b) { f2 s = swap32(a, b); float c = fmaxf(s.x, s.y); s = swap16(c, c); c = fmaxf(s.x, s.y); return swap32(c, c); }
DI f2 xrow_sum2(float a, float b) { f2 s = swap32(a, b); float c = s.x + s.y; s = swap16(c, c); c = s.x + s.y; return swap32(c, c); }
DI float wave_sum(float v) { v = row_sum(v); f2 s = swap16(v, v); v = s.x + s.y; s = swap32(v, v); return s.x + s.y; }

constexpr int LD_XT = 0;
constexpr int LD_KC = 65536;
constexpr int KC_COPY = 4160, KC_CH = 4 * KC_COPY;
constexpr int LD_U = LD_KC + 2 * KC_CH;
constexpr int U_CH = 4608;
constexpr int LD_X = LD_U + 2 * U_CH;
constexpr int LD_EX = LD_KC;
constexpr int LD_CONV_END = LD_X + 8192;
constexpr int LD_WA = 0, LD_WB = LD_CONV_END;
constexpr int LD_MISC = 131072;
constexpr int LD_PV = LD_MISC;
constexpr int PV_ROW = 104, PV_WAVE = 64 * PV_ROW;
constexpr int LD_STAT = LD_PV + 4 * PV_WAVE;
constexpr int LD_SCR = LD_STAT + 1024 + 256;
constexpr int LD_FLAG = LD_SCR + 2048;
constexpr int LD_OST = LD_PV;
constexpr int LDS_BYTES = 163840;
static_assert(LD_CONV_END <= LD_MISC && LD_FLAG + 64 <= LDS_BYTES, "LDS map");

struct Ctx {
  lds_t* L; rsrc_t ws; GAS unsigned* cw; unsigned char* wsp;
  int tid, lane, w, c, cp, rb, hp, layer;
};

DI void spin_fail(const Ctx& x) { __hip_atomic_store(x.cw + CW_TMO, 1u, RLX_AGENT); }
constexpr unsigned SPIN_CAP = 1u << 18;
DI void a2a_post(const Ctx& x, int e) {
  __syncthreads();
  if (x.tid < 8) __hip_atomic_fetch_add(x.cw + (CWI_A2A + (e * 8 + x.tid) * 16 + (x.c >> 4)) * CW_STRIDE, 1u, RLX_AGENT);
}
DI void a2a_signal(const Ctx& x, int e) { drain(); a2a_post(x, e); }
struct Poll { unsigned r0, r1, r2, r3; };
DI void poll16(const Ctx& x, unsigned word, Poll& q) {
  if (x.w == 0) {
    const rsrc_t rc = make_rsrc(x.wsp + WS_CTRL, CWI_END * CW_STRIDE * 4);
    const unsigned off = word * (CW_STRIDE * 4);
    unsigned n;
    asm volatile(
        "s_mov_b32 %4, 0\n\t"
        "buffer_load_dword %0, %5, %6, 0 offen sc1\n\ts_sleep 2\n\t"
        "buffer_load_dword %1, %5, %6, 0 offen sc1\n\ts_sleep 2\n\t"
        "buffer_load_dword %2, %5, %6, 0 offen sc1\n\ts_sleep 2\n\t"
        "buffer_load_dword %3, %5, %6, 0 offen sc1\n"
        "1:\n\t"
        "s_waitcnt vmcnt(3)\n\tv_cmp_gt_u32 vcc, 16, %0\n\ts_cbranch_vccz 2f\n\tbuffer_load_dword %0, %5, %6, 0 offen sc1\n\t"
        "s_waitcnt vmcnt(3)\n\tv_cmp_gt_u32 vcc, 16, %1\n\ts_cbranch_vccz 2f\n\tbuffer_load_dword %1, %5, %6, 0 offen sc1\n\t"
        "s_waitcnt vmcnt(3)\n\tv_cmp_gt_u32 vcc, 16, %2\n\ts_cbranch_vccz 2f\n\tbuffer_load_dword %2, %5, %6, 0 offen sc1\n\t"
        "s_waitcnt vmcnt(3)\n\tv_cmp_gt_u32 vcc, 16, %3\n\ts_cbranch_vccz 2f\n\tbuffer_load_dword %3, %5, %6, 0 offen sc1\n\t"
        "s_add_u32 %4, %4, 1\n\ts_cmp_lt_u32 %4, 0x20000\n\ts_cbranch_scc1 1b\n"
        "2:"
        : "=&v"(q.r0), "=&v"(q.r1), "=&v"(q.r2), "=&v"(q.r3), "=&s"(n) : "v"(off), "s"(rc) : "vcc", "scc", "memory");
    if (n >= 0x20000u) spin_fail(x);
  }
  __syncthreads();
}
DI void poll_retire(const Poll& q) { asm volatile("" :: "v"(q.r0), "v"(q.r1), "v"(q.r2), "v"(q.r3)); }
DI void a2a_wait(const Ctx& x, int e, Poll& q) { poll16(x, CWI_A2A + (e * 8 + (x.c & 7)) * 16 + (x.lane & 15), q); }

DI void conv_load_k(const Ctx& x, u4 (&kd)[3]) {
  const int ch = x.tid >> 7, q = x.tid & 127;
  const unsigned off = WS_KREV + ((2 * x.cp + ch) * 2048 + 16 * q) * 2;
  kd[0] = __builtin_amdgcn_raw_buffer_load_b128(x.ws, off, 0, 0);
  kd[1] = __builtin_amdgcn_raw_buffer_load_b128(x.ws, off + 16, 0, 0);
  kd[2] = __builtin_amdgcn_raw_buffer_load_b128(x.ws, off + 32, 0, 0);
}
DI void conv_build_k(const Ctx& x, const u4 (&kd)[3]) {
  const int ch = x.tid >> 7, q = x.tid & 127;
  const unsigned d8 = q == 127 ? 0u : kd[2].x, d9 = q == 127 ? 0u : kd[2].y;
  const unsigned d[10] = {kd[0].x, kd[0].y, kd[0].z, kd[0].w, kd[1].x, kd[1].y, kd[1].z, kd[1].w, d8, d9};
  lds_t* b = x.L + LD_KC + ch * KC_CH + 32 * q;
  *(LAS u4*)(b) = u4{d[0], d[1], d[2], d[3]}; *(LAS u4*)(b + 16) = u4{d[4], d[5], d[6], d[7]};
  *(LAS u4*)(b + 2 * KC_COPY) = u4{d[1], d[2], d[3], d[4]}; *(LAS u4*)(b + 2 * KC_COPY + 16) = u4{d[5], d[6], d[7], d[8]};
  unsigned e[9];
#pragma unroll
  for (int i = 0; i < 9; ++i) e[i] = __builtin_amdgcn_alignbit(d[i + 1], d[i], 16);
  *(LAS u4*)(b + KC_COPY) = u4{e[0], e[1], e[2], e[3]}; *(LAS u4*)(b + KC_COPY + 16) = u4{e[4], e[5], e[6], e[7]};
  *(LAS u4*)(b + 3 * KC_COPY) = u4{e[1], e[2], e[3], e[4]}; *(LAS u4*)(b + 3 * KC_COPY + 16) = u4{e[5], e[6], e[7], e[8]};
  if (x.tid < 16) {
    *(LAS u4*)(x.L + LD_KC + (x.tid >> 1) * KC_COPY + 4096 + (x.tid & 1) * 16) = u4{0, 0, 0, 0};
  }
  if (x.tid >= 64 && x.tid < 128) {
    const int i = x.tid - 64;
    *(LAS u4*)(x.L + LD_U + (i >> 5) * U_CH + (i & 31) * 16) = u4{0, 0, 0, 0};
  }
}
template <int PAR> struct Mu { static constexpr int v(int k) { return PAR ? (k == 0 ? 1 : k == 1 ? 2 : k == 2 ? 5 : 6) : (k == 0 ? 0 : k == 1 ? 3 : k == 2 ? 4 : 7); } };
template <int PAR> DI lds_t* conv_a_base(const Ctx& x) {
  const int b = x.lane & 15, kq = x.lane >> 4, ch = x.w >> 1;
  const int s0 = 2047 - 16 * (kq >> 1) - b + 8 * (kq & 1), cc = s0 & 3;
  unsigned bo = LD_KC + ch * KC_CH + cc * KC_COPY + 2 * (s0 - cc) - 64 * 63;
  asm volatile("" : "+v"(bo));
  return x.L + bo;
}
template <int PAR> DI u4 conv_a_frag(lds_t* base, int k, int i) {
  const int m = 8 * Mu<PAR>::v(k) + i;
  const u2 lo = *(volatile LAS u2*)(base + 64 * (63 - m)), hi = *(volatile LAS u2*)(base + 64 * (63 - m) + 8);
  return cat(lo, hi);
}
template <int PAR> DI void conv_load_a(const Ctx& x, u4 (&a)[32]) {
  lds_t* base = conv_a_base<PAR>(x);
#pragma unroll
  for (int k = 0; k < 4; ++k)
#pragma unroll
    for (int i = 0; i < 8; ++i) a[8 * k + i] = conv_a_frag<PAR>(base, k, i);
}
struct WDma { unsigned lane16, soff, lo, hi; };
DI WDma q_dma_w_setup(const Ctx& x) {
  const unsigned lo = x.w < 3 ? LD_WA + x.w * 24576 : LD_WB + 8192, hi = x.w < 2 ? lo : x.w == 2 ? LD_WB - 16384 : lo;
  return WDma{(unsigned)x.lane * 16, WS_WFRAG + x.hp * (96 * 1024) + x.w * 24576, lo, hi};
}
template <int I> DI void q_dma_w1(const Ctx& x, const WDma& d) {
  unsigned t;
  asm volatile("s_add_u32 m0, %4, %5\n\ts_add_u32 %0, %3, %5\n\tbuffer_load_dwordx4 %1, %2, %0 offen lds"
               : "=&s"(t) : "v"(d.lane16), "s"(x.ws), "s"(d.soff), "s"(I < 16 ? d.lo : d.hi), "n"(I * 1024) : "memory", "scc");
}
DI void q_take_w(const Ctx& x, u4 (&wf)[48]) {
  const int h = x.w & 1;
  const unsigned a0 = LD_WA + h * 49152 + x.lane * 16, a1 = (h ? LD_WB - 16384 : LD_WA) + x.lane * 16;
#pragma unroll
  for (int i = 0; i < 48; ++i)
    asm volatile("ds_read_b128 %0, %1 offset:%2" : "=a"(wf[i]) : "v"(i < 16 ? a0 : a1), "n"(i * 1024));
  asm volatile("s_waitcnt lgkmcnt(0)" ::: "memory");
  SB();
}
template <int TAU, int I> DI void conv_dma_w(const Ctx& x, const WDma& d) { if ((I & 1) && TAU < 6) { q_dma_w1<(TAU < 6 ? 4 * TAU + (I >> 1) : 0)>(x, d); SB(); } }
template <int PAR, bool FIRST> DI void conv_mfma(const Ctx& x, u4 (&a)[32], f4 (&acc)[8]) {
  const WDma wd = q_dma_w_setup(x);
  const int al = x.lane & 15, kq = x.lane >> 4, ch = x.w >> 1;
  unsigned bo = LD_U + ch * U_CH + 2 * (256 + 16 * al - 16 * (kq >> 1) + 8 * (kq & 1)) - 64 * 7;
  asm volatile("" : "+v"(bo));
  lds_t* base = x.L + bo;
  constexpr int NT = 8 - Mu<PAR>::v(0);
  u4 b[8];
  if (FIRST) {
    STAMP3(x, 37);
    lds_t* abase = conv_a_base<PAR>(x);
#pragma unroll
    for (int i = 0; i < 8; ++i) { b[i] = *(LAS u4*)(base + 64 * (7 - i)); a[i] = conv_a_frag<PAR>(abase, 0, i); }
    SB();
#pragma unroll
    for (int k = 0; k < 4; ++k) {
#pragma unroll
      for (int i = 0; i < 8; ++i) {
        acc[Mu<PAR>::v(k)] = mfma32(a[8 * k + i], b[i], acc[Mu<PAR>::v(k)]);
        if (k < 3) { a[8 * (k + 1) + i] = conv_a_frag<PAR>(abase, k + 1, i); SGB(SG_MFMA, 1); SGB(SG_DSR, 2); }
        else { b[i] = *(LAS u4*)(base + 64 * (8 - i + 7)); SGB(SG_MFMA, 1); SGB(SG_DSR, 1); }
        SB();
      }
      switch (k) {
        case 0: q_dma_w1<0>(x, wd); break; case 1: q_dma_w1<1>(x, wd); break;
        case 2: q_dma_w1<2>(x, wd); break; default: q_dma_w1<3>(x, wd); break;
      }
      SB();
    }
    STAMP3(x, 38);
  } else {
#pragma unroll
    for (int i = 0; i < 8; ++i) b[i] = *(LAS u4*)(base + 64 * (7 - i));
    SB();
  }
#pragma unroll
  for (int tau = FIRST ? 1 : 0; tau < 8; ++tau) {
    if (FIRST && tau == 4) STAMP3(x, 39);
    if (tau < NT) {
      int ng = 0;
#pragma unroll
      for (int k = 0; k < 4; ++k) ng += (Mu<PAR>::v(k) + tau <= 7);
#pragma unroll
      for (int i = 0; i < 8; ++i) {
#pragma unroll
        for (int k = 0; k < 4; ++k)
          if (Mu<PAR>::v(k) + tau <= 7) acc[Mu<PAR>::v(k) + tau] = mfma32(a[8 * k + i], b[i], acc[Mu<PAR>::v(k) + tau]);
        if (tau + 1 < NT) {
          b[i] = *(LAS u4*)(base + 64 * (8 * (tau + 1) - i + 7));
          if (ng == 4) SGB(SG_MFMA, 4); else if (ng == 3) SGB(SG_MFMA, 3); else if (ng == 2) SGB(SG_MFMA, 2); else SGB(SG_MFMA, 1);
          SGB(SG_DSR, 1);
        }
        SB();
        if (FIRST) {
          switch (8 * tau + i) {
#define W_(t, j) case 8 * t + j: conv_dma_w<t, j>(x, wd); break;
#define WT_(t) W_(t, 1) W_(t, 3) W_(t, 5) W_(t, 7)
            WT_(1) WT_(2) WT_(3) WT_(4) WT_(5)
#undef WT_
#undef W_
            default: break;
          }
        }
      }
    }
  }
}

struct ConvIn { u4 x0, x1, s[4]; };
DI void conv_load_in(const Ctx& x, int layer, ConvIn& in) {
  const unsigned xo = WS_XT + layer * XT_BYTES + (2 * x.cp * XT_STRIDE + 8 * x.tid) * 2;
  const unsigned so = WS_ST + layer * 2048 * 8 + x.tid * 32;
  in.s[0] = ld16(x.ws, so); in.s[1] = ld16(x.ws, so + 16); in.s[2] = ld16(x.ws, so + 8192); in.s[3] = ld16(x.ws, so + 8192 + 16);
  in.x0 = ld16(x.ws, xo); in.x1 = ld16(x.ws, xo + XT_STRIDE * 2);
}
DI f2 unpk(unsigned v) { return f2{blo(v), bhi(v)}; }
DI unsigned pk(f2 v) { return pack2(v.x, v.y); }
DI void conv_build_u(const Ctx& x, const ConvIn& in) {
  const f4 sa = __builtin_bit_cast(f4, in.s[0]), sb = __builtin_bit_cast(f4, in.s[1]), qa = __builtin_bit_cast(f4, in.s[2]), qb = __builtin_bit_cast(f4, in.s[3]);
  const f2 sv[4] = {f2{sa[0], sa[1]}, f2{sa[2], sa[3]}, f2{sb[0], sb[1]}, f2{sb[2], sb[3]}};
  const f2 qv[4] = {f2{qa[0], qa[1]}, f2{qa[2], qa[3]}, f2{qb[0], qb[1]}, f2{qb[2], qb[3]}};
  u4 u0, u1;
#pragma unroll
  for (int i = 0; i < 4; ++i) {
    const f2 m = sv[i] * (1.f / DM);
    const f2 ve = (qv[i] * (1.f / DM) + LN_EPS) - m * m;
    const f2 r = {__builtin_amdgcn_rsqf(__builtin_fabsf(ve.x)), __builtin_amdgcn_rsqf(__builtin_fabsf(ve.y))};
    const f2 nmr = -m * r;
    u0[i] = pk(unpk(in.x0[i]) * r + nmr);
    u1[i] = pk(unpk(in.x1[i]) * r + nmr);
  }
  *(LAS u4*)(x.L + LD_U + 512 + 16 * x.tid) = u0; *(LAS u4*)(x.L + LD_U + U_CH + 512 + 16 * x.tid) = u1;
  *(LAS u4*)(x.L + LD_X + 16 * x.tid) = in.x0; *(LAS u4*)(x.L + LD_X + 4096 + 16 * x.tid) = in.x1;
}
DI void poison_o(const Ctx& x, int layer) {
#pragma unroll
  for (int i = 0; i < 2; ++i) st16(x.ws, WS_OB + layer * OB_BYTES + (x.c * 2 + i) * 4096 + x.tid * 16, u4{~0u, ~0u, ~0u, ~0u});
}
template <int PAR, bool LOADW> DI void conv_wave(const Ctx& x, float dsk, u4 (&a)[32]) {
  const int ch = x.w >> 1;
  f4 acc[8];
#pragma unroll
  for (int t = 0; t < 8; ++t) acc[t] = f4{0, 0, 0, 0};
  conv_mfma<PAR, LOADW>(x, a, acc);
  STAMP(x.wsp, 6 + 16 * x.layer);
  const int al = x.lane & 15, g = x.lane >> 4;
  unsigned eo = LD_EX + x.lane * 16, uo = LD_U + ch * U_CH + 512 + 2 * (1024 * PAR + 16 * al + 4 * g);
  asm volatile("" : "+v"(eo), "+v"(uo));
  lds_t* mine = x.L + eo + x.w * 4096;
  lds_t* other = x.L + eo + (x.w ^ 1) * 4096;
#pragma unroll
  for (int t = 0; t < 4; ++t) *(LAS f4*)(mine + t * 1024) = acc[PAR ? t : 4 + t];
  f2 v[4][2];
#pragma unroll
  for (int t = 0; t < 4; ++t) {
    const u2 uu = *(LAS u2*)(x.L + uo + 512 * t), xx = *(LAS u2*)(x.L + uo + (LD_X - LD_U - 512) + ch * (4096 - U_CH) + 512 * t);
    const f4 own = acc[PAR ? 4 + t : t];
    v[t][0] = unpk(uu.x) * dsk + (unpk(xx.x) + f2{own[0], own[1]});
    v[t][1] = unpk(uu.y) * dsk + (unpk(xx.y) + f2{own[2], own[3]});
  }
  __syncthreads();
  STAMP(x.wsp, 7 + 16 * x.layer);
  const unsigned so = WS_X1 + x.layer * X1_BYTES + (((8 * PAR + 2 * (g & 1) + (al >> 3)) * 512 + 2 * x.cp + ch) * 128 + 16 * (al & 7) + 8 * (g >> 1)) * 2;
#pragma unroll
  for (int t = 0; t < 4; t += 2) {
    const f4 o0 = *(LAS f4*)(other + t * 1024), o1 = *(LAS f4*)(other + (t + 1) * 1024);
    const unsigned a0 = pk(v[t][0] + f2{o0[0], o0[1]}), a1 = pk(v[t][1] + f2{o0[2], o0[3]});
    const unsigned b0 = pk(v[t + 1][0] + f2{o1[0], o1[1]}), b1 = pk(v[t + 1][1] + f2{o1[2], o1[3]});
    const auto s0 = __builtin_amdgcn_permlane16_swap(a0, b0, false, false), s1 = __builtin_amdgcn_permlane16_swap(a1, b1, false, false);
    st16(x.ws, so + t * (2 * 512 * 256), u4{s0[0], s1[0], s0[1], s1[1]});
  }
  if (LOADW) poison_o(x, 0);
  drain();
}

DI f2 cmul(f2 a, f2 b) { return f2{a.x, a.x} * b + f2{a.y, a.y} * f2{-b.y, b.x}; }
DI f2 cexp_p(float wr2, float wrev, float p) {
  const float e = __builtin_amdgcn_exp2f(wr2 * p);
  const float a = __builtin_amdgcn_fractf(wrev * p);
  return f2{e * __builtin_amdgcn_cosf(a), e * __builtin_amdgcn_sinf(a)};
}
constexpr int KG_ROW = 528;
constexpr int KG_A = 0;
constexpr int KG_X = 2 * 48 * KG_ROW;
constexpr int KG_T = LD_CONV_END;
constexpr int KG_B = KG_T + 2 * 2056 * 4;
static_assert(KG_X + 64 * 136 <= LD_KC && KG_B + 2 * 16 * KG_ROW <= LDS_BYTES, "prologue LDS map");

struct ProW { u2 a0, a1; unsigned short b[4]; };
struct ProIn { unsigned short lar, aim, cre, cim, ldt; u4 xt[2], xr[2]; ProW w[2]; unsigned epoch; };
DI void pro_load_w(const Params& p, int F, int lane, ProW& w) {
  const int fh = F / 48, fm = (F % 48) >> 4, fks = F & 15, row = lane & 15, kq = lane >> 4;
  w.a0 = *(const u2*)(p.wqkv + (32 * fks + row) * 1536 + fm * 512 + 16 * fh + 4 * kq);
  w.a1 = *(const u2*)(p.wqkv + (32 * fks + 16 + row) * 1536 + fm * 512 + 16 * fh + 4 * kq);
  const unsigned short* q = p.wphi + fh * 256 + (4 * kq) * 16 + row;
#pragma unroll
  for (int j = 0; j < 4; ++j) w.b[j] = q[16 * j];
}
DI void pro_fold_w(const Ctx& x, int F, const ProW& w) {
  const bool fold = ((F % 48) >> 4) < 2;
  const int dd = (x.lane & 15) - 4 * (x.lane >> 4);
  const u2 wb = fold ? u2{w.b[0] | ((unsigned)w.b[1] << 16), w.b[2] | ((unsigned)w.b[3] << 16)}
                     : u2{(dd == 0 ? 0x3f80u : 0u) | (dd == 1 ? 0x3f800000u : 0u), (dd == 2 ? 0x3f80u : 0u) | (dd == 3 ? 0x3f800000u : 0u)};
  const f4 d0 = mfma16(w.a0, wb, f4{0, 0, 0, 0}), d1 = mfma16(w.a1, wb, f4{0, 0, 0, 0});
  float e[8];
#pragma unroll
  for (int r = 0; r < 4; ++r) {
    const float b0 = dpp_f<0x150>(d0[r]), b1 = dpp_f<0x150>(d1[r]);
    e[r] = d0[r] - (fold ? b0 : 0.f); e[4 + r] = d1[r] - (fold ? b1 : 0.f);
  }
  u4 o = {pack2(e[0], e[1]), pack2(e[2], e[3]), pack2(e[4], e[5]), pack2(e[6], e[7])};
  if (fold && (x.lane & 15) == 0) o = u4{0x3f803f80u, 0x3f803f80u, 0x3f803f80u, 0x3f803f80u};
  st16(x.ws, WS_WFRAG + F * 1024 + x.lane * 16, o);
}
DI void pro_load(const Ctx& x, const Params& p, ProIn& in) {
#pragma unroll
  for (int i = 0; i < 2; ++i) {
    in.xt[i] = *(const u4*)(p.u + (64 * (x.c >> 3) + 32 * i + (x.tid >> 3)) * DM + 64 * (x.c & 7) + 8 * (x.tid & 7));
    in.xr[i] = *(const u4*)(p.u + (8 * x.c + 2 * x.w + i) * DM + 8 * x.lane);
  }
  in.epoch = __builtin_amdgcn_raw_buffer_load_b32(x.ws, WS_EPOCH, 0, AUX_SC1);
  const int d = 2 * x.cp + (x.tid >> 7), n = x.tid & 127;
  in.lar = p.lar[d * NST + n]; in.aim = p.aim[d * NST + n]; in.cre = p.cre[d * NST + n]; in.cim = p.cim[d * NST + n]; in.ldt = p.ldt[d];
  pro_load_w(p, 6 * x.c + x.w, x.lane, in.w[0]);
  pro_load_w(p, 6 * x.c + 4 + (x.w & 1), x.lane, in.w[1]);
  SB();
}
constexpr int CODE_SKIP = 5, CODE_KB = 40;
struct Warm { unsigned a, b, c; };
DI void pro_warm(const Ctx& x, const Params& p, const unsigned char* code, Warm& wm) {
  const int j = x.c >> 3;
  static_assert(CODE_KB * 8 <= 32 * 64 / 4 * 4, "one line per lane");
  wm.a = wm.b = wm.c = 0;
  if (x.w == 3) {
    const int line = j * (CODE_KB * 8 / 32) + x.lane;
    if (x.lane < CODE_KB * 8 / 32) wm.a = *(const GAS unsigned*)(code + CODE_SKIP * 1024 + line * 128);
    wm.b = *(const GAS unsigned*)((const unsigned char*)p.wo + (j * 128 + x.lane) * 128);
    wm.c = *(const GAS unsigned*)((const unsigned char*)p.wo + (j * 128 + 64 + x.lane) * 128);
  }
}
DI void pro_warm_retire(const Warm& wm) { asm volatile("" :: "v"(wm.a), "v"(wm.b), "v"(wm.c)); }
DI unsigned pro_touch(const Ctx& x, const Params& p) {
  unsigned v = 0;
  constexpr int NWS = (WS_END + 65535) / 65536, PER = (NWS + 32 + 31) / 32;
  const int i = (x.c >> 3) * PER + x.lane;
  if (x.w == 3 && x.lane < PER && i < NWS + 32)
    v = *(const GAS unsigned*)(i < NWS ? (const unsigned char*)p.ws + (size_t)i * 65536 : (const unsigned char*)p.out + (size_t)(i - NWS) * 65536);
  return v;
}
DI void pro_init(const Ctx& x) {
  const rsrc_t ws = x.ws;
  if (x.c < 2) { const int i = x.c * 256 + x.tid; if (i < CWI_END) __builtin_amdgcn_raw_buffer_store_b32(0u, ws, i * (CW_STRIDE * 4), 0, AUX_SC1); }
  if (x.c >= 8 && x.c < 12) st16(ws, WS_ST + 2048 * 8 + ((x.c - 8) * 256 + x.tid) * 16, u4{0, 0, 0, 0});
  static_assert(WS_ZP == WS_KVP + 2 * KVP_BYTES && (2 * KVP_BYTES + 2 * ZP_BYTES) == 256 * 272 * 16, "poisoned block");
  st16(ws, WS_KVP + (x.c * 272 + x.tid) * 16, u4{~0u, ~0u, ~0u, ~0u});
  if (x.tid < 16) st16(ws, WS_KVP + (x.c * 272 + 256 + x.tid) * 16, u4{~0u, ~0u, ~0u, ~0u});
}
DI void pro_publish_x(const Ctx& x, const ProIn& in) {
#pragma unroll
  for (int i = 0; i < 2; ++i) {
    lds_t* a = x.L + KG_X + (32 * i + (x.tid >> 3)) * 136 + (x.tid & 7) * 16;
    *(LAS u2*)a = u2{in.xt[i].x, in.xt[i].y}; *(LAS u2*)(a + 8) = u2{in.xt[i].z, in.xt[i].w};
  }
  {
    f2 a[2], q[2];
#pragma unroll
    for (int i = 0; i < 2; ++i) {
      a[i] = unpk(in.xr[i][0]); q[i] = a[i] * a[i];
#pragma unroll
      for (int k = 1; k < 4; ++k) { const f2 v = unpk(in.xr[i][k]); a[i] += v; q[i] += v * v; }
    }
    const float t = xrow4<2>(row_sum(a[0].x + a[0].y), row_sum(q[0].x + q[0].y), row_sum(a[1].x + a[1].y), row_sum(q[1].x + q[1].y));
    const int g = x.lane >> 4;
    if ((x.lane & 15) == 0)
      __builtin_amdgcn_raw_buffer_store_b32(__builtin_bit_cast(unsigned, t), x.ws, WS_ST + (g >> 1) * 8192 + (8 * x.c + 2 * x.w + (g & 1)) * 4, 0, AUX_SC1);
  }
  __syncthreads();
  const int g = x.lane >> 4, cl = x.lane & 15;
#pragma unroll
  for (int j = 0; j < 2; ++j) {
    lds_t* a = x.L + KG_X + (8 * (g + 4 * j) + (cl >> 2)) * 136 + (16 * x.w + 4 * (cl & 3)) * 2;
    st16(x.ws, WS_XT + ((64 * (x.c & 7) + 16 * x.w + cl) * XT_STRIDE + 64 * (x.c >> 3) + 8 * (g + 4 * j)) * 2, cat(lds_tr(a), lds_tr(a + 4 * 136)));
  }
}
DI void pro_tables(const Ctx& x, const ProIn& in) {
  const float dt = rbf(__expf(bf2f(in.ldt)));
  const float ar = -rbf(__expf(bf2f(in.lar)));
  const float wr2 = (ar * dt) * LOG2E, wrev = (bf2f(in.aim) * dt) * 0.15915494309189535f;
  const f2 c2 = {2.0f * (bf2f(in.cre) * dt), -2.0f * (bf2f(in.cim) * dt)};
  const int chl = x.tid >> 7, n = x.tid & 127;
  LAS unsigned* TA = (LAS unsigned*)(x.L + KG_A + chl * 48 * KG_ROW) + n;
  LAS unsigned* TB = (LAS unsigned*)(x.L + KG_B + chl * 16 * KG_ROW) + n;
  constexpr int RS = KG_ROW / 4;
  const f2 e8 = cexp_p(wr2, wrev, 8.f), e128 = cexp_p(wr2, -wrev, 128.f), e1 = cexp_p(wr2, -wrev, 1.f);
  f2 y = e8, h = cmul(c2, cexp_p(wr2, -wrev, 1024.f)), g[8], e[8];
  g[0] = c2; e[1] = e1; e[2] = cmul(e1, e1);
  TB[0] = 0x00003f80u;
#pragma unroll
  for (int i = 0; i < 8; ++i) {
    TB[(2 * i + 1) * RS] = pk(y);
    TA[(32 + i) * RS] = pk(h);
    const f2 y1 = cmul(y, e8), h1 = cmul(h, e128);
    if (i < 7) g[i + 1] = cmul(g[i], e128);
    if (i == 0) { e[3] = cmul(e[2], e[1]); e[4] = cmul(e[2], e[2]); }
    if (i == 1) { e[5] = cmul(e[4], e[1]); e[6] = cmul(e[3], e[3]); e[7] = cmul(e[4], e[3]); }
    if (i < 7) { TB[(2 * i + 2) * RS] = pk(y1); y = cmul(y1, e8); }
    h = h1;
  }
  TA[40 * RS] = pk(h);
#pragma unroll
  for (int k = 0; k < 8; ++k) {
    TA[k * RS] = pk(k ? cmul(g[0], e[k]) : g[0]);
    TA[(8 + k) * RS] = pk(k ? cmul(g[1], e[k]) : g[1]);
    if (k < 4) { TA[(16 + k) * RS] = pk(k ? cmul(g[2], e[2 * k]) : g[2]); TA[(20 + k) * RS] = pk(k ? cmul(g[3], e[2 * k]) : g[3]); }
    else { TA[(24 + 2 * (k - 4)) * RS] = pk(g[k]); TA[(25 + 2 * (k - 4)) * RS] = pk(cmul(g[k], e[4])); }
  }
}
DI void pro_kgemm(const Ctx& x) {
  const int ch = x.w >> 1, j = x.lane & 15, g = x.lane >> 4, odd = x.w & 1;
  unsigned ao = KG_A + (ch * 48 + 16 * odd + j) * KG_ROW + g * 16, a2o = KG_A + (ch * 48 + 32 + j) * KG_ROW + g * 16;
  unsigned bo = KG_B + (ch * 16 + j) * KG_ROW + g * 16;
  asm volatile("" : "+v"(ao), "+v"(a2o), "+v"(bo));
  u4 a0[8], a2[8], b[8];
#pragma unroll
  for (int ks = 0; ks < 8; ++ks) { b[ks] = *(LAS u4*)(x.L + bo + ks * 64); a0[ks] = *(LAS u4*)(x.L + ao + ks * 64); a2[ks] = *(LAS u4*)(x.L + a2o + ks * 64); }
  SB();
  f4 acc0 = {0, 0, 0, 0}, acc2 = acc0;
#pragma unroll
  for (int ks = 0; ks < 8; ++ks) { acc0 = mfma32(a0[ks], b[ks], acc0); acc2 = mfma32(a2[ks], b[ks], acc2); }
  const int pe = 128 * (g >> 1) + 4 * (g & 1), po = g < 2 ? 256 + 128 * g : 512 + 256 * (g - 2);
  const unsigned t0 = KG_T + (ch * 2056 + 8 * j + (odd ? po : pe)) * 4;
  const int s1 = odd ? (g < 2 ? 8 : 16) : 4, s2 = odd ? (g < 2 ? 16 : 512) : 8;
  *(LAS float*)(x.L + t0) = acc0[0]; *(LAS float*)(x.L + t0 + s1) = acc0[1];
  *(LAS float*)(x.L + t0 + s2) = acc0[2]; *(LAS float*)(x.L + t0 + s1 + s2) = acc0[3];
  const unsigned t2 = KG_T + (ch * 2056 + 1024 + 512 * g + 8 * j) * 4, dummy = KG_T + 2052 * 4;
#pragma unroll
  for (int r = 0; r < 4; ++r) *(LAS float*)(x.L + ((4 * g + r < 8 || (4 * g + r == 8 && j == 0)) ? t2 + 512 * r : dummy)) = acc2[r];
}
constexpr int KG_D = 0, KG_DCH = 4096 + 64;
DI void pro_expand(const Ctx& x) {
  unsigned tb = KG_T, db = KG_D + x.tid * 4;
  f2 l0 = {(float)(2047 - 2 * x.tid), (float)(2046 - 2 * x.tid)};
  asm volatile("" : "+v"(tb), "+v"(db), "+v"(l0));
#pragma unroll
  for (int i = 0; i < 4; ++i) {
    unsigned r = pk(l0 - (float)(512 * i));
    asm("" : "+v"(r));
    const unsigned a0 = tb + 4 * (int)blo(r), a1 = tb + 4 * (int)bhi(r);
#pragma unroll
    for (int ch = 0; ch < 2; ++ch)
      *(LAS unsigned*)(x.L + db + ch * KG_DCH + i * 1024) = pack2(*(LAS float*)(x.L + a0 + ch * (2056 * 4)), *(LAS float*)(x.L + a1 + ch * (2056 * 4)));
  }
}
DI void pro_take_k(const Ctx& x, u4 (&kd)[3]) {
  const int ch = x.tid >> 7, q = x.tid & 127;
  lds_t* a = x.L + KG_D + ch * KG_DCH + 32 * q;
  kd[0] = *(LAS u4*)a; kd[1] = *(LAS u4*)(a + 16); kd[2] = *(LAS u4*)(a + 32);
  const unsigned off = WS_KREV + ((2 * x.cp + ch) * 2048 + 16 * q) * 2;
  __builtin_amdgcn_raw_buffer_store_b128(kd[0], x.ws, off, 0, 0);
  __builtin_amdgcn_raw_buffer_store_b128(kd[1], x.ws, off + 16, 0, 0);
}
DI void pro_wait(const Ctx& x, unsigned flags, unsigned tag, unsigned first) {
  unsigned v = first;
  for (unsigned s = 0;; ++s) {
    const unsigned ok = __all(v == tag);
    if (x.lane == 0) ((LAS unsigned*)(x.L + LD_FLAG))[x.w] = ok;
    __syncthreads();
    const u4 r = *(LAS u4*)(x.L + LD_FLAG);
    if (r.x & r.y & r.z & r.w) break;
    if (s > SPIN_CAP) { if (x.tid == 0) spin_fail(x); break; }
    v = __builtin_amdgcn_raw_buffer_load_b32(x.ws, flags + x.tid * 64, 0, AUX_SC1);
    __syncthreads();
  }
}

DI u4 pro_peek_wave(const Ctx& x, unsigned flags) {
  u4 v;
#pragma unroll
  for (int i = 0; i < 4; ++i) v[i] = __builtin_amdgcn_raw_buffer_load_b32(x.ws, flags + (64 * i + x.lane) * 64, 0, AUX_SC1);
  return v;
}
DI void pro_wait_wave(const Ctx& x, unsigned flags, unsigned tag, u4 v) {
  for (unsigned s = 0; !__all(v.x == tag && v.y == tag && v.z == tag && v.w == tag); ++s) {
    if (s > SPIN_CAP) { spin_fail(x); break; }
    asm volatile("" ::: "memory");
    v = pro_peek_wave(x, flags);
  }
}

DI void q_wsum(const u4 (&wf)[48], f4 (&ws)[3]) {
  const u4 ones = {0x3f803f80u, 0x3f803f80u, 0x3f803f80u, 0x3f803f80u};
  ws[0] = f4{0, 0, 0, 0}; ws[1] = ws[0]; ws[2] = ws[0];
#pragma unroll
  for (int ks = 0; ks < 16; ++ks)
#pragma unroll
    for (int m = 0; m < 3; ++m) ws[m] = mfma32(wf[16 * m + ks], ones, ws[m]);
}
struct QDma { unsigned v0, v1, base, lds; int layer; unsigned char* wsp; };
DI QDma q_dma_setup(const Ctx& x) {
  const int row = x.lane >> 4, pc = x.lane & 15;
  return QDma{(unsigned)(row * 256 + ((pc ^ (row << 1)) * 16)), (unsigned)(row * 256 + ((pc ^ ((4 + row) << 1)) * 16)),
              WS_X1 + x.layer * X1_BYTES + x.rb * (512 * 256) + x.w * 4096, (unsigned)(LD_XT + x.w * 4096), x.layer, x.wsp};
}
template <int OFF> DI void dma2_sc1(rsrc_t r, const QDma& d) {
  unsigned t;
  asm volatile("s_add_u32 m0, %5, %6\n\ts_add_u32 %0, %4, %6\n\t"
               "buffer_load_dwordx4 %1, %3, %0 offen sc1 lds\n\tbuffer_load_dwordx4 %2, %3, %0 offen offset:1024 sc1 lds"
               : "=&s"(t) : "v"(d.v0), "v"(d.v1), "s"(r), "s"(d.base), "s"(d.lds), "n"(OFF) : "memory", "scc");
}
template <int OFF> DI void dma1_sc1(rsrc_t r, const QDma& d) {
  unsigned t;
  asm volatile("s_add_u32 m0, %4, %5\n\ts_add_u32 %0, %3, %5\n\tbuffer_load_dwordx4 %1, %2, %0 offen sc1 lds"
               : "=&s"(t) : "v"((OFF & 1024) ? d.v1 : d.v0), "s"(r), "s"(d.base), "s"(d.lds), "n"(OFF) : "memory", "scc");
}
struct QAddr { lds_t* lo[4]; lds_t* hi[4]; };
DI void q_frag_addr(const Ctx& x, QAddr& ba) {
  const int g = x.lane >> 4, q = (x.lane & 15) >> 2, pp = x.lane & 3, ph = x.w >> 1;
  const int r7 = 4 * (g & 1) + q;
#pragma unroll
  for (int pt = 0; pt < 4; ++pt) {
    const int lc = 8 * ph + 2 * (pt ^ (2 * (x.w & 1))) + (pp >> 1);
    unsigned o = LD_XT + (4 * g + q) * 256 + ((lc ^ (r7 << 1)) * 16) + 8 * (pp & 1), o2 = o + 65536;
    asm volatile("" : "+v"(o), "+v"(o2));
    ba.lo[pt] = x.L + o; ba.hi[pt] = x.L + o2;
  }
}
template <int KS> DI u4 q_ldb1(const QAddr& ba, int pt) {
  lds_t* p = (KS < 8 ? ba.lo[pt] : ba.hi[pt]) + (KS & 7) * 8192;
  return cat(lds_tr(p), lds_tr(p + 4096));
}
template <int KS> DI void q_ldb(const QAddr& ba, u4 (&b)[4]) {
#pragma unroll
  for (int pt = 0; pt < 4; ++pt) b[pt] = q_ldb1<KS>(ba, pt);
}
template <int N> DI void wait_vm_barrier() { asm volatile("s_waitcnt vmcnt(%0)\n\ts_barrier" :: "n"(N) : "memory"); }

DI void softmax4(const f4 (&t)[4], const float (&cs)[4], float csr, lds_t* scr_w, lds_t* scr_r, u4 (&out)[4]) {
  float mx[4], mn[4];
#pragma unroll
  for (int pt = 0; pt < 4; ++pt) {
    mx[pt] = fmax2(fmax2(fmax2(t[pt][0], t[pt][1]), t[pt][2]), t[pt][3]);
    mn[pt] = fmin2(fmin2(fmin2(t[pt][0], t[pt][1]), t[pt][2]), t[pt][3]);
  }
  const float MX = xrow4<0>(mx[0], mx[1], mx[2], mx[3]), MN = xrow4<1>(mn[0], mn[1], mn[2], mn[3]);
  *(LAS f2*)scr_w = f2{-csr * MX, csr * MN};
  f2 ep[4][2], en[4][2];
  float sp[4], sn[4];
#pragma unroll
  for (int pt = 0; pt < 4; ++pt) {
    const f2 b = *(LAS f2*)(scr_r + 128 * pt);
    const f2 t0 = {t[pt][0], t[pt][1]}, t1 = {t[pt][2], t[pt][3]};
    const f2 p0 = t0 * cs[pt] + b.x, p1 = t1 * cs[pt] + b.x, n0 = b.y - t0 * cs[pt], n1 = b.y - t1 * cs[pt];
    ep[pt][0] = f2{__builtin_amdgcn_exp2f(p0.x), __builtin_amdgcn_exp2f(p0.y)}; ep[pt][1] = f2{__builtin_amdgcn_exp2f(p1.x), __builtin_amdgcn_exp2f(p1.y)};
    en[pt][0] = f2{__builtin_amdgcn_exp2f(n0.x), __builtin_amdgcn_exp2f(n0.y)}; en[pt][1] = f2{__builtin_amdgcn_exp2f(n1.x), __builtin_amdgcn_exp2f(n1.y)};
    const f2 a = ep[pt][0] + ep[pt][1], c = en[pt][0] + en[pt][1];
    sp[pt] = a.x + a.y; sn[pt] = c.x + c.y;
  }
  const float SP = xrow4<2>(sp[0], sp[1], sp[2], sp[3]), SN = xrow4<2>(sn[0], sn[1], sn[2], sn[3]);
  *(LAS f2*)scr_w = f2{__builtin_amdgcn_rcpf(SP), __builtin_amdgcn_rcpf(SN)};
#pragma unroll
  for (int pt = 0; pt < 4; ++pt) {
    const f2 iv = *(LAS f2*)(scr_r + 128 * pt);
    out[pt] = u4{pk(ep[pt][0] * iv.x), pk(ep[pt][1] * iv.x), pk(en[pt][0] * iv.y), pk(en[pt][1] * iv.y)};
  }
}

struct QState { u4 pq[4]; u2 res[4]; u4 pk8[8], az; };
DI void o_load_partials(const Ctx& x, QState& st) {
  const int h = x.w & 1, ph = x.w >> 1, hh = 2 * x.hp + h;
  const unsigned ko = WS_KVP + x.layer * KVP_BYTES + (hh * 16 + 8 * ph) * 1024 + x.lane * 16;
#pragma unroll
  for (int i = 0; i < 8; ++i) st.pk8[i] = ld16(x.ws, ko + i * 1024);
  st.az = ld16(x.ws, WS_ZP + x.layer * ZP_BYTES + (hh * 16 + (x.lane & 15)) * 64 + (x.lane >> 4) * 16);
}

template <int KS> struct QPass1 {
  static DI void run(rsrc_t ws, const QDma& d, const QAddr& ba, const u4 (&wf)[48], u4 (&b)[4], f4 (&ak)[4], f4 (&av)[4], f4 (&ag)[2]) {
    if (KS < 15 && (KS & 1)) wait_vm_barrier<(KS < 15 ? 8 + 2 * (KS < 10 ? KS : 10) - 4 * ((KS + 1) / 2) : 0)>();
    const u4 g0 = b[0], g1 = b[1];
#pragma unroll
    for (int pt = 0; pt < 4; ++pt) {
      ak[pt] = mfma32(wf[16 + KS], b[pt], ak[pt]); av[pt] = mfma32(wf[32 + KS], b[pt], av[pt]);
      if (KS < 15) {
        b[pt] = q_ldb1<(KS < 15 ? KS + 1 : 15)>(ba, pt);
        SGB(SG_MFMA, 1); SGB(SG_DSR, 1); SGB(SG_MFMA, 1); SGB(SG_DSR, 1);
      }
      SB();
    }
    ag[0] = mfma32(g0, g0, ag[0]);
    SB();
    if (KS < 10) dma1_sc1<(3 + KS / 2) * 16384 + (KS & 1) * 2048>(ws, d);
    SB();
    ag[1] = mfma32(g1, g1, ag[1]);
    SB();
    if (KS < 10) dma1_sc1<(3 + KS / 2) * 16384 + (KS & 1) * 2048 + 1024>(ws, d);
    SB();
    QPass1<KS + 1>::run(ws, d, ba, wf, b, ak, av, ag);
  }
};
template <> struct QPass1<16> { static DI void run(rsrc_t, const QDma&, const QAddr&, const u4 (&)[48], u4 (&)[4], f4 (&)[4], f4 (&)[4], f4 (&)[2]) {} };
template <int KS> struct QPass2 {
  static DI void run(const QAddr& ba, const u4 (&wf)[48], u4 (&b)[3][4], f4 (&aq)[4]) {
    if (KS < 14) q_ldb<(KS < 14 ? KS + 2 : 15)>(ba, b[(KS + 2) % 3]);
#pragma unroll
    for (int pt = 0; pt < 4; ++pt) aq[pt] = mfma32(wf[KS], b[KS % 3][pt], aq[pt]);
    if (KS < 14) {
#pragma unroll
      for (int i = 0; i < 4; ++i) { SGB(SG_MFMA, 1); SGB(SG_DSR, 2); }
    }
    SB();
    QPass2<KS + 1>::run(ba, wf, b, aq);
  }
};
template <> struct QPass2<16> { static DI void run(const QAddr&, const u4 (&)[48], u4 (&)[3][4], f4 (&)[4]) {} };

DI void o_dma_wo(const Ctx& x, const Params& p) {
  const rsrc_t r = make_rsrc(p.wo, 512 * 512 * 2);
  const int row = x.lane >> 3, pc = x.lane & 7, cb = x.hp >> 1;
  const int b1 = (row >> 1) & 1;
  const unsigned v0 = row * 1024 + ((pc ^ (b1 << 1)) * 16), v1 = row * 1024 + ((pc ^ ((b1 | 2) << 1)) * 16) + 7168;
#pragma unroll 1
  for (int i = 4 * x.w; i < 4 * x.w + 4; ++i) dma4(r, v0, v1, v0 + 2 * 7168, v1 + 2 * 7168, i * 32768 + cb * 128, LD_XT + i * 4096);
}

DI void q_phase(const Ctx& x, const Params& p, const u4 (&wf)[48], const f4 (&wsum)[3], QState& st, const Poll& pl) {
  const QDma dm = q_dma_setup(x);
  dma2_sc1<0>(x.ws, dm); dma2_sc1<2048>(x.ws, dm); dma2_sc1<16384>(x.ws, dm); dma2_sc1<16384 + 2048>(x.ws, dm);
  dma2_sc1<32768>(x.ws, dm); dma2_sc1<32768 + 2048>(x.ws, dm);
  QAddr ba;
  q_frag_addr(x, ba);
  const int h = x.w & 1, ph = x.w >> 1, g = x.lane >> 4, cl = x.lane & 15;
  f4 ak[4], av[4], ag[2];
#pragma unroll
  for (int i = 0; i < 4; ++i) { ak[i] = f4{0, 0, 0, 0}; av[i] = f4{0, 0, 0, 0}; }
  ag[0] = f4{0, 0, 0, 0}; ag[1] = f4{0, 0, 0, 0};
  STAMP(x.wsp, 9 + 16 * x.layer);
  {
    u4 b[4];
    wait_vm_barrier<8>();
    poll_retire(pl);
    q_ldb<0>(ba, b);
    SB();
    QPass1<0>::run(x.ws, dm, ba, wf, b, ak, av, ag);
  }
  STAMP(x.wsp, 10 + 16 * x.layer);
  {
    const int r = cl & 3;
    const unsigned so = LD_STAT + 4 * (64 * ph + 32 * h + cl);
    const unsigned a0 = g == 0 ? so : LD_STAT + 1024, a1 = g == (cl >> 2) ? so + 512 : LD_STAT + 1024;
#pragma unroll
    for (int i = 0; i < 2; ++i) {
      const float dq = r == 0 ? ag[i][0] : r == 1 ? ag[i][1] : r == 2 ? ag[i][2] : ag[i][3];
      *(LAS float*)(x.L + a0 + 64 * i) = ak[i][0];
      *(LAS float*)(x.L + a1 + 64 * i) = dq;
    }
  }
  __syncthreads();
  float mean[4], rs[4], cs[4];
#pragma unroll
  for (int pt = 0; pt < 4; ++pt) {
    LAS float* S = (LAS float*)(x.L + LD_STAT) + 64 * ph + 16 * (pt ^ (2 * h)) + cl;
    mean[pt] = S[0] * (1.f / DM);
    rs[pt] = __builtin_amdgcn_rsqf(__builtin_fabsf((S[128] * (1.f / DM) + LN_EPS) - mean[pt] * mean[pt]));
    cs[pt] = rs[pt] * LOG2E;
  }
  const float csr = g == 0 ? cs[0] : g == 1 ? cs[2] : g == 2 ? cs[1] : cs[3];
  lds_t* scr_r = x.L + LD_SCR + x.w * 512 + cl * 8;
  lds_t* scr_w = scr_r + 128 * ((g >> 1) | ((g & 1) << 1));
  lds_t* pv = x.L + LD_PV + x.w * PV_WAVE;
  {
    f4 t[4];
#pragma unroll
    for (int pt = 0; pt < 4; ++pt) t[pt] = ak[pt] - mean[pt] * wsum[1];
    u4 pk4[4];
    softmax4(t, cs, csr, scr_w, scr_r, pk4);
#pragma unroll
    for (int pt = 0; pt < 4; ++pt) {
      const f4 v = (av[pt] - mean[pt] * wsum[2]) * rs[pt];
      lds_t* row = pv + (16 * pt + cl) * PV_ROW + 8 * g;
      *(LAS u2*)(row) = u2{pk4[pt].x, pk4[pt].y}; *(LAS u2*)(row + 32) = u2{pk4[pt].z, pk4[pt].w};
      *(LAS u2*)(row + 64) = u2{pack2(v[0], v[1]), pack2(v[2], v[3])};
    }
  }
  f4 kv0 = {0, 0, 0, 0}, kv1 = kv0, z0 = kv0, z1 = kv0;
  {
    const u4 ones = {0x3f803f80u, 0x3f803f80u, 0x3f803f80u, 0x3f803f80u};
    const int q = cl >> 2, pp = cl & 3;
#pragma unroll
    for (int k2 = 0; k2 < 2; ++k2) {
      lds_t* a = pv + (32 * k2 + 8 * g + q) * PV_ROW + 8 * pp;
      const u4 a0 = cat(lds_tr(a), lds_tr(a + 4 * PV_ROW));
      const u4 a1 = cat(lds_tr(a + 32), lds_tr(a + 4 * PV_ROW + 32));
      const u4 bv = cat(lds_tr(a + 64), lds_tr(a + 4 * PV_ROW + 64));
      kv0 = mfma32(a0, bv, kv0); kv1 = mfma32(a1, bv, kv1); z0 = mfma32(a0, ones, z0); z1 = mfma32(a1, ones, z1);
    }
  }
  STAMP(x.wsp, 11 + 16 * x.layer);
  {
    lds_t* mine = x.L + LD_PV + x.w * PV_WAVE + x.lane * 16;
    if (ph == 1) { *(LAS f4*)(mine) = kv0; *(LAS f4*)(mine + 1024) = kv1; *(LAS f4*)(mine + 2048) = z0; *(LAS f4*)(mine + 3072) = z1; }
    __syncthreads();
    if (ph == 0) {
      lds_t* o = x.L + LD_PV + (x.w + 2) * PV_WAVE + x.lane * 16;
      kv0 += *(LAS f4*)(o); kv1 += *(LAS f4*)(o + 1024); z0 += *(LAS f4*)(o + 2048); z1 += *(LAS f4*)(o + 3072);
      const int hr = (2 * x.hp + h) * 16 + x.rb;
      st16(x.ws, WS_KVP + x.layer * KVP_BYTES + hr * 1024 + x.lane * 16,
           u4{pack2(kv0[0], kv0[1]), pack2(kv0[2], kv0[3]), pack2(kv1[0], kv1[1]), pack2(kv1[2], kv1[3])});
      if (cl == 0) st16(x.ws, WS_ZP + x.layer * ZP_BYTES + hr * 64 + g * 16,
                        u4{pack2(z0[0], z0[1]), pack2(z0[2], z0[3]), pack2(z1[0], z1[1]), pack2(z1[2], z1[3])});
    }
  }
  STAMP(x.wsp, 12 + 16 * x.layer);
  {
    f4 aq[4];
#pragma unroll
    for (int i = 0; i < 4; ++i) aq[i] = f4{0, 0, 0, 0};
    u4 b[3][4];
    q_ldb<0>(ba, b[0]); q_ldb<1>(ba, b[1]);
    SB();
    QPass2<0>::run(ba, wf, b, aq);
    {
      const int cb = x.hp >> 1, q = cl >> 2, pos = 64 * (x.hp & 1) + 16 * x.w + 4 * (cl & 3);
      const int row = 64 * cb + 4 * g + q;
      unsigned ro = LD_XT + row * 256 + (((pos >> 3) ^ ((row & 7) << 1)) * 16) + (pos & 4) * 2;
      asm volatile("" : "+v"(ro));
#pragma unroll
      for (int ct = 0; ct < 4; ++ct) st.res[ct] = lds_tr(x.L + ro + ct * 4096);
    }
    o_load_partials(x, st);
    f4 t[4];
#pragma unroll
    for (int pt = 0; pt < 4; ++pt) t[pt] = aq[pt] - mean[pt] * wsum[0];
    softmax4(t, cs, csr, scr_w, scr_r, st.pq);
  }
  STAMP(x.wsp, 13 + 16 * x.layer);
}

DI void o_compute(const Ctx& x, QState& st) {
  const int h = x.w & 1, ph = x.w >> 1, g = x.lane >> 4, cl = x.lane & 15, hh = 2 * x.hp + h;
  STAMP(x.wsp, 14 + 16 * x.layer);
  const unsigned one = g == (cl >> 2) ? ((cl & 1) ? 0x3f800000u : 0x3f80u) : 0u;
  const unsigned e0 = (cl & 2) ? 0u : one, e1 = (cl & 2) ? one : 0u;
  const u4 sel0 = {e0, e1, 0u, 0u}, sel1 = {0u, 0u, e0, e1};
  f4 k0, k1, den[4];
  for (unsigned spin = 0;; ++spin) {
    k0 = f4{0, 0, 0, 0}; k1 = k0;
#pragma unroll
    for (int i = 0; i < 8; ++i) { k0 = mfma32(sel0, st.pk8[i], k0); k1 = mfma32(sel1, st.pk8[i], k1); }
    const u2 ones = {0x3f803f80u, 0x3f803f80u};
    f4 dp[4];
#pragma unroll
    for (int pt = 0; pt < 4; ++pt) dp[pt] = mfma32(st.az, st.pq[pt], f4{0, 0, 0, 0});
#pragma unroll
    for (int pt = 0; pt < 4; ++pt) den[pt] = mfma16(ones, u2{pack2(dp[pt][0], dp[pt][1]), pack2(dp[pt][2], dp[pt][3])}, f4{1e-6f, 1e-6f, 1e-6f, 1e-6f});
    const float chk = (k0[0] + k1[0]) + den[0][0];
    if (!__any(chk != chk)) break;
    if (spin > 4096u) { spin_fail(x); break; }
    o_load_partials(x, st);
  }
  lds_t* mine = x.L + LD_PV + 8192 + x.w * 2048 + x.lane * 16;
  *(LAS f4*)(mine) = k0; *(LAS f4*)(mine + 1024) = k1;
  __syncthreads();
  STAMP(x.wsp, 15 + 16 * x.layer);
  lds_t* o = x.L + LD_PV + 8192 + (x.w ^ 2) * 2048 + x.lane * 16;
  k0 += *(LAS f4*)(o); k1 += *(LAS f4*)(o + 1024);
  const u4 akv = {pack2(k0[0], k0[1]), pack2(k0[2], k0[3]), pack2(k1[0], k1[1]), pack2(k1[2], k1[3])};
  f4 num[4];
#pragma unroll
  for (int pt = 0; pt < 4; ++pt) num[pt] = mfma32(akv, st.pq[pt], f4{0, 0, 0, 0});
  const unsigned oo = WS_OB + x.layer * OB_BYTES + ((2 * x.rb + ph) * 32 + hh) * 2048 + (16 * ((g & 1) ^ (2 * h)) + cl) * 32 + 16 * (g >> 1);
#pragma unroll
  for (int pt = 0; pt < 4; pt += 2) {
    const f4 va = num[pt] * __builtin_amdgcn_rcpf(den[pt][0]), vb = num[pt + 1] * __builtin_amdgcn_rcpf(den[pt + 1][0]);
    const auto s0 = __builtin_amdgcn_permlane16_swap(pack2(va[0], va[1]), pack2(vb[0], vb[1]), false, false);
    const auto s1 = __builtin_amdgcn_permlane16_swap(pack2(va[2], va[3]), pack2(vb[2], vb[3]), false, false);
    st16(x.ws, oo ^ (pt * 512), u4{s0[0], s1[0], s0[1], s1[1]});
  }
}

struct OAddr { lds_t* p[4]; };
template <int KS> DI void o_ldw(const OAddr& wb, u4 (&b)[4]) {
#pragma unroll
  for (int ct = 0; ct < 4; ++ct) b[ct] = cat(lds_tr(wb.p[ct] + KS * 4096), lds_tr(wb.p[ct] + KS * 4096 + 512));
}
DI unsigned umax3(unsigned a, unsigned b, unsigned c) { const unsigned m = a > b ? a : b; return m > c ? m : c; }
template <int KS> struct OGemm {
  static DI void run(const OAddr& wb, const u4 (&of)[16], u4 (&b)[3][4], f4 (&acc)[4], unsigned& top) {
    if (KS < 14) o_ldw<(KS < 14 ? KS + 2 : 15)>(wb, b[(KS + 2) % 3]);
#pragma unroll
    for (int ct = 0; ct < 4; ++ct) acc[ct] = mfma32(b[KS % 3][ct], of[KS], acc[ct]);
    top = umax3(umax3(top, of[KS].x, of[KS].y), of[KS].z, of[KS].w);
    if (KS < 14) {
#pragma unroll
      for (int i = 0; i < 4; ++i) { SGB(SG_MFMA, 1); SGB(SG_DSR, 2); }
    }
    SB();
    OGemm<KS + 1>::run(wb, of, b, acc, top);
  }
};
template <> struct OGemm<16> { static DI void run(const OAddr&, const u4 (&)[16], u4 (&)[3][4], f4 (&)[4], unsigned&) {} };

DI void o_gemm(const Ctx& x, const Params& p, const QState& st, u4 (&kd)[3]) {
  const int g = x.lane >> 4, cl = x.lane & 15, pb = 2 * x.rb + (x.hp & 1), cb = x.hp >> 1;
  const unsigned oo = WS_OB + x.layer * OB_BYTES + ((pb * 32 + (g >> 1)) * 64 + 16 * x.w + cl) * 32 + 16 * (g & 1);
  OAddr wb;
  {
    const int q = cl >> 2, pp = cl & 3, f = ((q >> 1) & 1) | ((g & 1) << 1);
#pragma unroll
    for (int ct = 0; ct < 4; ++ct) wb.p[ct] = x.L + LD_XT + (8 * g + q) * 128 + (((2 * ct + (pp >> 1)) ^ (f << 1)) * 16) + 8 * (pp & 1);
  }
  f4 acc[4];
  o_dma_wo(x, p);
  for (unsigned spin = 0;; ++spin) {
    u4 of[16];
#pragma unroll
    for (int ks = 0; ks < 16; ++ks) of[ks] = ld16(x.ws, oo + ks * 4096);
    if (spin == 0) wait_vm_barrier<16>();
    STAMP(x.wsp, 17 + 16 * x.layer);
#pragma unroll
    for (int i = 0; i < 4; ++i) acc[i] = f4{0, 0, 0, 0};
    u4 b[3][4];
    o_ldw<0>(wb, b[0]); o_ldw<1>(wb, b[1]);
    SB();
    unsigned top = 0;
    OGemm<0>::run(wb, of, b, acc, top);
    if (!__any(top == ~0u)) break;
    if (spin > 4096u) { spin_fail(x); break; }
  }
  STAMP(x.wsp, 18 + 16 * x.layer);
  if (x.layer == 0) conv_load_k(x, kd);
  float s = 0.f, q = 0.f;
#pragma unroll
  for (int ct = 0; ct < 4; ++ct) {
    const unsigned a = pack2(blo(st.res[ct].x) + acc[ct][0], bhi(st.res[ct].x) + acc[ct][1]);
    const unsigned b2 = pack2(blo(st.res[ct].y) + acc[ct][2], bhi(st.res[ct].y) + acc[ct][3]);
    const float v0 = blo(a), v1 = bhi(a), v2 = blo(b2), v3 = bhi(b2);
    s += (v0 + v1) + (v2 + v3); q += (v0 * v0 + v1 * v1) + (v2 * v2 + v3 * v3);
    *(LAS u2*)(x.L + LD_OST + (16 * x.w + cl) * 144 + (16 * ct + 4 * g) * 2) = u2{a, b2};
  }
  if (x.layer == 0) {
    const f2 sq = xrow_sum2(s, q);
    if (g < 2) {
      GAS float* d = (GAS float*)(p.ws + WS_ST + 2048 * 8) + g * 2048 + 64 * pb + 16 * x.w + cl;
      __hip_atomic_fetch_add(d, g ? sq.y : sq.x, RLX_AGENT);
    }
  }
  __syncthreads();
  if (x.layer == 0) {
    const int qq = cl >> 2, pp = cl & 3;
#pragma unroll
    for (int j = 0; j < 2; ++j) {
      lds_t* a = x.L + LD_OST + (8 * (g + 4 * j) + qq) * 144 + (16 * x.w + 4 * pp) * 2;
      const u4 v = cat(lds_tr(a), lds_tr(a + 4 * 144));
      st16(x.ws, WS_XT + XT_BYTES + ((64 * cb + 16 * x.w + cl) * XT_STRIDE + 64 * pb + 8 * (g + 4 * j)) * 2, v);
    }
  } else {
#pragma unroll
    for (int i = 0; i < 2; ++i) {
      const int k = i * 256 + x.tid, pos = k >> 3, q8 = k & 7;
      *(GAS u4*)(p.out + (64 * pb + pos) * DM + 64 * cb + 8 * q8) = *(LAS u4*)(x.L + LD_OST + pos * 144 + q8 * 16);
    }
  }
  STAMP(x.wsp, 19 + 16 * x.layer);
}

DI Ctx make_ctx(const Params& p, unsigned char* smem) {
  Ctx x;
  x.L = (lds_t*)smem; x.ws = make_rsrc(p.ws, WS_END); x.cw = (GAS unsigned*)(p.ws + WS_CTRL); x.wsp = p.ws;
  x.tid = threadIdx.x; x.lane = x.tid & 63; x.w = __builtin_amdgcn_readfirstlane(x.tid >> 6); x.c = blockIdx.x;
  x.cp = (x.c & 7) * 32 + (x.c >> 3);
  x.rb = 2 * (x.c & 7) + (x.c >> 7); x.hp = (x.c >> 3) & 15; x.layer = 0;
  return x;
}

DI Ctx fresh(const Ctx& x, int layer) { Ctx y = x; int t = x.tid; asm volatile("" : "+v"(t)); y.tid = t; y.lane = t & 63; y.layer = layer; return y; }

__global__ void __launch_bounds__(256, 1) k_main(Params p) {
  extern __shared__ __attribute__((aligned(16))) unsigned char smem[];
  const unsigned char* code0;
  asm volatile("s_getpc_b64 %0" : "=s"(code0));
  const Ctx x0 = make_ctx(p, smem);
  STAMP(p.ws, 0);
  u4 wf[48];
  f4 wsum[3];
  const float dsk = bf2f(p.dsk[2 * x0.cp + (x0.w >> 1)]);
  unsigned tag;
  {
    const Ctx x = fresh(x0, 0);
    u4 kd[3], a[32];
    ProIn pin;
    pro_load(x, p, pin);
    tag = pin.epoch + 1u;
    pro_publish_x(x, pin);
    SB();
    pro_fold_w(x, 6 * x.c + x.w, pin.w[0]);
    if (x.w < 2) { pro_fold_w(x, 6 * x.c + 4 + x.w, pin.w[1]); asm volatile("s_waitcnt vmcnt(2)" ::: "memory"); }
    else asm volatile("s_waitcnt vmcnt(1)" ::: "memory");
    __syncthreads();
    if (x.tid == 0) __builtin_amdgcn_raw_buffer_store_b32(tag, x.ws, WS_FLAG + x.c * 64, 0, AUX_SC1);
    const unsigned touched = pro_touch(x, p);
    pro_init(x);
    STAMP(p.ws, 1);
    pro_tables(x, pin);
    drain();
    asm volatile("" :: "v"(touched));
    __syncthreads();
    if (x.tid == 0) __builtin_amdgcn_raw_buffer_store_b32(tag, x.ws, WS_FLAG2 + x.c * 64, 0, AUX_SC1);
    STAMP(p.ws, 2);
    const unsigned fl = __builtin_amdgcn_raw_buffer_load_b32(x.ws, WS_FLAG + x.tid * 64, 0, AUX_SC1);
    pro_kgemm(x);
    pro_wait(x, WS_FLAG, tag, fl);
    STAMP(p.ws, 3);
    ConvIn in;
    conv_load_in(x, 0, in);
    SB();
    pro_expand(x);
    __syncthreads();
    pro_take_k(x, kd);
    conv_build_k(x, kd);
    STAMP(p.ws, 4);
    conv_build_u(x, in);
    const u4 fl2 = pro_peek_wave(x, WS_FLAG2);
    __syncthreads();
    STAMP(p.ws, 5);
    pro_wait_wave(x, WS_FLAG2, tag, fl2);
    STAMP3(x, 36);
    Warm wm;
    pro_warm(x, p, code0, wm);
    if (x.w & 1) conv_wave<1, true>(x, dsk, a); else conv_wave<0, true>(x, dsk, a);
    pro_warm_retire(wm);
    a2a_post(fresh(x0, 0), 0);
    poison_o(fresh(x0, 0), 1);
    q_take_w(fresh(x0, 0), wf);
    q_wsum(wf, wsum);
  }
#pragma unroll 1
  for (int layer = 0; layer < 2; ++layer) {
    Ctx x = fresh(x0, layer);
    Poll pl;
    a2a_wait(x, 2 * layer, pl);
    STAMP(p.ws, 8 + 16 * layer);
    QState st;
    x = fresh(x0, layer);
    q_phase(x, p, wf, wsum, st, pl);
    x = fresh(x0, layer);
    o_compute(x, st);
    STAMP(p.ws, 16 + 16 * layer);
    x = fresh(x0, layer);
    u4 kd[3];
    o_gemm(x, p, st, kd);
    if (layer == 0) {
      u4 a[32];
      conv_build_k(x, kd);
      a2a_signal(x, 1);
      x = fresh(x0, 1);
      if (x.w & 1) conv_load_a<1>(x, a); else conv_load_a<0>(x, a);
      SB();
      a2a_wait(x, 1, pl);
      STAMP(p.ws, 20);
      ConvIn in;
      conv_load_in(x, 1, in);
      conv_build_u(x, in);
      poll_retire(pl);
      __syncthreads();
      STAMP(p.ws, 21);
      if (x.w & 1) conv_wave<1, false>(x, dsk, a); else conv_wave<0, false>(x, dsk, a);
      a2a_post(fresh(x0, 1), 2);
    }
  }
  if (x0.c == 0 && x0.tid == 0) __builtin_amdgcn_raw_buffer_store_b32(tag, x0.ws, WS_EPOCH, 0, AUX_SC1);
}

extern "C" void kernel_launch(void* const* d_in, const int* in_sizes, int n_in, void* d_out, int out_size, void* d_ws, size_t ws_size,
                              hipStream_t stream) {
  Params p;
  p.u = (const unsigned short*)d_in[0]; p.lar = (const unsigned short*)d_in[1]; p.aim = (const unsigned short*)d_in[2];
  p.cre = (const unsigned short*)d_in[3]; p.cim = (const unsigned short*)d_in[4]; p.ldt = (const unsigned short*)d_in[5];
  p.dsk = (const unsigned short*)d_in[6]; p.wqkv = (const unsigned short*)d_in[8]; p.wphi = (const unsigned short*)d_in[9];
  p.wo = (const unsigned short*)d_in[10]; p.out = (unsigned short*)d_out; p.ws = (unsigned char*)d_ws;
  (void)hipFuncSetAttribute((const void*)k_main, hipFuncAttributeMaxDynamicSharedMemorySize, LDS_BYTES);
  k_main<<<256, 256, LDS_BYTES, stream>>>(p);
}
```

```cpp
#include <hip/hip_runtime.h>
#include <hip/hip_bf16.h>
#include <stdint.h>

#define MK_STAMP 0
#define STAMP(ws, i) do { } while (0)
#define STAMP2(x, i) do { } while (0)
#define STAMP3(x, i) do { } while (0)
typedef short s4 __attribute__((ext_vector_type(4)));
typedef short s8 __attribute__((ext_vector_type(8)));
typedef float f2 __attribute__((ext_vector_type(2)));
typedef float f4 __attribute__((ext_vector_type(4)));
typedef unsigned u2 __attribute__((ext_vector_type(2)));
typedef unsigned u4 __attribute__((ext_vector_type(4)));
typedef __bf16 bf2 __attribute__((ext_vector_type(2)));
#define GAS __attribute__((address_space(1)))
#define LAS __attribute__((address_space(3)))
#define DI __device__ __forceinline__
#define RLX_AGENT __ATOMIC_RELAXED, __HIP_MEMORY_SCOPE_AGENT
typedef GAS unsigned gu32;
typedef LAS unsigned char lds_t;
typedef __amdgpu_buffer_rsrc_t rsrc_t;

constexpr int SEQ = 2048, DM = 512, NST = 128, NH = 32;
constexpr float LOG2E = 1.4426950408889634f;
constexpr float LN_EPS = 1e-5f;
constexpr int AUX_SC1 = 16;

constexpr int XT_STRIDE = 2112;
constexpr unsigned WS_CTRL = 0;
constexpr unsigned WS_KREV = 262144;
constexpr unsigned WS_WFRAG = WS_KREV + 512u * 2048 * 2;
constexpr unsigned WS_XT = WS_WFRAG + 32u * 3 * 16 * 1024;
constexpr unsigned XT_BYTES = 512u * XT_STRIDE * 2;
constexpr unsigned WS_ST = WS_XT + 2 * XT_BYTES;
constexpr unsigned WS_X1 = WS_ST + 2u * 2048 * 8;
constexpr unsigned X1_BYTES = 2048u * 512 * 2;
constexpr unsigned WS_KVP = WS_X1 + 2 * X1_BYTES;
constexpr unsigned KVP_BYTES = 32u * 16 * 1024;
constexpr unsigned WS_ZP = WS_KVP + 2 * KVP_BYTES;
constexpr unsigned ZP_BYTES = 32u * 16 * 64;
constexpr unsigned WS_OB = WS_ZP + 2 * ZP_BYTES;
constexpr unsigned OB_BYTES = 2048u * 512 * 2;
constexpr unsigned WS_END = WS_OB + 2 * OB_BYTES;
constexpr int CW_TMO = 0;
#define A2A_NS 16
#define A2A_REP 8
constexpr int NS = A2A_NS, REP = A2A_REP;
constexpr int NCW_A2A = 3 * REP * NS;
constexpr int CW_STRIDE = 64;
constexpr int CWI_A2A = 1, CWI_END = CWI_A2A + NCW_A2A;
constexpr unsigned WS_EPOCH = 131072, WS_FLAG = WS_EPOCH + 256, WS_FLAG2 = WS_FLAG + 256 * 64;
static_assert(CWI_END <= 512 && CWI_END * CW_STRIDE * 4 <= WS_EPOCH && WS_FLAG2 + 256 * 64 <= WS_KREV, "control block");

struct Params {
  const unsigned short* u;
  const unsigned short* lar;
  const unsigned short* aim;
  const unsigned short* cre;
  const unsigned short* cim;
  const unsigned short* ldt;
  const unsigned short* dsk;
  const unsigned short* wqkv;
  const unsigned short* wphi;
  const unsigned short* wo;
  unsigned short* out;
  unsigned char* ws;
};

DI float bf2f(unsigned short b) { return __builtin_bit_cast(float, (unsigned)b << 16); }
DI float blo(unsigned x) { return __builtin_bit_cast(float, x << 16); }
DI float bhi(unsigned x) { return __builtin_bit_cast(float, x & 0xffff0000u); }
DI unsigned pack2(float a, float b) { bf2 v = {(__bf16)a, (__bf16)b}; return __builtin_bit_cast(unsigned, v); }
DI float rbf(float a) { return blo(pack2(a, a)); }
DI f4 mfma32(u4 a, u4 b, f4 c) { return __builtin_amdgcn_mfma_f32_16x16x32_bf16(__builtin_bit_cast(s8, a), __builtin_bit_cast(s8, b), c, 0, 0, 0); }
DI f4 mfma16(u2 a, u2 b, f4 c) { return __builtin_amdgcn_mfma_f32_16x16x16bf16_1k(__builtin_bit_cast(s4, a), __builtin_bit_cast(s4, b), c, 0, 0, 0); }
DI void mfma_va(f4& acc, const u4& a, const u4& b) { asm volatile("v_mfma_f32_16x16x32_bf16 %0, %1, %2, %0" : "+v"(acc) : "a"(a), "v"(b)); }
DI void mfma_va0(f4& acc, const u4& a, const u4& b) { asm volatile("v_mfma_f32_16x16x32_bf16 %0, %1, %2, 0" : "=&v"(acc) : "a"(a), "v"(b)); }
DI void mfma_vv(f4& acc, const u4& a, const u4& b) { asm volatile("v_mfma_f32_16x16x32_bf16 %0, %1, %2, %0" : "+v"(acc) : "v"(a), "v"(b)); }
DI void mfma_vv0(f4& acc, const u4& a, const u4& b) { asm volatile("v_mfma_f32_16x16x32_bf16 %0, %1, %2, 0" : "=&v"(acc) : "v"(a), "v"(b)); }
DI void mfma_done() { asm volatile("s_nop 7" ::: "memory"); __builtin_amdgcn_sched_barrier(0); }
DI u2 lds_tr(lds_t* p) { return __builtin_bit_cast(u2, __builtin_amdgcn_ds_read_tr16_b64_v4i16((LAS s4*)p)); }
DI u4 cat(u2 a, u2 b) { return u4{a.x, a.y, b.x, b.y}; }
DI rsrc_t make_rsrc(const void* p, unsigned bytes) { return __builtin_amdgcn_make_buffer_rsrc((void*)p, 0, (int)bytes, 0x00020000); }
DI u4 ld16(rsrc_t r, unsigned off) { return __builtin_amdgcn_raw_buffer_load_b128(r, off, 0, AUX_SC1); }
DI void st16(rsrc_t r, unsigned off, u4 v) { __builtin_amdgcn_raw_buffer_store_b128(v, r, off, 0, AUX_SC1); }
DI void drain() { asm volatile("s_waitcnt vmcnt(0)" ::: "memory"); }
DI void dma4_sc1(rsrc_t r, unsigned v0, unsigned v1, unsigned soff, unsigned lds) {
  asm volatile("s_mov_b32 m0, %4\n\ts_nop 0\n\t"
               "buffer_load_dwordx4 %0, %2, %3 offen sc1 lds\n\tbuffer_load_dwordx4 %1, %2, %3 offen offset:1024 sc1 lds\n\t"
               "buffer_load_dwordx4 %0, %2, %3 offen offset:2048 sc1 lds\n\tbuffer_load_dwordx4 %1, %2, %3 offen offset:3072 sc1 lds"
               :: "v"(v0), "v"(v1), "s"(r), "s"(soff), "s"(lds) : "memory");
}
DI void dma4(rsrc_t r, unsigned v0, unsigned v1, unsigned v2, unsigned v3, unsigned soff, unsigned lds) {
  asm volatile("s_mov_b32 m0, %6\n\ts_nop 0\n\t"
               "buffer_load_dwordx4 %0, %4, %5 offen lds\n\tbuffer_load_dwordx4 %1, %4, %5 offen offset:1024 lds\n\t"
               "buffer_load_dwordx4 %2, %4, %5 offen offset:2048 lds\n\tbuffer_load_dwordx4 %3, %4, %5 offen offset:3072 lds"
               :: "v"(v0), "v"(v1), "v"(v2), "v"(v3), "s"(r), "s"(soff), "s"(lds) : "memory");
}
#define SB() __builtin_amdgcn_sched_barrier(0)
#define SGB(mask, n) __builtin_amdgcn_sched_group_barrier(mask, n, 0)
constexpr int SG_MFMA = 0x8, SG_DSR = 0x100;
template <int CTRL> DI float dpp_f(float v) { return __builtin_bit_cast(float, __builtin_amdgcn_mov_dpp(__builtin_bit_cast(int, v), CTRL, 0xf, 0xf, true)); }
DI float row_sum(float v) { v += dpp_f<0x128>(v); v += dpp_f<0x124>(v); v += dpp_f<0x122>(v); v += dpp_f<0x121>(v); return v; }
DI f2 swap32(float a, float b) { auto r = __builtin_amdgcn_permlane32_swap(__builtin_bit_cast(unsigned, a), __builtin_bit_cast(unsigned, b), false, false);
  const unsigned r0 = r[0], r1 = r[1];
  return f2{__builtin_bit_cast(float, r0), __builtin_bit_cast(float, r1)}; }
DI f2 swap16(float a, float b) { auto r = __builtin_amdgcn_permlane16_swap(__builtin_bit_cast(unsigned, a), __builtin_bit_cast(unsigned, b), false, false);
  const unsigned r0 = r[0], r1 = r[1];
  return f2{__builtin_bit_cast(float, r0), __builtin_bit_cast(float, r1)}; }
DI float fmax2(float a, float b) { return __builtin_fmaxf(a, b); }
DI float fmin2(float a, float b) { return __builtin_fminf(a, b); }
template <int OP> DI float op2(float a, float b) { return OP == 0 ? fmax2(a, b) : OP == 1 ? fmin2(a, b) : a + b; }
template <int OP> DI float xrow4(float a, float b, float c, float d) {
  f2 s = swap32(a, b); const float ab = op2<OP>(s.x, s.y);
  s = swap32(c, d); const float cd = op2<OP>(s.x, s.y);
  s = swap16(ab, cd); return op2<OP>(s.x, s.y);
}
DI f2 xrow4_maxmin(float a0, float a1, float a2, float a3, float b0, float b1, float b2, float b3) {
  asm("s_nop 1\n\t"
      "v_permlane32_swap_b32 %0, %1\n\tv_permlane32_swap_b32 %2, %3\n\tv_permlane32_swap_b32 %4, %5\n\tv_permlane32_swap_b32 %6, %7\n\t"
      "v_max_f32 %0, %0, %1\n\tv_max_f32 %2, %2, %3\n\tv_min_f32 %4, %4, %5\n\tv_min_f32 %6, %6, %7\n\t"
      "v_permlane16_swap_b32 %0, %2\n\ts_nop 0\n\tv_permlane16_swap_b32 %4, %6\n\t"
      "v_max_f32 %0, %0, %2\n\tv_min_f32 %4, %4, %6"
      : "+v"(a0), "+v"(a1), "+v"(a2), "+v"(a3), "+v"(b0), "+v"(b1), "+v"(b2), "+v"(b3));
  return f2{a0, b0};
}
DI f2 xrow_max2(float a, float b) { f2 s = swap32(a, b); float c = fmaxf(s.x, s.y); s = swap16(c, c); c = fmaxf(s.x, s.y); return swap32(c, c); }
DI f2 xrow_sum2(float a, float b) { f2 s = swap32(a, b); float c = s.x + s.y; s = swap16(c, c); c = s.x + s.y; return swap32(c, c); }
DI float wave_sum(float v) { v = row_sum(v); f2 s = swap16(v, v); v = s.x + s.y; s = swap32(v, v); return s.x + s.y; }

constexpr int LD_XT = 0;
constexpr int LD_KC = 65536;
constexpr int KC_COPY = 4160, KC_CH = 4 * KC_COPY;
constexpr int LD_U = LD_KC + 2 * KC_CH;
constexpr int U_CH = 4608;
constexpr int LD_X = LD_U + 2 * U_CH;
constexpr int LD_EX = LD_KC;
constexpr int LD_CONV_END = LD_X + 8192;
constexpr int LD_WA = 0, LD_WB = LD_CONV_END;
constexpr int LD_MISC = 131072;
constexpr int LD_PV = LD_MISC;
constexpr int PV_ROW = 104, PV_WAVE = 64 * PV_ROW;
constexpr int LD_STAT = LD_PV + 4 * PV_WAVE;
constexpr int LD_SCR = LD_STAT + 1024 + 256;
constexpr int LD_FLAG = LD_SCR + 2048;
constexpr int LD_OST = LD_PV;
constexpr int LDS_BYTES = 163840;
static_assert(LD_CONV_END <= LD_MISC && LD_FLAG + 64 <= LDS_BYTES, "LDS map");

struct Ctx {
  lds_t* L; rsrc_t ws; GAS unsigned* cw; unsigned char* wsp;
  int tid, lane, w, c, cp, rb, hp, layer;
};

DI void spin_fail(const Ctx& x) { __hip_atomic_store(x.cw + CW_TMO, 1u, RLX_AGENT); }
constexpr unsigned SPIN_CAP = 1u << 18;
DI void a2a_post(const Ctx& x, int e) {
  __syncthreads();
  if (x.tid < REP) __hip_atomic_fetch_add(x.cw + (CWI_A2A + (e * REP + x.tid) * NS + (x.c >> 3) % NS) * CW_STRIDE, 1u, RLX_AGENT);
}
DI void a2a_signal(const Ctx& x, int e) { drain(); a2a_post(x, e); }
DI void poll16(const Ctx& x, unsigned word) {
  if (x.w == 0) {
    GAS unsigned* q = x.cw + word * CW_STRIDE;
    for (unsigned s = 0;; ++s) {
      if (__all(__hip_atomic_load(q, RLX_AGENT) >= 256u / NS)) break;
      if (s > SPIN_CAP) { spin_fail(x); break; }
      __builtin_amdgcn_s_sleep(1);
    }
    __builtin_amdgcn_fence(__ATOMIC_ACQUIRE, "wavefront");
  }
  __syncthreads();
}
DI void a2a_wait(const Ctx& x, int e) { poll16(x, CWI_A2A + (e * REP + (x.c & 7) % REP) * NS + x.lane % NS); }

DI void conv_load_k(const Ctx& x, u4 (&kd)[3]) {
  const int ch = x.tid >> 7, q = x.tid & 127;
  const unsigned off = WS_KREV + ((2 * x.cp + ch) * 2048 + 16 * q) * 2;
  kd[0] = __builtin_amdgcn_raw_buffer_load_b128(x.ws, off, 0, 0);
  kd[1] = __builtin_amdgcn_raw_buffer_load_b128(x.ws, off + 16, 0, 0);
  kd[2] = __builtin_amdgcn_raw_buffer_load_b128(x.ws, off + 32, 0, 0);
}
DI void conv_build_k(const Ctx& x, const u4 (&kd)[3]) {
  const int ch = x.tid >> 7, q = x.tid & 127;
  const unsigned d8 = q == 127 ? 0u : kd[2].x, d9 = q == 127 ? 0u : kd[2].y;
  const unsigned d[10] = {kd[0].x, kd[0].y, kd[0].z, kd[0].w, kd[1].x, kd[1].y, kd[1].z, kd[1].w, d8, d9};
  lds_t* b = x.L + LD_KC + ch * KC_CH + 32 * q;
  *(LAS u4*)(b) = u4{d[0], d[1], d[2], d[3]}; *(LAS u4*)(b + 16) = u4{d[4], d[5], d[6], d[7]};
  *(LAS u4*)(b + 2 * KC_COPY) = u4{d[1], d[2], d[3], d[4]}; *(LAS u4*)(b + 2 * KC_COPY + 16) = u4{d[5], d[6], d[7], d[8]};
  unsigned e[9];
#pragma unroll
  for (int i = 0; i < 9; ++i) e[i] = __builtin_amdgcn_alignbit(d[i + 1], d[i], 16);
  *(LAS u4*)(b + KC_COPY) = u4{e[0], e[1], e[2], e[3]}; *(LAS u4*)(b + KC_COPY + 16) = u4{e[4], e[5], e[6], e[7]};
  *(LAS u4*)(b + 3 * KC_COPY) = u4{e[1], e[2], e[3], e[4]}; *(LAS u4*)(b + 3 * KC_COPY + 16) = u4{e[5], e[6], e[7], e[8]};
  if (x.tid < 16) {
    *(LAS u4*)(x.L + LD_KC + (x.tid >> 1) * KC_COPY + 4096 + (x.tid & 1) * 16) = u4{0, 0, 0, 0};
  }
  if (x.tid >= 64 && x.tid < 128) {
    const int i = x.tid - 64;
    *(LAS u4*)(x.L + LD_U + (i >> 5) * U_CH + (i & 31) * 16) = u4{0, 0, 0, 0};
  }
}
template <int PAR> struct Mu { static constexpr int v(int k) { return PAR ? (k == 0 ? 1 : k == 1 ? 2 : k == 2 ? 5 : 6) : (k == 0 ? 0 : k == 1 ? 3 : k == 2 ? 4 : 7); } };
template <int PAR> DI lds_t* conv_a_base(const Ctx& x) {
  const int b = x.lane & 15, kq = x.lane >> 4, ch = x.w >> 1;
  const int s0 = 2047 - 16 * (kq >> 1) - b + 8 * (kq & 1), cc = s0 & 3;
  unsigned bo = LD_KC + ch * KC_CH + cc * KC_COPY + 2 * (s0 - cc) - 64 * 63;
  asm volatile("" : "+v"(bo));
  return x.L + bo;
}
template <int PAR> DI u4 conv_a_frag(lds_t* base, int k, int i) {
  const int m = 8 * Mu<PAR>::v(k) + i;
  const u2 lo = *(volatile LAS u2*)(base + 64 * (63 - m)), hi = *(volatile LAS u2*)(base + 64 * (63 - m) + 8);
  return cat(lo, hi);
}
template <int PAR> DI void conv_load_a(const Ctx& x, u4 (&a)[32]) {
  lds_t* base = conv_a_base<PAR>(x);
#pragma unroll
  for (int k = 0; k < 4; ++k)
#pragma unroll
    for (int i = 0; i < 8; ++i) a[8 * k + i] = conv_a_frag<PAR>(base, k, i);
}
struct WDma { unsigned lane16, soff, lo, hi; };
DI WDma q_dma_w_setup(const Ctx& x) {
  const unsigned lo = x.w < 3 ? LD_WA + x.w * 24576 : LD_WB + 8192, hi = x.w < 2 ? lo : x.w == 2 ? LD_WB - 16384 : lo;
  return WDma{(unsigned)x.lane * 16, WS_WFRAG + x.hp * (96 * 1024) + x.w * 24576, lo, hi};
}
template <int I> DI void q_dma_w1(const Ctx& x, const WDma& d) {
  unsigned t;
  asm volatile("s_add_u32 m0, %4, %5\n\ts_add_u32 %0, %3, %5\n\tbuffer_load_dwordx4 %1, %2, %0 offen sc1 lds"
               : "=&s"(t) : "v"(d.lane16), "s"(x.ws), "s"(d.soff), "s"(I < 16 ? d.lo : d.hi), "n"(I * 1024) : "memory", "scc");
}
DI void q_take_w(const Ctx& x, u4 (&wf)[48]) {
  const int h = x.w & 1;
  const unsigned a0 = LD_WA + h * 49152 + x.lane * 16, a1 = (h ? LD_WB - 16384 : LD_WA) + x.lane * 16;
#pragma unroll
  for (int i = 0; i < 48; ++i)
    asm volatile("ds_read_b128 %0, %1 offset:%2" : "=a"(wf[i]) : "v"(i < 16 ? a0 : a1), "n"(i * 1024));
  asm volatile("s_waitcnt lgkmcnt(0)" ::: "memory");
  SB();
}
template <int TAU, int I> DI void conv_dma_w(const Ctx& x, const WDma& d) { if ((I & 1) && TAU < 6) { q_dma_w1<(TAU < 6 ? 4 * TAU + (I >> 1) : 0)>(x, d); SB(); } }
template <int PAR, bool FIRST> DI void conv_mfma(const Ctx& x, u4 (&a)[32], f4 (&acc)[8]) {
  const WDma wd = q_dma_w_setup(x);
  const int al = x.lane & 15, kq = x.lane >> 4, ch = x.w >> 1;
  unsigned bo = LD_U + ch * U_CH + 2 * (256 + 16 * al - 16 * (kq >> 1) + 8 * (kq & 1)) - 64 * 7;
  asm volatile("" : "+v"(bo));
  lds_t* base = x.L + bo;
  constexpr int NT = 8 - Mu<PAR>::v(0);
  u4 b[8];
  if (FIRST) {
    STAMP3(x, 37);
    lds_t* abase = conv_a_base<PAR>(x);
#pragma unroll
    for (int i = 0; i < 8; ++i) { b[i] = *(LAS u4*)(base + 64 * (7 - i)); a[i] = conv_a_frag<PAR>(abase, 0, i); }
    SB();
#pragma unroll
    for (int k = 0; k < 4; ++k) {
#pragma unroll
      for (int i = 0; i < 8; ++i) {
        acc[Mu<PAR>::v(k)] = mfma32(a[8 * k + i], b[i], acc[Mu<PAR>::v(k)]);
        if (k < 3) { a[8 * (k + 1) + i] = conv_a_frag<PAR>(abase, k + 1, i); SGB(SG_MFMA, 1); SGB(SG_DSR, 2); }
        else { b[i] = *(LAS u4*)(base + 64 * (8 - i + 7)); SGB(SG_MFMA, 1); SGB(SG_DSR, 1); }
        SB();
      }
      switch (k) {
        case 0: q_dma_w1<0>(x, wd); break; case 1: q_dma_w1<1>(x, wd); break;
        case 2: q_dma_w1<2>(x, wd); break; default: q_dma_w1<3>(x, wd); break;
      }
      SB();
    }
    STAMP3(x, 38);
  } else {
#pragma unroll
    for (int i = 0; i < 8; ++i) b[i] = *(LAS u4*)(base + 64 * (7 - i));
    SB();
  }
#pragma unroll
  for (int tau = FIRST ? 1 : 0; tau < 8; ++tau) {
    if (FIRST && tau == 4) STAMP3(x, 39);
    if (tau < NT) {
      int ng = 0;
#pragma unroll
      for (int k = 0; k < 4; ++k) ng += (Mu<PAR>::v(k) + tau <= 7);
#pragma unroll
      for (int i = 0; i < 8; ++i) {
#pragma unroll
        for (int k = 0; k < 4; ++k)
          if (Mu<PAR>::v(k) + tau <= 7) acc[Mu<PAR>::v(k) + tau] = mfma32(a[8 * k + i], b[i], acc[Mu<PAR>::v(k) + tau]);
        if (tau + 1 < NT) {
          b[i] = *(LAS u4*)(base + 64 * (8 * (tau + 1) - i + 7));
          if (ng == 4) SGB(SG_MFMA, 4); else if (ng == 3) SGB(SG_MFMA, 3); else if (ng == 2) SGB(SG_MFMA, 2); else SGB(SG_MFMA, 1);
          SGB(SG_DSR, 1);
        }
        SB();
        if (FIRST) {
          switch (8 * tau + i) {
#define W_(t, j) case 8 * t + j: conv_dma_w<t, j>(x, wd); break;
#define WT_(t) W_(t, 1) W_(t, 3) W_(t, 5) W_(t, 7)
            WT_(1) WT_(2) WT_(3) WT_(4) WT_(5)
#undef WT_
#undef W_
            default: break;
          }
        }
      }
    }
  }
}

struct ConvIn { u4 x0, x1, s[4]; };
DI void conv_load_in(const Ctx& x, int layer, ConvIn& in) {
  const unsigned xo = WS_XT + layer * XT_BYTES + (2 * x.cp * XT_STRIDE + 8 * x.tid) * 2;
  const unsigned so = WS_ST + layer * 2048 * 8 + x.tid * 32;
  in.s[0] = ld16(x.ws, so); in.s[1] = ld16(x.ws, so + 16); in.s[2] = ld16(x.ws, so + 8192); in.s[3] = ld16(x.ws, so + 8192 + 16);
  in.x0 = ld16(x.ws, xo); in.x1 = ld16(x.ws, xo + XT_STRIDE * 2);
}
DI f2 unpk(unsigned v) { return f2{blo(v), bhi(v)}; }
DI unsigned pk(f2 v) { return pack2(v.x, v.y); }
DI void conv_build_u(const Ctx& x, const ConvIn& in) {
  const f4 sa = __builtin_bit_cast(f4, in.s[0]), sb = __builtin_bit_cast(f4, in.s[1]), qa = __builtin_bit_cast(f4, in.s[2]), qb = __builtin_bit_cast(f4, in.s[3]);
  const f2 sv[4] = {f2{sa[0], sa[1]}, f2{sa[2], sa[3]}, f2{sb[0], sb[1]}, f2{sb[2], sb[3]}};
  const f2 qv[4] = {f2{qa[0], qa[1]}, f2{qa[2], qa[3]}, f2{qb[0], qb[1]}, f2{qb[2], qb[3]}};
  u4 u0, u1;
#pragma unroll
  for (int i = 0; i < 4; ++i) {
    const f2 m = sv[i] * (1.f / DM);
    const f2 ve = (qv[i] * (1.f / DM) + LN_EPS) - m * m;
    const f2 r = {__builtin_amdgcn_rsqf(__builtin_fabsf(ve.x)), __builtin_amdgcn_rsqf(__builtin_fabsf(ve.y))};
    const f2 nmr = -m * r;
    u0[i] = pk(unpk(in.x0[i]) * r + nmr);
    u1[i] = pk(unpk(in.x1[i]) * r + nmr);
  }
  *(LAS u4*)(x.L + LD_U + 512 + 16 * x.tid) = u0; *(LAS u4*)(x.L + LD_U + U_CH + 512 + 16 * x.tid) = u1;
  *(LAS u4*)(x.L + LD_X + 16 * x.tid) = in.x0; *(LAS u4*)(x.L + LD_X + 4096 + 16 * x.tid) = in.x1;
}
DI void poison_o(const Ctx& x, int layer) {
#pragma unroll
  for (int i = 0; i < 2; ++i) st16(x.ws, WS_OB + layer * OB_BYTES + (x.c * 2 + i) * 4096 + x.tid * 16, u4{~0u, ~0u, ~0u, ~0u});
}
template <int PAR, bool LOADW> DI void conv_wave(const Ctx& x, float dsk, u4 (&a)[32]) {
  const int ch = x.w >> 1;
  f4 acc[8];
#pragma unroll
  for (int t = 0; t < 8; ++t) acc[t] = f4{0, 0, 0, 0};
  conv_mfma<PAR, LOADW>(x, a, acc);
  if (LOADW) __syncthreads();
  STAMP(x.wsp, 6 + 16 * x.layer);
  const int al = x.lane & 15, g = x.lane >> 4;
  unsigned eo = LD_EX + x.lane * 16, uo = LD_U + ch * U_CH + 512 + 2 * (1024 * PAR + 16 * al + 4 * g);
  asm volatile("" : "+v"(eo), "+v"(uo));
  lds_t* mine = x.L + eo + x.w * 4096;
  lds_t* other = x.L + eo + (x.w ^ 1) * 4096;
#pragma unroll
  for (int t = 0; t < 4; ++t) *(LAS f4*)(mine + t * 1024) = acc[PAR ? t : 4 + t];
  f2 v[4][2];
#pragma unroll
  for (int t = 0; t < 4; ++t) {
    const u2 uu = *(LAS u2*)(x.L + uo + 512 * t), xx = *(LAS u2*)(x.L + uo + (LD_X - LD_U - 512) + ch * (4096 - U_CH) + 512 * t);
    const f4 own = acc[PAR ? 4 + t : t];
    v[t][0] = unpk(uu.x) * dsk + (unpk(xx.x) + f2{own[0], own[1]});
    v[t][1] = unpk(uu.y) * dsk + (unpk(xx.y) + f2{own[2], own[3]});
  }
  __syncthreads();
  STAMP(x.wsp, 7 + 16 * x.layer);
  const unsigned so = WS_X1 + x.layer * X1_BYTES + (((8 * PAR + 2 * (g & 1) + (al >> 3)) * 512 + 2 * x.cp + ch) * 128 + 16 * (al & 7) + 8 * (g >> 1)) * 2;
#pragma unroll
  for (int t = 0; t < 4; t += 2) {
    const f4 o0 = *(LAS f4*)(other + t * 1024), o1 = *(LAS f4*)(other + (t + 1) * 1024);
    const unsigned a0 = pk(v[t][0] + f2{o0[0], o0[1]}), a1 = pk(v[t][1] + f2{o0[2], o0[3]});
    const unsigned b0 = pk(v[t + 1][0] + f2{o1[0], o1[1]}), b1 = pk(v[t + 1][1] + f2{o1[2], o1[3]});
    const auto s0 = __builtin_amdgcn_permlane16_swap(a0, b0, false, false), s1 = __builtin_amdgcn_permlane16_swap(a1, b1, false, false);
    st16(x.ws, so + t * (2 * 512 * 256), u4{s0[0], s1[0], s0[1], s1[1]});
  }
  if (LOADW) poison_o(x, 0);
  drain();
}

DI f2 cmul(f2 a, f2 b) { return f2{a.x, a.x} * b + f2{a.y, a.y} * f2{-b.y, b.x}; }
DI f2 cexp_p(float wr2, float wrev, float p) {
  const float e = __builtin_amdgcn_exp2f(wr2 * p);
  const float a = __builtin_amdgcn_fractf(wrev * p);
  return f2{e * __builtin_amdgcn_cosf(a), e * __builtin_amdgcn_sinf(a)};
}
constexpr int KG_ROW = 528;
constexpr int KG_A = 0;
constexpr int KG_X = 2 * 48 * KG_ROW;
constexpr int KG_T = LD_CONV_END;
constexpr int KG_B = KG_T + 2 * 2056 * 4;
static_assert(KG_X + 64 * 136 <= LD_KC && KG_B + 2 * 16 * KG_ROW <= LDS_BYTES, "prologue LDS map");

struct ProW { u2 a0, a1; unsigned short b[4]; };
struct ProIn { unsigned short lar, aim, cre, cim, ldt; u4 xt[2], xr[2]; ProW w[2]; unsigned epoch; };
DI void pro_load_w(const Params& p, int F, int lane, ProW& w) {
  const int fh = F / 48, fm = (F % 48) >> 4, fks = F & 15, row = lane & 15, kq = lane >> 4;
  w.a0 = *(const u2*)(p.wqkv + (32 * fks + row) * 1536 + fm * 512 + 16 * fh + 4 * kq);
  w.a1 = *(const u2*)(p.wqkv + (32 * fks + 16 + row) * 1536 + fm * 512 + 16 * fh + 4 * kq);
  const unsigned short* q = p.wphi + fh * 256 + (4 * kq) * 16 + row;
#pragma unroll
  for (int j = 0; j < 4; ++j) w.b[j] = q[16 * j];
}
DI void pro_fold_w(const Ctx& x, int F, const ProW& w) {
  const bool fold = ((F % 48) >> 4) < 2;
  const int dd = (x.lane & 15) - 4 * (x.lane >> 4);
  const u2 wb = fold ? u2{w.b[0] | ((unsigned)w.b[1] << 16), w.b[2] | ((unsigned)w.b[3] << 16)}
                     : u2{(dd == 0 ? 0x3f80u : 0u) | (dd == 1 ? 0x3f800000u : 0u), (dd == 2 ? 0x3f80u : 0u) | (dd == 3 ? 0x3f800000u : 0u)};
  const f4 d0 = mfma16(w.a0, wb, f4{0, 0, 0, 0}), d1 = mfma16(w.a1, wb, f4{0, 0, 0, 0});
  float e[8];
#pragma unroll
  for (int r = 0; r < 4; ++r) {
    const float b0 = dpp_f<0x150>(d0[r]), b1 = dpp_f<0x150>(d1[r]);
    e[r] = d0[r] - (fold ? b0 : 0.f); e[4 + r] = d1[r] - (fold ? b1 : 0.f);
  }
  u4 o = {pack2(e[0], e[1]), pack2(e[2], e[3]), pack2(e[4], e[5]), pack2(e[6], e[7])};
  if (fold && (x.lane & 15) == 0) o = u4{0x3f803f80u, 0x3f803f80u, 0x3f803f80u, 0x3f803f80u};
  st16(x.ws, WS_WFRAG + F * 1024 + x.lane * 16, o);
}
DI void pro_load(const Ctx& x, const Params& p, ProIn& in) {
#pragma unroll
  for (int i = 0; i < 2; ++i) {
    in.xt[i] = *(const u4*)(p.u + (64 * (x.c >> 3) + 32 * i + (x.tid >> 3)) * DM + 64 * (x.c & 7) + 8 * (x.tid & 7));
    in.xr[i] = *(const u4*)(p.u + (8 * x.c + 2 * x.w + i) * DM + 8 * x.lane);
  }
  in.epoch = __builtin_amdgcn_raw_buffer_load_b32(x.ws, WS_EPOCH, 0, AUX_SC1);
  const int d = 2 * x.cp + (x.tid >> 7), n = x.tid & 127;
  in.lar = p.lar[d * NST + n]; in.aim = p.aim[d * NST + n]; in.cre = p.cre[d * NST + n]; in.cim = p.cim[d * NST + n]; in.ldt = p.ldt[d];
  pro_load_w(p, 6 * x.c + x.w, x.lane, in.w[0]);
  pro_load_w(p, 6 * x.c + 4 + (x.w & 1), x.lane, in.w[1]);
  SB();
}
constexpr int CODE_SKIP = 5, CODE_KB = 40;
struct Warm { unsigned a, b, c; };
DI void pro_warm(const Ctx& x, const Params& p, const unsigned char* code, Warm& wm) {
  const int j = x.c >> 3;
  static_assert(CODE_KB * 8 <= 32 * 64 / 4 * 4, "one line per lane");
  wm.a = wm.b = wm.c = 0;
  if (x.w == 3) {
    const int line = j * (CODE_KB * 8 / 32) + x.lane;
    if (x.lane < CODE_KB * 8 / 32) wm.a = *(const GAS unsigned*)(code + CODE_SKIP * 1024 + line * 128);
    wm.b = *(const GAS unsigned*)((const unsigned char*)p.wo + (j * 128 + x.lane) * 128);
    wm.c = *(const GAS unsigned*)((const unsigned char*)p.wo + (j * 128 + 64 + x.lane) * 128);
  }
}
DI void pro_warm_retire(const Warm& wm) { asm volatile("" :: "v"(wm.a), "v"(wm.b), "v"(wm.c)); }
DI void pro_init(const Ctx& x) {
  const rsrc_t ws = x.ws;
  if (x.c < 2) { const int i = x.c * 256 + x.tid; if (i < CWI_END) __builtin_amdgcn_raw_buffer_store_b32(0u, ws, i * (CW_STRIDE * 4), 0, AUX_SC1); }
  if (x.c >= 8 && x.c < 12) st16(ws, WS_ST + 2048 * 8 + ((x.c - 8) * 256 + x.tid) * 16, u4{0, 0, 0, 0});
  static_assert(WS_ZP == WS_KVP + 2 * KVP_BYTES && (2 * KVP_BYTES + 2 * ZP_BYTES) == 256 * 272 * 16, "poisoned block");
  st16(ws, WS_KVP + (x.c * 272 + x.tid) * 16, u4{~0u, ~0u, ~0u, ~0u});
  if (x.tid < 16) st16(ws, WS_KVP + (x.c * 272 + 256 + x.tid) * 16, u4{~0u, ~0u, ~0u, ~0u});
}
DI void pro_publish_x(const Ctx& x, const ProIn& in) {
#pragma unroll
  for (int i = 0; i < 2; ++i) {
    lds_t* a = x.L + KG_X + (32 * i + (x.tid >> 3)) * 136 + (x.tid & 7) * 16;
    *(LAS u2*)a = u2{in.xt[i].x, in.xt[i].y}; *(LAS u2*)(a + 8) = u2{in.xt[i].z, in.xt[i].w};
  }
  {
    f2 a[2], q[2];
#pragma unroll
    for (int i = 0; i < 2; ++i) {
      a[i] = unpk(in.xr[i][0]); q[i] = a[i] * a[i];
#pragma unroll
      for (int k = 1; k < 4; ++k) { const f2 v = unpk(in.xr[i][k]); a[i] += v; q[i] += v * v; }
    }
    const float t = xrow4<2>(row_sum(a[0].x + a[0].y), row_sum(q[0].x + q[0].y), row_sum(a[1].x + a[1].y), row_sum(q[1].x + q[1].y));
    const int g = x.lane >> 4;
    if ((x.lane & 15) == 0)
      __builtin_amdgcn_raw_buffer_store_b32(__builtin_bit_cast(unsigned, t), x.ws, WS_ST + (g >> 1) * 8192 + (8 * x.c + 2 * x.w + (g & 1)) * 4, 0, AUX_SC1);
  }
  __syncthreads();
  const int g = x.lane >> 4, cl = x.lane & 15;
#pragma unroll
  for (int j = 0; j < 2; ++j) {
    lds_t* a = x.L + KG_X + (8 * (g + 4 * j) + (cl >> 2)) * 136 + (16 * x.w + 4 * (cl & 3)) * 2;
    st16(x.ws, WS_XT + ((64 * (x.c & 7) + 16 * x.w + cl) * XT_STRIDE + 64 * (x.c >> 3) + 8 * (g + 4 * j)) * 2, cat(lds_tr(a), lds_tr(a + 4 * 136)));
  }
}
DI void pro_tables(const Ctx& x, const ProIn& in) {
  const float dt = rbf(__expf(bf2f(in.ldt)));
  const float ar = -rbf(__expf(bf2f(in.lar)));
  const float wr2 = (ar * dt) * LOG2E, wrev = (bf2f(in.aim) * dt) * 0.15915494309189535f;
  const f2 c2 = {2.0f * (bf2f(in.cre) * dt), -2.0f * (bf2f(in.cim) * dt)};
  const int chl = x.tid >> 7, n = x.tid & 127;
  LAS unsigned* TA = (LAS unsigned*)(x.L + KG_A + chl * 48 * KG_ROW) + n;
  LAS unsigned* TB = (LAS unsigned*)(x.L + KG_B + chl * 16 * KG_ROW) + n;
  constexpr int RS = KG_ROW / 4;
  const f2 e8 = cexp_p(wr2, wrev, 8.f), e128 = cexp_p(wr2, -wrev, 128.f), e1 = cexp_p(wr2, -wrev, 1.f);
  f2 y = e8, h = cmul(c2, cexp_p(wr2, -wrev, 1024.f)), g[8], e[8];
  g[0] = c2; e[1] = e1; e[2] = cmul(e1, e1);
  TB[0] = 0x00003f80u;
#pragma unroll
  for (int i = 0; i < 8; ++i) {
    TB[(2 * i + 1) * RS] = pk(y);
    TA[(32 + i) * RS] = pk(h);
    const f2 y1 = cmul(y, e8), h1 = cmul(h, e128);
    if (i < 7) g[i + 1] = cmul(g[i], e128);
    if (i == 0) { e[3] = cmul(e[2], e[1]); e[4] = cmul(e[2], e[2]); }
    if (i == 1) { e[5] = cmul(e[4], e[1]); e[6] = cmul(e[3], e[3]); e[7] = cmul(e[4], e[3]); }
    if (i < 7) { TB[(2 * i + 2) * RS] = pk(y1); y = cmul(y1, e8); }
    h = h1;
  }
  TA[40 * RS] = pk(h);
#pragma unroll
  for (int k = 0; k < 8; ++k) {
    TA[k * RS] = pk(k ? cmul(g[0], e[k]) : g[0]);
    TA[(8 + k) * RS] = pk(k ? cmul(g[1], e[k]) : g[1]);
    if (k < 4) { TA[(16 + k) * RS] = pk(k ? cmul(g[2], e[2 * k]) : g[2]); TA[(20 + k) * RS] = pk(k ? cmul(g[3], e[2 * k]) : g[3]); }
    else { TA[(24 + 2 * (k - 4)) * RS] = pk(g[k]); TA[(25 + 2 * (k - 4)) * RS] = pk(cmul(g[k], e[4])); }
  }
}
DI void pro_kgemm(const Ctx& x) {
  const int ch = x.w >> 1, j = x.lane & 15, g = x.lane >> 4, odd = x.w & 1;
  unsigned ao = KG_A + (ch * 48 + 16 * odd + j) * KG_ROW + g * 16, a2o = KG_A + (ch * 48 + 32 + j) * KG_ROW + g * 16;
  unsigned bo = KG_B + (ch * 16 + j) * KG_ROW + g * 16;
  asm volatile("" : "+v"(ao), "+v"(a2o), "+v"(bo));
  u4 a0[8], a2[8], b[8];
#pragma unroll
  for (int ks = 0; ks < 8; ++ks) { b[ks] = *(LAS u4*)(x.L + bo + ks * 64); a0[ks] = *(LAS u4*)(x.L + ao + ks * 64); a2[ks] = *(LAS u4*)(x.L + a2o + ks * 64); }
  SB();
  f4 acc0 = {0, 0, 0, 0}, acc2 = acc0;
#pragma unroll
  for (int ks = 0; ks < 8; ++ks) { acc0 = mfma32(a0[ks], b[ks], acc0); acc2 = mfma32(a2[ks], b[ks], acc2); }
  const int pe = 128 * (g >> 1) + 4 * (g & 1), po = g < 2 ? 256 + 128 * g : 512 + 256 * (g - 2);
  const unsigned t0 = KG_T + (ch * 2056 + 8 * j + (odd ? po : pe)) * 4;
  const int s1 = odd ? (g < 2 ? 8 : 16) : 4, s2 = odd ? (g < 2 ? 16 : 512) : 8;
  *(LAS float*)(x.L + t0) = acc0[0]; *(LAS float*)(x.L + t0 + s1) = acc0[1];
  *(LAS float*)(x.L + t0 + s2) = acc0[2]; *(LAS float*)(x.L + t0 + s1 + s2) = acc0[3];
  const unsigned t2 = KG_T + (ch * 2056 + 1024 + 512 * g + 8 * j) * 4, dummy = KG_T + 2052 * 4;
#pragma unroll
  for (int r = 0; r < 4; ++r) *(LAS float*)(x.L + ((4 * g + r < 8 || (4 * g + r == 8 && j == 0)) ? t2 + 512 * r : dummy)) = acc2[r];
}
constexpr int KG_D = 0, KG_DCH = 4096 + 64;
DI void pro_expand(const Ctx& x) {
  unsigned tb = KG_T, db = KG_D + x.tid * 4;
  f2 l0 = {(float)(2047 - 2 * x.tid), (float)(2046 - 2 * x.tid)};
  asm volatile("" : "+v"(tb), "+v"(db), "+v"(l0));
#pragma unroll
  for (int i = 0; i < 4; ++i) {
    unsigned r = pk(l0 - (float)(512 * i));
    asm("" : "+v"(r));
    const unsigned a0 = tb + 4 * (int)blo(r), a1 = tb + 4 * (int)bhi(r);
#pragma unroll
    for (int ch = 0; ch < 2; ++ch)
      *(LAS unsigned*)(x.L + db + ch * KG_DCH + i * 1024) = pack2(*(LAS float*)(x.L + a0 + ch * (2056 * 4)), *(LAS float*)(x.L + a1 + ch * (2056 * 4)));
  }
}
DI void pro_take_k(const Ctx& x, u4 (&kd)[3]) {
  const int ch = x.tid >> 7, q = x.tid & 127;
  lds_t* a = x.L + KG_D + ch * KG_DCH + 32 * q;
  kd[0] = *(LAS u4*)a; kd[1] = *(LAS u4*)(a + 16); kd[2] = *(LAS u4*)(a + 32);
  const unsigned off = WS_KREV + ((2 * x.cp + ch) * 2048 + 16 * q) * 2;
  __builtin_amdgcn_raw_buffer_store_b128(kd[0], x.ws, off, 0, 0);
  __builtin_amdgcn_raw_buffer_store_b128(kd[1], x.ws, off + 16, 0, 0);
}
DI void pro_wait(const Ctx& x, unsigned flags, unsigned tag, unsigned first) {
  unsigned v = first;
  for (unsigned s = 0;; ++s) {
    const unsigned ok = __all(v == tag);
    if (x.lane == 0) ((LAS unsigned*)(x.L + LD_FLAG))[x.w] = ok;
    __syncthreads();
    const u4 r = *(LAS u4*)(x.L + LD_FLAG);
    if (r.x & r.y & r.z & r.w) break;
    if (s > SPIN_CAP) { if (x.tid == 0) spin_fail(x); break; }
    v = __builtin_amdgcn_raw_buffer_load_b32(x.ws, flags + x.tid * 64, 0, AUX_SC1);
    __syncthreads();
  }
}

DI u4 pro_peek_wave(const Ctx& x, unsigned flags) {
  u4 v;
#pragma unroll
  for (int i = 0; i < 4; ++i) v[i] = __builtin_amdgcn_raw_buffer_load_b32(x.ws, flags + (64 * i + x.lane) * 64, 0, AUX_SC1);
  return v;
}
DI void pro_wait_wave(const Ctx& x, unsigned flags, unsigned tag, u4 v) {
  for (unsigned s = 0; !__all(v.x == tag && v.y == tag && v.z == tag && v.w == tag); ++s) {
    if (s > SPIN_CAP) { spin_fail(x); break; }
    asm volatile("" ::: "memory");
    v = pro_peek_wave(x, flags);
  }
}

DI void q_wsum(const u4 (&wf)[48], f4 (&ws)[3]) {
  const u4 ones = {0x3f803f80u, 0x3f803f80u, 0x3f803f80u, 0x3f803f80u};
  ws[0] = f4{0, 0, 0, 0}; ws[1] = ws[0]; ws[2] = ws[0];
#pragma unroll
  for (int ks = 0; ks < 16; ++ks)
#pragma unroll
    for (int m = 0; m < 3; ++m) ws[m] = mfma32(wf[16 * m + ks], ones, ws[m]);
}
struct QDma { unsigned v0, v1, base, lds; int layer; unsigned char* wsp; };
DI QDma q_dma_setup(const Ctx& x) {
  const int row = x.lane >> 4, pc = x.lane & 15;
  return QDma{(unsigned)(row * 256 + ((pc ^ (row << 1)) * 16)), (unsigned)(row * 256 + ((pc ^ ((4 + row) << 1)) * 16)),
              WS_X1 + x.layer * X1_BYTES + x.rb * (512 * 256) + x.w * 4096, (unsigned)(LD_XT + x.w * 4096), x.layer, x.wsp};
}
template <int OFF> DI void dma2_sc1(rsrc_t r, const QDma& d) {
  unsigned t;
  asm volatile("s_add_u32 m0, %5, %6\n\ts_add_u32 %0, %4, %6\n\t"
               "buffer_load_dwordx4 %1, %3, %0 offen sc1 lds\n\tbuffer_load_dwordx4 %2, %3, %0 offen offset:1024 sc1 lds"
               : "=&s"(t) : "v"(d.v0), "v"(d.v1), "s"(r), "s"(d.base), "s"(d.lds), "n"(OFF) : "memory", "scc");
}
template <int OFF> DI void dma1_sc1(rsrc_t r, const QDma& d) {
  unsigned t;
  asm volatile("s_add_u32 m0, %4, %5\n\ts_add_u32 %0, %3, %5\n\tbuffer_load_dwordx4 %1, %2, %0 offen sc1 lds"
               : "=&s"(t) : "v"((OFF & 1024) ? d.v1 : d.v0), "s"(r), "s"(d.base), "s"(d.lds), "n"(OFF) : "memory", "scc");
}
struct QAddr { lds_t* lo[4]; lds_t* hi[4]; };
DI void q_frag_addr(const Ctx& x, QAddr& ba) {
  const int g = x.lane >> 4, q = (x.lane & 15) >> 2, pp = x.lane & 3, ph = x.w >> 1;
  const int r7 = 4 * (g & 1) + q;
#pragma unroll
  for (int pt = 0; pt < 4; ++pt) {
    const int lc = 8 * ph + 2 * (pt ^ (2 * (x.w & 1))) + (pp >> 1);
    unsigned o = LD_XT + (4 * g + q) * 256 + ((lc ^ (r7 << 1)) * 16) + 8 * (pp & 1), o2 = o + 65536;
    asm volatile("" : "+v"(o), "+v"(o2));
    ba.lo[pt] = x.L + o; ba.hi[pt] = x.L + o2;
  }
}
template <int KS> DI u4 q_ldb1(const QAddr& ba, int pt) {
  lds_t* p = (KS < 8 ? ba.lo[pt] : ba.hi[pt]) + (KS & 7) * 8192;
  return cat(lds_tr(p), lds_tr(p + 4096));
}
template <int KS> DI void q_ldb(const QAddr& ba, u4 (&b)[4]) {
#pragma unroll
  for (int pt = 0; pt < 4; ++pt) b[pt] = q_ldb1<KS>(ba, pt);
}
template <int N> DI void wait_vm_barrier() { asm volatile("s_waitcnt vmcnt(%0)\n\ts_barrier" :: "n"(N) : "memory"); }

DI f4 bcast4(float r) {
  const f2 xy = swap16(r, r), ab = swap32(xy.x, xy.x), cd = swap32(xy.y, xy.y);
  return f4{ab.x, ab.y, cd.x, cd.y};
}
template <class F> DI void softmax4(const f4 (&t)[4], const float (&cs)[4], float csr, lds_t* scr_w, lds_t* scr_r, u4 (&out)[4], F mid) {
  float mx[4], mn[4];
#pragma unroll
  for (int pt = 0; pt < 4; ++pt) {
    mx[pt] = fmax2(fmax2(fmax2(t[pt][0], t[pt][1]), t[pt][2]), t[pt][3]);
    mn[pt] = fmin2(fmin2(fmin2(t[pt][0], t[pt][1]), t[pt][2]), t[pt][3]);
  }
  const f2 M = xrow4_maxmin(mx[0], mx[1], mx[2], mx[3], mn[0], mn[1], mn[2], mn[3]);
  *(LAS f2*)scr_w = M * f2{-csr, csr};
  f2 bb[4];
#pragma unroll
  for (int pt = 0; pt < 4; ++pt) bb[pt] = *(LAS f2*)(scr_r + 128 * pt);
  SB();
  mid();
  SB();
  f2 ep[4][2], en[4][2];
  float sp[4], sn[4];
#pragma unroll
  for (int pt = 0; pt < 4; ++pt) {
    const f2 b = bb[pt];
    const f2 t0 = {t[pt][0], t[pt][1]}, t1 = {t[pt][2], t[pt][3]};
    const f2 p0 = t0 * cs[pt] + b.x, p1 = t1 * cs[pt] + b.x, n0 = b.y - t0 * cs[pt], n1 = b.y - t1 * cs[pt];
    ep[pt][0] = f2{__builtin_amdgcn_exp2f(p0.x), __builtin_amdgcn_exp2f(p0.y)}; ep[pt][1] = f2{__builtin_amdgcn_exp2f(p1.x), __builtin_amdgcn_exp2f(p1.y)};
    en[pt][0] = f2{__builtin_amdgcn_exp2f(n0.x), __builtin_amdgcn_exp2f(n0.y)}; en[pt][1] = f2{__builtin_amdgcn_exp2f(n1.x), __builtin_amdgcn_exp2f(n1.y)};
    const f2 a = ep[pt][0] + ep[pt][1], c = en[pt][0] + en[pt][1];
    sp[pt] = a.x + a.y; sn[pt] = c.x + c.y;
  }
  const f4 ip = bcast4(__builtin_amdgcn_rcpf(xrow4<2>(sp[0], sp[1], sp[2], sp[3])));
  const f4 in = bcast4(__builtin_amdgcn_rcpf(xrow4<2>(sn[0], sn[1], sn[2], sn[3])));
#pragma unroll
  for (int pt = 0; pt < 4; ++pt)
    out[pt] = u4{pk(ep[pt][0] * ip[pt]), pk(ep[pt][1] * ip[pt]), pk(en[pt][0] * in[pt]), pk(en[pt][1] * in[pt])};
}

struct QState { u4 pq[4]; u2 res[4]; u4 pk8[8], az; };
DI void o_load_partials(const Ctx& x, QState& st) {
  const int h = x.w & 1, ph = x.w >> 1, hh = 2 * x.hp + h;
  const unsigned ko = WS_KVP + x.layer * KVP_BYTES + (hh * 16 + 8 * ph) * 1024 + x.lane * 16;
#pragma unroll
  for (int i = 0; i < 8; ++i) st.pk8[i] = ld16(x.ws, ko + i * 1024);
  st.az = ld16(x.ws, WS_ZP + x.layer * ZP_BYTES + (hh * 16 + (x.lane & 15)) * 64 + (x.lane >> 4) * 16);
}

template <int KS> struct QPass1 {
  static DI void run(rsrc_t ws, const QDma& d, const QAddr& ba, const u4 (&wf)[48], u4 (&b)[4], f4 (&ak)[4], f4 (&av)[4], f4 (&ag)[2]) {
    if (KS < 15 && (KS & 1)) wait_vm_barrier<(KS < 15 ? 8 + 2 * (KS < 10 ? KS : 10) - 4 * ((KS + 1) / 2) : 0)>();
    const u4 g0 = b[0], g1 = b[1];
#pragma unroll
    for (int pt = 0; pt < 4; ++pt) {
      const u4 cur = b[pt];
      lds_t* p = ((KS + 1) < 8 ? ba.lo[pt] : ba.hi[pt]) + ((KS + 1) & 7) * 8192;
      u2 t0, t1;
      if (KS == 0) mfma_va0(ak[pt], wf[16 + KS], cur); else mfma_va(ak[pt], wf[16 + KS], cur);
      if (KS < 15) t0 = lds_tr(p);
      SB();
      if (KS == 0) mfma_va0(av[pt], wf[32 + KS], cur); else mfma_va(av[pt], wf[32 + KS], cur);
      if (KS < 15) { t1 = lds_tr(p + 4096); b[pt] = cat(t0, t1); }
      SB();
    }
    if (KS == 0) mfma_vv0(ag[0], g0, g0); else mfma_vv(ag[0], g0, g0);
    SB();
    if (KS < 10) dma1_sc1<(3 + KS / 2) * 16384 + (KS & 1) * 2048>(ws, d);
    SB();
    if (KS == 0) mfma_vv0(ag[1], g1, g1); else mfma_vv(ag[1], g1, g1);
    SB();
    if (KS < 10) dma1_sc1<(3 + KS / 2) * 16384 + (KS & 1) * 2048 + 1024>(ws, d);
    SB();
    QPass1<KS + 1>::run(ws, d, ba, wf, b, ak, av, ag);
  }
};
template <> struct QPass1<16> { static DI void run(rsrc_t, const QDma&, const QAddr&, const u4 (&)[48], u4 (&)[4], f4 (&)[4], f4 (&)[4], f4 (&)[2]) {} };
template <int KS> struct QPass2 {
  static DI void run(const QAddr& ba, const u4 (&wf)[48], u4 (&b)[3][4], f4 (&aq)[4]) {
#pragma unroll
    for (int pt = 0; pt < 4; ++pt) {
      if (KS == 0) mfma_va0(aq[pt], wf[KS], b[0][pt]); else mfma_va(aq[pt], wf[KS], b[KS % 3][pt]);
      if (KS < 14) b[(KS + 2) % 3][pt] = q_ldb1<(KS < 14 ? KS + 2 : 15)>(ba, pt);
      SB();
    }
    QPass2<KS + 1>::run(ba, wf, b, aq);
  }
};
template <> struct QPass2<16> { static DI void run(const QAddr&, const u4 (&)[48], u4 (&)[3][4], f4 (&)[4]) {} };

DI void o_dma_wo(const Ctx& x, const Params& p) {
  const rsrc_t r = make_rsrc(p.wo, 512 * 512 * 2);
  const int row = x.lane >> 3, pc = x.lane & 7, cb = x.hp >> 1;
  const int b1 = (row >> 1) & 1;
  const unsigned v0 = row * 1024 + ((pc ^ (b1 << 1)) * 16), v1 = row * 1024 + ((pc ^ ((b1 | 2) << 1)) * 16) + 7168;
#pragma unroll 1
  for (int i = 4 * x.w; i < 4 * x.w + 4; ++i) dma4(r, v0, v1, v0 + 2 * 7168, v1 + 2 * 7168, i * 32768 + cb * 128, LD_XT + i * 4096);
}

DI void q_phase(const Ctx& x, const Params& p, const u4 (&wf)[48], const f4 (&wsum)[3], QState& st) {
  const QDma dm = q_dma_setup(x);
  dma2_sc1<0>(x.ws, dm); dma2_sc1<2048>(x.ws, dm); dma2_sc1<16384>(x.ws, dm); dma2_sc1<16384 + 2048>(x.ws, dm);
  dma2_sc1<32768>(x.ws, dm); dma2_sc1<32768 + 2048>(x.ws, dm);
  QAddr ba;
  q_frag_addr(x, ba);
  const int h = x.w & 1, ph = x.w >> 1, g = x.lane >> 4, cl = x.lane & 15;
  f4 ak[4], av[4], ag[2];
  STAMP(x.wsp, 9 + 16 * x.layer);
  {
    u4 b[4];
    wait_vm_barrier<8>();
    q_ldb<0>(ba, b);
    SB();
    QPass1<0>::run(x.ws, dm, ba, wf, b, ak, av, ag);
    mfma_done();
  }
  STAMP(x.wsp, 10 + 16 * x.layer);
  {
    const int r = cl & 3;
    const unsigned so = LD_STAT + 4 * (64 * ph + 32 * h + cl);
    const unsigned a0 = g == 0 ? so : LD_STAT + 1024, a1 = g == (cl >> 2) ? so + 512 : LD_STAT + 1024;
#pragma unroll
    for (int i = 0; i < 2; ++i) {
      const float dq = r == 0 ? ag[i][0] : r == 1 ? ag[i][1] : r == 2 ? ag[i][2] : ag[i][3];
      *(LAS float*)(x.L + a0 + 64 * i) = ak[i][0];
      *(LAS float*)(x.L + a1 + 64 * i) = dq;
    }
  }
  __syncthreads();
  float mean[4], rs[4], cs[4];
#pragma unroll
  for (int pt = 0; pt < 4; ++pt) {
    LAS float* S = (LAS float*)(x.L + LD_STAT) + 64 * ph + 16 * (pt ^ (2 * h)) + cl;
    mean[pt] = S[0] * (1.f / DM);
    rs[pt] = __builtin_amdgcn_rsqf(__builtin_fabsf((S[128] * (1.f / DM) + LN_EPS) - mean[pt] * mean[pt]));
    cs[pt] = rs[pt] * LOG2E;
  }
  const float csr = g == 0 ? cs[0] : g == 1 ? cs[2] : g == 2 ? cs[1] : cs[3];
  lds_t* scr_r = x.L + LD_SCR + x.w * 512 + cl * 8;
  lds_t* scr_w = scr_r + 128 * ((g >> 1) | ((g & 1) << 1));
  lds_t* pv = x.L + LD_PV + x.w * PV_WAVE;
  {
    f4 t[4];
#pragma unroll
    for (int pt = 0; pt < 4; ++pt) t[pt] = ak[pt] - mean[pt] * wsum[1];
    u4 pk4[4];
    softmax4(t, cs, csr, scr_w, scr_r, pk4, [&]() {
#pragma unroll
      for (int pt = 0; pt < 4; ++pt) {
        const f4 v = (av[pt] - mean[pt] * wsum[2]) * rs[pt];
        *(LAS u2*)(pv + (16 * pt + cl) * PV_ROW + 8 * g + 64) = u2{pack2(v[0], v[1]), pack2(v[2], v[3])};
      }
    });
#pragma unroll
    for (int pt = 0; pt < 4; ++pt) {
      lds_t* row = pv + (16 * pt + cl) * PV_ROW + 8 * g;
      *(LAS u2*)(row) = u2{pk4[pt].x, pk4[pt].y}; *(LAS u2*)(row + 32) = u2{pk4[pt].z, pk4[pt].w};
    }
  }
  f4 kv0 = {0, 0, 0, 0}, kv1 = kv0, z0 = kv0, z1 = kv0;
  {
    const u4 ones = {0x3f803f80u, 0x3f803f80u, 0x3f803f80u, 0x3f803f80u};
    const int q = cl >> 2, pp = cl & 3;
#pragma unroll
    for (int k2 = 0; k2 < 2; ++k2) {
      lds_t* a = pv + (32 * k2 + 8 * g + q) * PV_ROW + 8 * pp;
      const u4 a0 = cat(lds_tr(a), lds_tr(a + 4 * PV_ROW));
      const u4 a1 = cat(lds_tr(a + 32), lds_tr(a + 4 * PV_ROW + 32));
      const u4 bv = cat(lds_tr(a + 64), lds_tr(a + 4 * PV_ROW + 64));
      kv0 = mfma32(a0, bv, kv0); kv1 = mfma32(a1, bv, kv1); z0 = mfma32(a0, ones, z0); z1 = mfma32(a1, ones, z1);
    }
  }
  STAMP(x.wsp, 11 + 16 * x.layer);
  {
    lds_t* mine = x.L + LD_PV + x.w * PV_WAVE + x.lane * 16;
    if (ph == 1) { *(LAS f4*)(mine) = kv0; *(LAS f4*)(mine + 1024) = kv1; *(LAS f4*)(mine + 2048) = z0; *(LAS f4*)(mine + 3072) = z1; }
    __syncthreads();
    if (ph == 0) {
      lds_t* o = x.L + LD_PV + (x.w + 2) * PV_WAVE + x.lane * 16;
      kv0 += *(LAS f4*)(o); kv1 += *(LAS f4*)(o + 1024); z0 += *(LAS f4*)(o + 2048); z1 += *(LAS f4*)(o + 3072);
      const int hr = (2 * x.hp + h) * 16 + x.rb;
      st16(x.ws, WS_KVP + x.layer * KVP_BYTES + hr * 1024 + x.lane * 16,
           u4{pack2(kv0[0], kv0[1]), pack2(kv0[2], kv0[3]), pack2(kv1[0], kv1[1]), pack2(kv1[2], kv1[3])});
      if (cl == 0) st16(x.ws, WS_ZP + x.layer * ZP_BYTES + hr * 64 + g * 16,
                        u4{pack2(z0[0], z0[1]), pack2(z0[2], z0[3]), pack2(z1[0], z1[1]), pack2(z1[2], z1[3])});
    }
  }
  STAMP(x.wsp, 12 + 16 * x.layer);
  {
    {
      const int cb = x.hp >> 1, q = cl >> 2, pos = 64 * (x.hp & 1) + 16 * x.w + 4 * (cl & 3);
      const int row = 64 * cb + 4 * g + q;
      unsigned ro = LD_XT + row * 256 + (((pos >> 3) ^ ((row & 7) << 1)) * 16) + (pos & 4) * 2;
      asm volatile("" : "+v"(ro));
#pragma unroll
      for (int ct = 0; ct < 4; ++ct) st.res[ct] = lds_tr(x.L + ro + ct * 4096);
    }
    f4 aq[4];
    u4 b[3][4];
    q_ldb<0>(ba, b[0]); q_ldb<1>(ba, b[1]);
    SB();
    QPass2<0>::run(ba, wf, b, aq);
    mfma_done();
    f4 t[4];
#pragma unroll
    for (int pt = 0; pt < 4; ++pt) t[pt] = aq[pt] - mean[pt] * wsum[0];
    softmax4(t, cs, csr, scr_w, scr_r, st.pq, [&]() { o_load_partials(x, st); });
  }
  STAMP(x.wsp, 13 + 16 * x.layer);
}

DI void o_compute(const Ctx& x, QState& st) {
  const int h = x.w & 1, ph = x.w >> 1, g = x.lane >> 4, cl = x.lane & 15, hh = 2 * x.hp + h;
  STAMP(x.wsp, 14 + 16 * x.layer);
  const unsigned one = g == (cl >> 2) ? ((cl & 1) ? 0x3f800000u : 0x3f80u) : 0u;
  const unsigned e0 = (cl & 2) ? 0u : one, e1 = (cl & 2) ? one : 0u;
  const u4 sel0 = {e0, e1, 0u, 0u}, sel1 = {0u, 0u, e0, e1};
  f4 k0, k1, den[4];
  for (unsigned spin = 0;; ++spin) {
    k0 = f4{0, 0, 0, 0}; k1 = k0;
#pragma unroll
    for (int i = 0; i < 8; ++i) { k0 = mfma32(sel0, st.pk8[i], k0); k1 = mfma32(sel1, st.pk8[i], k1); }
    const u2 ones = {0x3f803f80u, 0x3f803f80u};
    f4 dp[4];
#pragma unroll
    for (int pt = 0; pt < 4; ++pt) dp[pt] = mfma32(st.az, st.pq[pt], f4{0, 0, 0, 0});
#pragma unroll
    for (int pt = 0; pt < 4; ++pt) den[pt] = mfma16(ones, u2{pack2(dp[pt][0], dp[pt][1]), pack2(dp[pt][2], dp[pt][3])}, f4{1e-6f, 1e-6f, 1e-6f, 1e-6f});
    const float chk = (k0[0] + k1[0]) + den[0][0];
    if (!__any(chk != chk)) break;
    if (spin > 4096u) { spin_fail(x); break; }
    o_load_partials(x, st);
  }
  lds_t* mine = x.L + LD_PV + x.w * PV_WAVE + (x.w >> 1) * 4096 + x.lane * 16;
  *(LAS f4*)(mine) = k0; *(LAS f4*)(mine + 1024) = k1;
  __syncthreads();
  STAMP(x.wsp, 15 + 16 * x.layer);
  lds_t* o = x.L + LD_PV + (x.w ^ 2) * PV_WAVE + ((x.w ^ 2) >> 1) * 4096 + x.lane * 16;
  k0 += *(LAS f4*)(o); k1 += *(LAS f4*)(o + 1024);
  const u4 akv = {pack2(k0[0], k0[1]), pack2(k0[2], k0[3]), pack2(k1[0], k1[1]), pack2(k1[2], k1[3])};
  f4 num[4];
#pragma unroll
  for (int pt = 0; pt < 4; ++pt) num[pt] = mfma32(akv, st.pq[pt], f4{0, 0, 0, 0});
  const unsigned oo = WS_OB + x.layer * OB_BYTES + ((2 * x.rb + ph) * 32 + hh) * 2048 + (16 * ((g & 1) ^ (2 * h)) + cl) * 32 + 16 * (g >> 1);
#pragma unroll
  for (int pt = 0; pt < 4; pt += 2) {
    const f4 va = num[pt] * __builtin_amdgcn_rcpf(den[pt][0]), vb = num[pt + 1] * __builtin_amdgcn_rcpf(den[pt + 1][0]);
    const auto s0 = __builtin_amdgcn_permlane16_swap(pack2(va[0], va[1]), pack2(vb[0], vb[1]), false, false);
    const auto s1 = __builtin_amdgcn_permlane16_swap(pack2(va[2], va[3]), pack2(vb[2], vb[3]), false, false);
    st16(x.ws, oo ^ (pt * 512), u4{s0[0], s1[0], s0[1], s1[1]});
  }
}

struct OAddr { lds_t* p[4]; };
template <int KS> DI void o_ldw(const OAddr& wb, u4 (&b)[4]) {
#pragma unroll
  for (int ct = 0; ct < 4; ++ct) b[ct] = cat(lds_tr(wb.p[ct] + KS * 4096), lds_tr(wb.p[ct] + KS * 4096 + 512));
}
DI unsigned umax3(unsigned a, unsigned b, unsigned c) { const unsigned m = a > b ? a : b; return m > c ? m : c; }
template <int KS> struct OGemm {
  static DI void run(const OAddr& wb, const u4 (&of)[16], u4 (&b)[3][4], f4 (&acc)[4], unsigned& top) {
#pragma unroll
    for (int ct = 0; ct < 4; ++ct) {
      if (KS == 0) mfma_vv0(acc[ct], b[0][ct], of[KS]); else mfma_vv(acc[ct], b[KS % 3][ct], of[KS]);
      if (KS < 14) b[(KS + 2) % 3][ct] = cat(lds_tr(wb.p[ct] + (KS + 2) * 4096), lds_tr(wb.p[ct] + (KS + 2) * 4096 + 512));
      if (ct == 1) top = umax3(top, of[KS].x, of[KS].y);
      if (ct == 3) top = umax3(top, of[KS].z, of[KS].w);
      SB();
    }
    OGemm<KS + 1>::run(wb, of, b, acc, top);
  }
};
template <> struct OGemm<16> { static DI void run(const OAddr&, const u4 (&)[16], u4 (&)[3][4], f4 (&)[4], unsigned&) {} };

DI void o_gemm(const Ctx& x, const Params& p, const QState& st, u4 (&kd)[3]) {
  const int g = x.lane >> 4, cl = x.lane & 15, pb = 2 * x.rb + (x.hp & 1), cb = x.hp >> 1;
  const unsigned oo = WS_OB + x.layer * OB_BYTES + ((pb * 32 + (g >> 1)) * 64 + 16 * x.w + cl) * 32 + 16 * (g & 1);
  OAddr wb;
  {
    const int q = cl >> 2, pp = cl & 3, f = ((q >> 1) & 1) | ((g & 1) << 1);
#pragma unroll
    for (int ct = 0; ct < 4; ++ct) wb.p[ct] = x.L + LD_XT + (8 * g + q) * 128 + (((2 * ct + (pp >> 1)) ^ (f << 1)) * 16) + 8 * (pp & 1);
  }
  f4 acc[4];
  o_dma_wo(x, p);
  for (unsigned spin = 0;; ++spin) {
    u4 of[16];
#pragma unroll
    for (int ks = 0; ks < 16; ++ks) of[ks] = ld16(x.ws, oo + ks * 4096);
    if (spin == 0) wait_vm_barrier<16>();
    STAMP(x.wsp, 17 + 16 * x.layer);
    u4 b[3][4];
    o_ldw<0>(wb, b[0]); o_ldw<1>(wb, b[1]);
    SB();
    unsigned top = 0;
    OGemm<0>::run(wb, of, b, acc, top);
    mfma_done();
    if (!__any(top == ~0u)) break;
    if (spin > 4096u) { spin_fail(x); break; }
  }
  STAMP(x.wsp, 18 + 16 * x.layer);
  f2 s2 = {0.f, 0.f}, q2 = s2;
  unsigned so = LD_OST + (16 * x.w + cl) * 144 + 8 * g;
  asm volatile("" : "+v"(so));
#pragma unroll
  for (int ct = 0; ct < 4; ++ct) {
    const f2 a = unpk(st.res[ct].x) + f2{acc[ct][0], acc[ct][1]}, b2 = unpk(st.res[ct].y) + f2{acc[ct][2], acc[ct][3]};
    s2 += a + b2; q2 += a * a; q2 += b2 * b2;
    *(LAS u2*)(x.L + so + 32 * ct) = u2{pk(a), pk(b2)};
  }
  if (x.layer == 0) {
    const f2 sq = xrow_sum2(s2.x + s2.y, q2.x + q2.y);
    if (g < 2) {
      GAS float* d = (GAS float*)(p.ws + WS_ST + 2048 * 8) + g * 2048 + 64 * pb + 16 * x.w + cl;
      __hip_atomic_fetch_add(d, g ? sq.y : sq.x, RLX_AGENT);
    }
    conv_load_k(x, kd);
  }
  __syncthreads();
  if (x.layer == 0) {
    const int qq = cl >> 2, pp = cl & 3;
#pragma unroll
    for (int j = 0; j < 2; ++j) {
      lds_t* a = x.L + LD_OST + (8 * (g + 4 * j) + qq) * 144 + (16 * x.w + 4 * pp) * 2;
      const u4 v = cat(lds_tr(a), lds_tr(a + 4 * 144));
      st16(x.ws, WS_XT + XT_BYTES + ((64 * cb + 16 * x.w + cl) * XT_STRIDE + 64 * pb + 8 * (g + 4 * j)) * 2, v);
    }
  } else {
#pragma unroll
    for (int i = 0; i < 2; ++i) {
      const int k = i * 256 + x.tid, pos = k >> 3, q8 = k & 7;
      *(GAS u4*)(p.out + (64 * pb + pos) * DM + 64 * cb + 8 * q8) = *(LAS u4*)(x.L + LD_OST + pos * 144 + q8 * 16);
    }
  }
  STAMP(x.wsp, 19 + 16 * x.layer);
}

DI Ctx make_ctx(const Params& p, unsigned char* smem) {
  Ctx x;
  x.L = (lds_t*)smem; x.ws = make_rsrc(p.ws, WS_END); x.cw = (GAS unsigned*)(p.ws + WS_CTRL); x.wsp = p.ws;
  x.tid = threadIdx.x; x.lane = x.tid & 63; x.w = __builtin_amdgcn_readfirstlane(x.tid >> 6); x.c = blockIdx.x;
  x.cp = (x.c & 7) * 32 + (x.c >> 3);
  x.rb = 2 * (x.c & 7) + (x.c >> 7); x.hp = (x.c >> 3) & 15; x.layer = 0;
  return x;
}

DI Ctx fresh(const Ctx& x, int layer) { Ctx y = x; int t = x.tid; asm volatile("" : "+v"(t)); y.tid = t; y.lane = t & 63; y.layer = layer; return y; }

__global__ void __launch_bounds__(256, 1) k_main(Params p) {
  extern __shared__ __attribute__((aligned(16))) unsigned char smem[];
  const unsigned char* code0;
  asm volatile("s_getpc_b64 %0" : "=s"(code0));
  const Ctx x0 = make_ctx(p, smem);
  STAMP(p.ws, 0);
  u4 wf[48];
  f4 wsum[3];
  const float dsk = bf2f(p.dsk[2 * x0.cp + (x0.w >> 1)]);
  unsigned tag;
  {
    const Ctx x = fresh(x0, 0);
    u4 kd[3], a[32];
    ProIn pin;
    pro_load(x, p, pin);
    pro_init(x);
    tag = pin.epoch + 1u;
    pro_publish_x(x, pin);
    SB();
    pro_fold_w(x, 6 * x.c + x.w, pin.w[0]);
    if (x.w < 2) { pro_fold_w(x, 6 * x.c + 4 + x.w, pin.w[1]); asm volatile("s_waitcnt vmcnt(2)" ::: "memory"); }
    else asm volatile("s_waitcnt vmcnt(1)" ::: "memory");
    __syncthreads();
    if (x.tid == 0) __builtin_amdgcn_raw_buffer_store_b32(tag, x.ws, WS_FLAG + x.c * 64, 0, AUX_SC1);
    STAMP(p.ws, 1);
    pro_tables(x, pin);
    drain();
    __syncthreads();
    if (x.tid == 0) __builtin_amdgcn_raw_buffer_store_b32(tag, x.ws, WS_FLAG2 + x.c * 64, 0, AUX_SC1);
    STAMP(p.ws, 2);
    const unsigned fl = __builtin_amdgcn_raw_buffer_load_b32(x.ws, WS_FLAG + x.tid * 64, 0, AUX_SC1);
    pro_kgemm(x);
    pro_wait(x, WS_FLAG, tag, fl);
    STAMP(p.ws, 3);
    ConvIn in;
    conv_load_in(x, 0, in);
    SB();
    pro_expand(x);
    __syncthreads();
    pro_take_k(x, kd);
    conv_build_k(x, kd);
    STAMP(p.ws, 4);
    conv_build_u(x, in);
    const u4 fl2 = pro_peek_wave(x, WS_FLAG2);
    __syncthreads();
    STAMP(p.ws, 5);
    pro_wait_wave(x, WS_FLAG2, tag, fl2);
    STAMP3(x, 36);
    Warm wm;
    pro_warm(x, p, code0, wm);
    if (x.w & 1) conv_wave<1, true>(x, dsk, a); else conv_wave<0, true>(x, dsk, a);
    pro_warm_retire(wm);
    a2a_post(fresh(x0, 0), 0);
    poison_o(fresh(x0, 0), 1);
    q_take_w(fresh(x0, 0), wf);
    q_wsum(wf, wsum);
  }
#pragma unroll 1
  for (int layer = 0; layer < 2; ++layer) {
    Ctx x = fresh(x0, layer);
    a2a_wait(x, 2 * layer);
    STAMP(p.ws, 8 + 16 * layer);
    QState st;
    x = fresh(x0, layer);
    q_phase(x, p, wf, wsum, st);
    x = fresh(x0, layer);
    o_compute(x, st);
    STAMP(p.ws, 16 + 16 * layer);
    x = fresh(x0, layer);
    u4 kd[3];
    o_gemm(x, p, st, kd);
    if (layer == 0) {
      u4 a[32];
      conv_build_k(x, kd);
      a2a_signal(x, 1);
      x = fresh(x0, 1);
      if (x.w & 1) conv_load_a<1>(x, a); else conv_load_a<0>(x, a);
      SB();
      a2a_wait(x, 1);
      STAMP(p.ws, 20);
      ConvIn in;
      conv_load_in(x, 1, in);
      conv_build_u(x, in);
        __syncthreads();
      STAMP(p.ws, 21);
      if (x.w & 1) conv_wave<1, false>(x, dsk, a); else conv_wave<0, false>(x, dsk, a);
      a2a_post(fresh(x0, 1), 2);
    }
  }
  if (x0.c == 0 && x0.tid == 0) __builtin_amdgcn_raw_buffer_store_b32(tag, x0.ws, WS_EPOCH, 0, AUX_SC1);
}

extern "C" void kernel_launch(void* const* d_in, const int* in_sizes, int n_in, void* d_out, int out_size, void* d_ws, size_t ws_size,
                              hipStream_t stream) {
  Params p;
  p.u = (const unsigned short*)d_in[0]; p.lar = (const unsigned short*)d_in[1]; p.aim = (const unsigned short*)d_in[2];
  p.cre = (const unsigned short*)d_in[3]; p.cim = (const unsigned short*)d_in[4]; p.ldt = (const unsigned short*)d_in[5];
  p.dsk = (const unsigned short*)d_in[6]; p.wqkv = (const unsigned short*)d_in[8]; p.wphi = (const unsigned short*)d_in[9];
  p.wo = (const unsigned short*)d_in[10]; p.out = (unsigned short*)d_out; p.ws = (unsigned char*)d_ws;
  (void)hipFuncSetAttribute((const void*)k_main, hipFuncAttributeMaxDynamicSharedMemorySize, LDS_BYTES);
  k_main<<<256, 256, LDS_BYTES, stream>>>(p);
}
```

```cpp
#include <hip/hip_runtime.h>
#include <hip/hip_bf16.h>
#include <stdint.h>

#define MK_STAMP 0
#define STAMP(ws, i) do { } while (0)
#define STAMP2(x, i) do { } while (0)
#define STAMP3(x, i) do { } while (0)
typedef short s4 __attribute__((ext_vector_type(4)));
typedef short s8 __attribute__((ext_vector_type(8)));
typedef float f2 __attribute__((ext_vector_type(2)));
typedef float f4 __attribute__((ext_vector_type(4)));
typedef unsigned u2 __attribute__((ext_vector_type(2)));
typedef unsigned u4 __attribute__((ext_vector_type(4)));
typedef __bf16 bf2 __attribute__((ext_vector_type(2)));
#define GAS __attribute__((address_space(1)))
#define LAS __attribute__((address_space(3)))
#define DI __device__ __forceinline__
#define RLX_AGENT __ATOMIC_RELAXED, __HIP_MEMORY_SCOPE_AGENT
typedef GAS unsigned gu32;
typedef LAS unsigned char lds_t;
typedef __amdgpu_buffer_rsrc_t rsrc_t;

constexpr int SEQ = 2048, DM = 512, NST = 128, NH = 32;
constexpr float LOG2E = 1.4426950408889634f;
constexpr float LN_EPS = 1e-5f;
constexpr int AUX_SC1 = 16;

constexpr int XT_STRIDE = 2112;
constexpr unsigned WS_CTRL = 0;
constexpr unsigned WS_KREV = 262144;
constexpr unsigned WS_WFRAG = WS_KREV + 512u * 2048 * 2;
constexpr unsigned WS_XT = WS_WFRAG + 32u * 3 * 16 * 1024;
constexpr unsigned XT_BYTES = 512u * XT_STRIDE * 2;
constexpr unsigned WS_ST = WS_XT + 2 * XT_BYTES;
constexpr unsigned WS_X1 = WS_ST + 2u * 2048 * 8;
constexpr unsigned X1_BYTES = 2048u * 512 * 2;
constexpr unsigned WS_KVP = WS_X1 + 2 * X1_BYTES;
constexpr unsigned KVP_BYTES = 32u * 16 * 1024;
constexpr unsigned WS_ZP = WS_KVP + 2 * KVP_BYTES;
constexpr unsigned ZP_BYTES = 32u * 16 * 64;
constexpr unsigned WS_OB = WS_ZP + 2 * ZP_BYTES;
constexpr unsigned OB_BYTES = 2048u * 512 * 2;
constexpr unsigned WS_END = WS_OB + 2 * OB_BYTES;
constexpr int CW_TMO = 0;
#define A2A_NS 16
#define A2A_REP 8
constexpr int NS = A2A_NS, REP = A2A_REP;
constexpr int NCW_A2A = 3 * REP * NS;
constexpr int CW_STRIDE = 64;
constexpr int CWI_A2A = 1, CWI_END = CWI_A2A + NCW_A2A;
constexpr unsigned WS_EPOCH = 131072, WS_FLAG = WS_EPOCH + 256, WS_FLAG2 = WS_FLAG + 256 * 64;
static_assert(CWI_END <= 512 && CWI_END * CW_STRIDE * 4 <= WS_EPOCH && WS_FLAG2 + 256 * 64 <= WS_KREV, "control block");

struct Params {
  const unsigned short* u;
  const unsigned short* lar;
  const unsigned short* aim;
  const unsigned short* cre;
  const unsigned short* cim;
  const unsigned short* ldt;
  const unsigned short* dsk;
  const unsigned short* wqkv;
  const unsigned short* wphi;
  const unsigned short* wo;
  unsigned short* out;
  unsigned char* ws;
};

DI float bf2f(unsigned short b) { return __builtin_bit_cast(float, (unsigned)b << 16); }
DI float blo(unsigned x) { return __builtin_bit_cast(float, x << 16); }
DI float bhi(unsigned x) { return __builtin_bit_cast(float, x & 0xffff0000u); }
DI unsigned pack2(float a, float b) { bf2 v = {(__bf16)a, (__bf16)b}; return __builtin_bit_cast(unsigned, v); }
DI float rbf(float a) { return blo(pack2(a, a)); }
DI f4 mfma32(u4 a, u4 b, f4 c) { return __builtin_amdgcn_mfma_f32_16x16x32_bf16(__builtin_bit_cast(s8, a), __builtin_bit_cast(s8, b), c, 0, 0, 0); }
DI f4 mfma16(u2 a, u2 b, f4 c) { return __builtin_amdgcn_mfma_f32_16x16x16bf16_1k(__builtin_bit_cast(s4, a), __builtin_bit_cast(s4, b), c, 0, 0, 0); }
DI void mfma_va(f4& acc, const u4& a, const u4& b) { asm volatile("v_mfma_f32_16x16x32_bf16 %0, %1, %2, %0" : "+v"(acc) : "a"(a), "v"(b)); }
DI void mfma_va0(f4& acc, const u4& a, const u4& b) { asm volatile("v_mfma_f32_16x16x32_bf16 %0, %1, %2, 0" : "=&v"(acc) : "a"(a), "v"(b)); }
DI void mfma_vv(f4& acc, const u4& a, const u4& b) { asm volatile("v_mfma_f32_16x16x32_bf16 %0, %1, %2, %0" : "+v"(acc) : "v"(a), "v"(b)); }
DI void mfma_vv0(f4& acc, const u4& a, const u4& b) { asm volatile("v_mfma_f32_16x16x32_bf16 %0, %1, %2, 0" : "=&v"(acc) : "v"(a), "v"(b)); }
DI void mfma_done() { asm volatile("s_nop 7" ::: "memory"); __builtin_amdgcn_sched_barrier(0); }
DI u2 lds_tr(lds_t* p) { return __builtin_bit_cast(u2, __builtin_amdgcn_ds_read_tr16_b64_v4i16((LAS s4*)p)); }
DI u4 cat(u2 a, u2 b) { return u4{a.x, a.y, b.x, b.y}; }
DI rsrc_t make_rsrc(const void* p, unsigned bytes) { return __builtin_amdgcn_make_buffer_rsrc((void*)p, 0, (int)bytes, 0x00020000); }
DI u4 ld16(rsrc_t r, unsigned off) { return __builtin_amdgcn_raw_buffer_load_b128(r, off, 0, AUX_SC1); }
DI void st16(rsrc_t r, unsigned off, u4 v) { __builtin_amdgcn_raw_buffer_store_b128(v, r, off, 0, AUX_SC1); }
DI void drain() { asm volatile("s_waitcnt vmcnt(0)" ::: "memory"); }
DI void dma4_sc1(rsrc_t r, unsigned v0, unsigned v1, unsigned soff, unsigned lds) {
  asm volatile("s_mov_b32 m0, %4\n\ts_nop 0\n\t"
               "buffer_load_dwordx4 %0, %2, %3 offen sc1 lds\n\tbuffer_load_dwordx4 %1, %2, %3 offen offset:1024 sc1 lds\n\t"
               "buffer_load_dwordx4 %0, %2, %3 offen offset:2048 sc1 lds\n\tbuffer_load_dwordx4 %1, %2, %3 offen offset:3072 sc1 lds"
               :: "v"(v0), "v"(v1), "s"(r), "s"(soff), "s"(lds) : "memory");
}
DI void dma4(rsrc_t r, unsigned v0, unsigned v1, unsigned v2, unsigned v3, unsigned soff, unsigned lds) {
  asm volatile("s_mov_b32 m0, %6\n\ts_nop 0\n\t"
               "buffer_load_dwordx4 %0, %4, %5 offen lds\n\tbuffer_load_dwordx4 %1, %4, %5 offen offset:1024 lds\n\t"
               "buffer_load_dwordx4 %2, %4, %5 offen offset:2048 lds\n\tbuffer_load_dwordx4 %3, %4, %5 offen offset:3072 lds"
               :: "v"(v0), "v"(v1), "v"(v2), "v"(v3), "s"(r), "s"(soff), "s"(lds) : "memory");
}
#define SB() __builtin_amdgcn_sched_barrier(0)
#define SGB(mask, n) __builtin_amdgcn_sched_group_barrier(mask, n, 0)
constexpr int SG_MFMA = 0x8, SG_DSR = 0x100;
template <int CTRL> DI float dpp_f(float v) { return __builtin_bit_cast(float, __builtin_amdgcn_mov_dpp(__builtin_bit_cast(int, v), CTRL, 0xf, 0xf, true)); }
DI float row_sum(float v) { v += dpp_f<0x128>(v); v += dpp_f<0x124>(v); v += dpp_f<0x122>(v); v += dpp_f<0x121>(v); return v; }
DI f2 swap32(float a, float b) { auto r = __builtin_amdgcn_permlane32_swap(__builtin_bit_cast(unsigned, a), __builtin_bit_cast(unsigned, b), false, false);
  const unsigned r0 = r[0], r1 = r[1];
  return f2{__builtin_bit_cast(float, r0), __builtin_bit_cast(float, r1)}; }
DI f2 swap16(float a, float b) { auto r = __builtin_amdgcn_permlane16_swap(__builtin_bit_cast(unsigned, a), __builtin_bit_cast(unsigned, b), false, false);
  const unsigned r0 = r[0], r1 = r[1];
  return f2{__builtin_bit_cast(float, r0), __builtin_bit_cast(float, r1)}; }
DI float fmax2(float a, float b) { return __builtin_fmaxf(a, b); }
DI float fmin2(float a, float b) { return __builtin_fminf(a, b); }
template <int OP> DI float op2(float a, float b) { return OP == 0 ? fmax2(a, b) : OP == 1 ? fmin2(a, b) : a + b; }
template <int OP> DI float xrow4(float a, float b, float c, float d) {
  f2 s = swap32(a, b); const float ab = op2<OP>(s.x, s.y);
  s = swap32(c, d); const float cd = op2<OP>(s.x, s.y);
  s = swap16(ab, cd); return op2<OP>(s.x, s.y);
}
DI f2 xrow4_maxmin(float a0, float a1, float a2, float a3, float b0, float b1, float b2, float b3) {
  asm("s_nop 1\n\t"
      "v_permlane32_swap_b32 %0, %1\n\tv_permlane32_swap_b32 %2, %3\n\tv_permlane32_swap_b32 %4, %5\n\tv_permlane32_swap_b32 %6, %7\n\t"
      "v_max_f32 %0, %0, %1\n\tv_max_f32 %2, %2, %3\n\tv_min_f32 %4, %4, %5\n\tv_min_f32 %6, %6, %7\n\t"
      "v_permlane16_swap_b32 %0, %2\n\ts_nop 0\n\tv_permlane16_swap_b32 %4, %6\n\t"
      "v_max_f32 %0, %0, %2\n\tv_min_f32 %4, %4, %6"
      : "+v"(a0), "+v"(a1), "+v"(a2), "+v"(a3), "+v"(b0), "+v"(b1), "+v"(b2), "+v"(b3));
  return f2{a0, b0};
}
DI f2 xrow_max2(float a, float b) { f2 s = swap32(a, b); float c = fmaxf(s.x, s.y); s = swap16(c, c); c = fmaxf(s.x, s.y); return swap32(c, c); }
DI f2 xrow_sum2(float a, float b) { f2 s = swap32(a, b); float c = s.x + s.y; s = swap16(c, c); c = s.x + s.y; return swap32(c, c); }
DI float wave_sum(float v) { v = row_sum(v); f2 s = swap16(v, v); v = s.x + s.y; s = swap32(v, v); return s.x + s.y; }

constexpr int LD_XT = 0;
constexpr int LD_KC = 65536;
constexpr int KC_COPY = 4160, KC_CH = 4 * KC_COPY;
constexpr int LD_U = LD_KC + 2 * KC_CH;
constexpr int U_CH = 4608;
constexpr int LD_X = LD_U + 2 * U_CH;
constexpr int LD_EX = LD_KC;
constexpr int LD_CONV_END = LD_X + 8192;
constexpr int LD_WA = 0, LD_WB = LD_CONV_END;
constexpr int LD_MISC = 131072;
constexpr int LD_PV = LD_MISC;
constexpr int PV_ROW = 104, PV_WAVE = 64 * PV_ROW;
constexpr int LD_STAT = LD_PV + 4 * PV_WAVE;
constexpr int LD_SCR = LD_STAT + 1024 + 256;
constexpr int LD_FLAG = LD_SCR + 2048;
constexpr int LD_OST = LD_PV;
constexpr int LDS_BYTES = 163840;
static_assert(LD_CONV_END <= LD_MISC && LD_FLAG + 64 <= LDS_BYTES, "LDS map");

struct Ctx {
  lds_t* L; rsrc_t ws; GAS unsigned* cw; unsigned char* wsp;
  int tid, lane, w, c, cp, rb, hp, layer;
};

DI void spin_fail(const Ctx& x) { __hip_atomic_store(x.cw + CW_TMO, 1u, RLX_AGENT); }
constexpr unsigned SPIN_CAP = 1u << 18;
DI void a2a_post(const Ctx& x, int e) {
  __syncthreads();
  if (x.tid < REP) __hip_atomic_fetch_add(x.cw + (CWI_A2A + (e * REP + x.tid) * NS + (x.c >> 3) % NS) * CW_STRIDE, 1u, RLX_AGENT);
}
DI void a2a_signal(const Ctx& x, int e) { drain(); a2a_post(x, e); }
DI void poll16(const Ctx& x, unsigned word) {
  if (x.w == 0) {
    GAS unsigned* q = x.cw + word * CW_STRIDE;
    for (unsigned s = 0;; ++s) {
      if (__all(__hip_atomic_load(q, RLX_AGENT) >= 256u / NS)) break;
      if (s > SPIN_CAP) { spin_fail(x); break; }
      __builtin_amdgcn_s_sleep(1);
    }
    __builtin_amdgcn_fence(__ATOMIC_ACQUIRE, "wavefront");
  }
  __syncthreads();
}
DI void a2a_wait(const Ctx& x, int e) { poll16(x, CWI_A2A + (e * REP + (x.c & 7) % REP) * NS + x.lane % NS); }

DI void conv_load_k(const Ctx& x, u4 (&kd)[3]) {
  const int ch = x.tid >> 7, q = x.tid & 127;
  const unsigned off = WS_KREV + ((2 * x.cp + ch) * 2048 + 16 * q) * 2;
  kd[0] = __builtin_amdgcn_raw_buffer_load_b128(x.ws, off, 0, 0);
  kd[1] = __builtin_amdgcn_raw_buffer_load_b128(x.ws, off + 16, 0, 0);
  kd[2] = __builtin_amdgcn_raw_buffer_load_b128(x.ws, off + 32, 0, 0);
}
DI void conv_build_k(const Ctx& x, const u4 (&kd)[3]) {
  const int ch = x.tid >> 7, q = x.tid & 127;
  const unsigned d8 = q == 127 ? 0u : kd[2].x, d9 = q == 127 ? 0u : kd[2].y;
  const unsigned d[10] = {kd[0].x, kd[0].y, kd[0].z, kd[0].w, kd[1].x, kd[1].y, kd[1].z, kd[1].w, d8, d9};
  lds_t* b = x.L + LD_KC + ch * KC_CH + 32 * q;
  *(LAS u4*)(b) = u4{d[0], d[1], d[2], d[3]}; *(LAS u4*)(b + 16) = u4{d[4], d[5], d[6], d[7]};
  *(LAS u4*)(b + 2 * KC_COPY) = u4{d[1], d[2], d[3], d[4]}; *(LAS u4*)(b + 2 * KC_COPY + 16) = u4{d[5], d[6], d[7], d[8]};
  unsigned e[9];
#pragma unroll
  for (int i = 0; i < 9; ++i) e[i] = __builtin_amdgcn_alignbit(d[i + 1], d[i], 16);
  *(LAS u4*)(b + KC_COPY) = u4{e[0], e[1], e[2], e[3]}; *(LAS u4*)(b + KC_COPY + 16) = u4{e[4], e[5], e[6], e[7]};
  *(LAS u4*)(b + 3 * KC_COPY) = u4{e[1], e[2], e[3], e[4]}; *(LAS u4*)(b + 3 * KC_COPY + 16) = u4{e[5], e[6], e[7], e[8]};
  if (x.tid < 16) {
    *(LAS u4*)(x.L + LD_KC + (x.tid >> 1) * KC_COPY + 4096 + (x.tid & 1) * 16) = u4{0, 0, 0, 0};
  }
  if (x.tid >= 64 && x.tid < 128) {
    const int i = x.tid - 64;
    *(LAS u4*)(x.L + LD_U + (i >> 5) * U_CH + (i & 31) * 16) = u4{0, 0, 0, 0};
  }
}
template <int PAR> struct Mu { static constexpr int v(int k) { return PAR ? (k == 0 ? 1 : k == 1 ? 2 : k == 2 ? 5 : 6) : (k == 0 ? 0 : k == 1 ? 3 : k == 2 ? 4 : 7); } };
template <int PAR> DI lds_t* conv_a_base(const Ctx& x) {
  const int b = x.lane & 15, kq = x.lane >> 4, ch = x.w >> 1;
  const int s0 = 2047 - 16 * (kq >> 1) - b + 8 * (kq & 1), cc = s0 & 3;
  unsigned bo = LD_KC + ch * KC_CH + cc * KC_COPY + 2 * (s0 - cc) - 64 * 63;
  asm volatile("" : "+v"(bo));
  return x.L + bo;
}
template <int PAR> DI u4 conv_a_frag(lds_t* base, int k, int i) {
  const int m = 8 * Mu<PAR>::v(k) + i;
  const u2 lo = *(volatile LAS u2*)(base + 64 * (63 - m)), hi = *(volatile LAS u2*)(base + 64 * (63 - m) + 8);
  return cat(lo, hi);
}
template <int PAR> DI void conv_load_a(const Ctx& x, u4 (&a)[32]) {
  lds_t* base = conv_a_base<PAR>(x);
#pragma unroll
  for (int k = 0; k < 4; ++k)
#pragma unroll
    for (int i = 0; i < 8; ++i) a[8 * k + i] = conv_a_frag<PAR>(base, k, i);
}
struct WDma { unsigned lane16, soff, lo, hi; };
DI WDma q_dma_w_setup(const Ctx& x) {
  const unsigned lo = x.w < 3 ? LD_WA + x.w * 24576 : LD_WB + 8192, hi = x.w < 2 ? lo : x.w == 2 ? LD_WB - 16384 : lo;
  return WDma{(unsigned)x.lane * 16, WS_WFRAG + x.hp * (96 * 1024) + x.w * 24576, lo, hi};
}
template <int I> DI void q_dma_w1(const Ctx& x, const WDma& d) {
  unsigned t;
  asm volatile("s_add_u32 m0, %4, %5\n\ts_add_u32 %0, %3, %5\n\tbuffer_load_dwordx4 %1, %2, %0 offen sc1 lds"
               : "=&s"(t) : "v"(d.lane16), "s"(x.ws), "s"(d.soff), "s"(I < 16 ? d.lo : d.hi), "n"(I * 1024) : "memory", "scc");
}
DI void q_take_w(const Ctx& x, u4 (&wf)[48]) {
  const int h = x.w & 1;
  const unsigned a0 = LD_WA + h * 49152 + x.lane * 16, a1 = (h ? LD_WB - 16384 : LD_WA) + x.lane * 16;
#pragma unroll
  for (int i = 0; i < 48; ++i)
    asm volatile("ds_read_b128 %0, %1 offset:%2" : "=a"(wf[i]) : "v"(i < 16 ? a0 : a1), "n"(i * 1024));
  asm volatile("s_waitcnt lgkmcnt(0)" ::: "memory");
  SB();
}
template <int TAU, int I> DI void conv_dma_w(const Ctx& x, const WDma& d) { if ((I & 1) && TAU < 6) { q_dma_w1<(TAU < 6 ? 4 * TAU + (I >> 1) : 0)>(x, d); SB(); } }
template <int PAR, bool FIRST> DI void conv_mfma(const Ctx& x, u4 (&a)[32], f4 (&acc)[8]) {
  const WDma wd = q_dma_w_setup(x);
  const int al = x.lane & 15, kq = x.lane >> 4, ch = x.w >> 1;
  unsigned bo = LD_U + ch * U_CH + 2 * (256 + 16 * al - 16 * (kq >> 1) + 8 * (kq & 1)) - 64 * 7;
  asm volatile("" : "+v"(bo));
  lds_t* base = x.L + bo;
  constexpr int NT = 8 - Mu<PAR>::v(0);
  u4 b[8];
  if (FIRST) {
    STAMP3(x, 37);
    lds_t* abase = conv_a_base<PAR>(x);
#pragma unroll
    for (int i = 0; i < 8; ++i) { b[i] = *(LAS u4*)(base + 64 * (7 - i)); a[i] = conv_a_frag<PAR>(abase, 0, i); }
    SB();
#pragma unroll
    for (int k = 0; k < 4; ++k) {
#pragma unroll
      for (int i = 0; i < 8; ++i) {
        acc[Mu<PAR>::v(k)] = mfma32(a[8 * k + i], b[i], acc[Mu<PAR>::v(k)]);
        if (k < 3) { a[8 * (k + 1) + i] = conv_a_frag<PAR>(abase, k + 1, i); SGB(SG_MFMA, 1); SGB(SG_DSR, 2); }
        else { b[i] = *(LAS u4*)(base + 64 * (8 - i + 7)); SGB(SG_MFMA, 1); SGB(SG_DSR, 1); }
        SB();
      }
      switch (k) {
        case 0: q_dma_w1<0>(x, wd); break; case 1: q_dma_w1<1>(x, wd); break;
        case 2: q_dma_w1<2>(x, wd); break; default: q_dma_w1<3>(x, wd); break;
      }
      SB();
    }
    STAMP3(x, 38);
  } else {
#pragma unroll
    for (int i = 0; i < 8; ++i) b[i] = *(LAS u4*)(base + 64 * (7 - i));
    SB();
  }
#pragma unroll
  for (int tau = FIRST ? 1 : 0; tau < 8; ++tau) {
    if (FIRST && tau == 4) STAMP3(x, 39);
    if (tau < NT) {
      int ng = 0;
#pragma unroll
      for (int k = 0; k < 4; ++k) ng += (Mu<PAR>::v(k) + tau <= 7);
#pragma unroll
      for (int i = 0; i < 8; ++i) {
#pragma unroll
        for (int k = 0; k < 4; ++k)
          if (Mu<PAR>::v(k) + tau <= 7) acc[Mu<PAR>::v(k) + tau] = mfma32(a[8 * k + i], b[i], acc[Mu<PAR>::v(k) + tau]);
        if (tau + 1 < NT) {
          b[i] = *(LAS u4*)(base + 64 * (8 * (tau + 1) - i + 7));
          if (ng == 4) SGB(SG_MFMA, 4); else if (ng == 3) SGB(SG_MFMA, 3); else if (ng == 2) SGB(SG_MFMA, 2); else SGB(SG_MFMA, 1);
          SGB(SG_DSR, 1);
        }
        SB();
        if (FIRST) {
          switch (8 * tau + i) {
#define W_(t, j) case 8 * t + j: conv_dma_w<t, j>(x, wd); break;
#define WT_(t) W_(t, 1) W_(t, 3) W_(t, 5) W_(t, 7)
            WT_(1) WT_(2) WT_(3) WT_(4) WT_(5)
#undef WT_
#undef W_
            default: break;
          }
        }
      }
    }
  }
}

struct ConvIn { u4 x0, x1, s[4]; };
DI void conv_load_in(const Ctx& x, int layer, ConvIn& in) {
  const unsigned xo = WS_XT + layer * XT_BYTES + (2 * x.cp * XT_STRIDE + 8 * x.tid) * 2;
  const unsigned so = WS_ST + layer * 2048 * 8 + x.tid * 32;
  in.s[0] = ld16(x.ws, so); in.s[1] = ld16(x.ws, so + 16); in.s[2] = ld16(x.ws, so + 8192); in.s[3] = ld16(x.ws, so + 8192 + 16);
  in.x0 = ld16(x.ws, xo); in.x1 = ld16(x.ws, xo + XT_STRIDE * 2);
}
DI f2 unpk(unsigned v) { return f2{blo(v), bhi(v)}; }
DI unsigned pk(f2 v) { return pack2(v.x, v.y); }
DI void conv_build_u(const Ctx& x, const ConvIn& in) {
  const f4 sa = __builtin_bit_cast(f4, in.s[0]), sb = __builtin_bit_cast(f4, in.s[1]), qa = __builtin_bit_cast(f4, in.s[2]), qb = __builtin_bit_cast(f4, in.s[3]);
  const f2 sv[4] = {f2{sa[0], sa[1]}, f2{sa[2], sa[3]}, f2{sb[0], sb[1]}, f2{sb[2], sb[3]}};
  const f2 qv[4] = {f2{qa[0], qa[1]}, f2{qa[2], qa[3]}, f2{qb[0], qb[1]}, f2{qb[2], qb[3]}};
  u4 u0, u1;
#pragma unroll
  for (int i = 0; i < 4; ++i) {
    const f2 m = sv[i] * (1.f / DM);
    const f2 ve = (qv[i] * (1.f / DM) + LN_EPS) - m * m;
    const f2 r = {__builtin_amdgcn_rsqf(__builtin_fabsf(ve.x)), __builtin_amdgcn_rsqf(__builtin_fabsf(ve.y))};
    const f2 nmr = -m * r;
    u0[i] = pk(unpk(in.x0[i]) * r + nmr);
    u1[i] = pk(unpk(in.x1[i]) * r + nmr);
  }
  *(LAS u4*)(x.L + LD_U + 512 + 16 * x.tid) = u0; *(LAS u4*)(x.L + LD_U + U_CH + 512 + 16 * x.tid) = u1;
  *(LAS u4*)(x.L + LD_X + 16 * x.tid) = in.x0; *(LAS u4*)(x.L + LD_X + 4096 + 16 * x.tid) = in.x1;
}
DI void poison_o(const Ctx& x, int layer) {
#pragma unroll
  for (int i = 0; i < 2; ++i) st16(x.ws, WS_OB + layer * OB_BYTES + (x.c * 2 + i) * 4096 + x.tid * 16, u4{~0u, ~0u, ~0u, ~0u});
}
template <int PAR, bool LOADW> DI void conv_wave(const Ctx& x, float dsk, u4 (&a)[32]) {
  const int ch = x.w >> 1;
  f4 acc[8];
#pragma unroll
  for (int t = 0; t < 8; ++t) acc[t] = f4{0, 0, 0, 0};
  conv_mfma<PAR, LOADW>(x, a, acc);
  if (LOADW) __syncthreads();
  STAMP(x.wsp, 6 + 16 * x.layer);
  const int al = x.lane & 15, g = x.lane >> 4;
  unsigned eo = LD_EX + x.lane * 16, uo = LD_U + ch * U_CH + 512 + 2 * (1024 * PAR + 16 * al + 4 * g);
  asm volatile("" : "+v"(eo), "+v"(uo));
  lds_t* mine = x.L + eo + x.w * 4096;
  lds_t* other = x.L + eo + (x.w ^ 1) * 4096;
#pragma unroll
  for (int t = 0; t < 4; ++t) *(LAS f4*)(mine + t * 1024) = acc[PAR ? t : 4 + t];
  f2 v[4][2];
#pragma unroll
  for (int t = 0; t < 4; ++t) {
    const u2 uu = *(LAS u2*)(x.L + uo + 512 * t), xx = *(LAS u2*)(x.L + uo + (LD_X - LD_U - 512) + ch * (4096 - U_CH) + 512 * t);
    const f4 own = acc[PAR ? 4 + t : t];
    v[t][0] = unpk(uu.x) * dsk + (unpk(xx.x) + f2{own[0], own[1]});
    v[t][1] = unpk(uu.y) * dsk + (unpk(xx.y) + f2{own[2], own[3]});
  }
  __syncthreads();
  STAMP(x.wsp, 7 + 16 * x.layer);
  const unsigned so = WS_X1 + x.layer * X1_BYTES + (((8 * PAR + 2 * (g & 1) + (al >> 3)) * 512 + 2 * x.cp + ch) * 128 + 16 * (al & 7) + 8 * (g >> 1)) * 2;
#pragma unroll
  for (int t = 0; t < 4; t += 2) {
    const f4 o0 = *(LAS f4*)(other + t * 1024), o1 = *(LAS f4*)(other + (t + 1) * 1024);
    const unsigned a0 = pk(v[t][0] + f2{o0[0], o0[1]}), a1 = pk(v[t][1] + f2{o0[2], o0[3]});
    const unsigned b0 = pk(v[t + 1][0] + f2{o1[0], o1[1]}), b1 = pk(v[t + 1][1] + f2{o1[2], o1[3]});
    const auto s0 = __builtin_amdgcn_permlane16_swap(a0, b0, false, false), s1 = __builtin_amdgcn_permlane16_swap(a1, b1, false, false);
    st16(x.ws, so + t * (2 * 512 * 256), u4{s0[0], s1[0], s0[1], s1[1]});
  }
  if (LOADW) poison_o(x, 0);
  drain();
}

DI f2 cmul(f2 a, f2 b) { return f2{a.x, a.x} * b + f2{a.y, a.y} * f2{-b.y, b.x}; }
DI f2 cexp_p(float wr2, float wrev, float p) {
  const float e = __builtin_amdgcn_exp2f(wr2 * p);
  const float a = __builtin_amdgcn_fractf(wrev * p);
  return f2{e * __builtin_amdgcn_cosf(a), e * __builtin_amdgcn_sinf(a)};
}
constexpr int KG_ROW = 528;
constexpr int KG_A = 0;
constexpr int KG_X = 2 * 48 * KG_ROW;
constexpr int KG_T = LD_CONV_END;
constexpr int KG_B = KG_T + 2 * 2056 * 4;
static_assert(KG_X + 64 * 136 <= LD_KC && KG_B + 2 * 16 * KG_ROW <= LDS_BYTES, "prologue LDS map");

struct ProW { u2 a0, a1; unsigned short b[4]; };
struct ProIn { unsigned short lar, aim, cre, cim, ldt; u4 xt[2], xr[2]; ProW w[2]; unsigned epoch; };
DI void pro_load_w(const Params& p, int F, int lane, ProW& w) {
  const int fh = F / 48, fm = (F % 48) >> 4, fks = F & 15, row = lane & 15, kq = lane >> 4;
  w.a0 = *(const u2*)(p.wqkv + (32 * fks + row) * 1536 + fm * 512 + 16 * fh + 4 * kq);
  w.a1 = *(const u2*)(p.wqkv + (32 * fks + 16 + row) * 1536 + fm * 512 + 16 * fh + 4 * kq);
  const unsigned short* q = p.wphi + fh * 256 + (4 * kq) * 16 + row;
#pragma unroll
  for (int j = 0; j < 4; ++j) w.b[j] = q[16 * j];
}
DI void pro_fold_w(const Ctx& x, int F, const ProW& w) {
  const bool fold = ((F % 48) >> 4) < 2;
  const int dd = (x.lane & 15) - 4 * (x.lane >> 4);
  const u2 wb = fold ? u2{w.b[0] | ((unsigned)w.b[1] << 16), w.b[2] | ((unsigned)w.b[3] << 16)}
                     : u2{(dd == 0 ? 0x3f80u : 0u) | (dd == 1 ? 0x3f800000u : 0u), (dd == 2 ? 0x3f80u : 0u) | (dd == 3 ? 0x3f800000u : 0u)};
  const f4 d0 = mfma16(w.a0, wb, f4{0, 0, 0, 0}), d1 = mfma16(w.a1, wb, f4{0, 0, 0, 0});
  float e[8];
#pragma unroll
  for (int r = 0; r < 4; ++r) {
    const float b0 = dpp_f<0x150>(d0[r]), b1 = dpp_f<0x150>(d1[r]);
    e[r] = d0[r] - (fold ? b0 : 0.f); e[4 + r] = d1[r] - (fold ? b1 : 0.f);
  }
  u4 o = {pack2(e[0], e[1]), pack2(e[2], e[3]), pack2(e[4], e[5]), pack2(e[6], e[7])};
  if (fold && (x.lane & 15) == 0) o = u4{0x3f803f80u, 0x3f803f80u, 0x3f803f80u, 0x3f803f80u};
  st16(x.ws, WS_WFRAG + F * 1024 + x.lane * 16, o);
}
DI void pro_load(const Ctx& x, const Params& p, ProIn& in) {
#pragma unroll
  for (int i = 0; i < 2; ++i) {
    in.xt[i] = *(const u4*)(p.u + (64 * (x.c >> 3) + 32 * i + (x.tid >> 3)) * DM + 64 * (x.c & 7) + 8 * (x.tid & 7));
    in.xr[i] = *(const u4*)(p.u + (8 * x.c + 2 * x.w + i) * DM + 8 * x.lane);
  }
  in.epoch = __builtin_amdgcn_raw_buffer_load_b32(x.ws, WS_EPOCH, 0, AUX_SC1);
  const int d = 2 * x.cp + (x.tid >> 7), n = x.tid & 127;
  in.lar = p.lar[d * NST + n]; in.aim = p.aim[d * NST + n]; in.cre = p.cre[d * NST + n]; in.cim = p.cim[d * NST + n]; in.ldt = p.ldt[d];
  pro_load_w(p, 6 * x.c + x.w, x.lane, in.w[0]);
  pro_load_w(p, 6 * x.c + 4 + (x.w & 1), x.lane, in.w[1]);
  SB();
}
constexpr int CODE_KB = 44;
struct Warm { unsigned a, b, c; };
DI unsigned pro_warm_code(const Ctx& x, const unsigned char* code) {
  unsigned v = 0;
  if (x.w == 3 && x.lane < CODE_KB * 8 / 32) v = *(const GAS unsigned*)(code + ((x.c >> 3) * (CODE_KB * 8 / 32) + x.lane) * 128);
  return v;
}
DI void pro_warm(const Ctx& x, const Params& p, Warm& wm) {
  const int j = x.c >> 3;
  wm.b = wm.c = 0;
  if (x.w == 3) {
    wm.b = *(const GAS unsigned*)((const unsigned char*)p.wo + (j * 128 + x.lane) * 128);
    wm.c = *(const GAS unsigned*)((const unsigned char*)p.wo + (j * 128 + 64 + x.lane) * 128);
  }
}
DI void pro_warm_retire(const Warm& wm) { asm volatile("" :: "v"(wm.a), "v"(wm.b), "v"(wm.c)); }
DI void pro_init(const Ctx& x) {
  const rsrc_t ws = x.ws;
  if (x.c < 2) { const int i = x.c * 256 + x.tid; if (i < CWI_END) __builtin_amdgcn_raw_buffer_store_b32(0u, ws, i * (CW_STRIDE * 4), 0, AUX_SC1); }
  if (x.c >= 8 && x.c < 12) st16(ws, WS_ST + 2048 * 8 + ((x.c - 8) * 256 + x.tid) * 16, u4{0, 0, 0, 0});
  static_assert(WS_ZP == WS_KVP + 2 * KVP_BYTES && (2 * KVP_BYTES + 2 * ZP_BYTES) == 256 * 272 * 16, "poisoned block");
  st16(ws, WS_KVP + (x.c * 272 + x.tid) * 16, u4{~0u, ~0u, ~0u, ~0u});
  if (x.tid < 16) st16(ws, WS_KVP + (x.c * 272 + 256 + x.tid) * 16, u4{~0u, ~0u, ~0u, ~0u});
}
DI void pro_publish_x(const Ctx& x, const ProIn& in) {
#pragma unroll
  for (int i = 0; i < 2; ++i) {
    lds_t* a = x.L + KG_X + (32 * i + (x.tid >> 3)) * 136 + (x.tid & 7) * 16;
    *(LAS u2*)a = u2{in.xt[i].x, in.xt[i].y}; *(LAS u2*)(a + 8) = u2{in.xt[i].z, in.xt[i].w};
  }
  {
    f2 a[2], q[2];
#pragma unroll
    for (int i = 0; i < 2; ++i) {
      a[i] = unpk(in.xr[i][0]); q[i] = a[i] * a[i];
#pragma unroll
      for (int k = 1; k < 4; ++k) { const f2 v = unpk(in.xr[i][k]); a[i] += v; q[i] += v * v; }
    }
    const float t = xrow4<2>(row_sum(a[0].x + a[0].y), row_sum(q[0].x + q[0].y), row_sum(a[1].x + a[1].y), row_sum(q[1].x + q[1].y));
    const int g = x.lane >> 4;
    if ((x.lane & 15) == 0)
      __builtin_amdgcn_raw_buffer_store_b32(__builtin_bit_cast(unsigned, t), x.ws, WS_ST + (g >> 1) * 8192 + (8 * x.c + 2 * x.w + (g & 1)) * 4, 0, AUX_SC1);
  }
  __syncthreads();
  const int g = x.lane >> 4, cl = x.lane & 15;
#pragma unroll
  for (int j = 0; j < 2; ++j) {
    lds_t* a = x.L + KG_X + (8 * (g + 4 * j) + (cl >> 2)) * 136 + (16 * x.w + 4 * (cl & 3)) * 2;
    st16(x.ws, WS_XT + ((64 * (x.c & 7) + 16 * x.w + cl) * XT_STRIDE + 64 * (x.c >> 3) + 8 * (g + 4 * j)) * 2, cat(lds_tr(a), lds_tr(a + 4 * 136)));
  }
}
DI void pro_tables(const Ctx& x, const ProIn& in) {
  const float dt = rbf(__expf(bf2f(in.ldt)));
  const float ar = -rbf(__expf(bf2f(in.lar)));
  const float wr2 = (ar * dt) * LOG2E, wrev = (bf2f(in.aim) * dt) * 0.15915494309189535f;
  const f2 c2 = {2.0f * (bf2f(in.cre) * dt), -2.0f * (bf2f(in.cim) * dt)};
  const int chl = x.tid >> 7, n = x.tid & 127;
  LAS unsigned* TA = (LAS unsigned*)(x.L + KG_A + chl * 48 * KG_ROW) + n;
  LAS unsigned* TB = (LAS unsigned*)(x.L + KG_B + chl * 16 * KG_ROW) + n;
  constexpr int RS = KG_ROW / 4;
  const f2 e8 = cexp_p(wr2, wrev, 8.f), e128 = cexp_p(wr2, -wrev, 128.f), e1 = cexp_p(wr2, -wrev, 1.f);
  f2 y = e8, h = cmul(c2, cexp_p(wr2, -wrev, 1024.f)), g[8], e[8];
  g[0] = c2; e[1] = e1; e[2] = cmul(e1, e1);
  TB[0] = 0x00003f80u;
#pragma unroll
  for (int i = 0; i < 8; ++i) {
    TB[(2 * i + 1) * RS] = pk(y);
    TA[(32 + i) * RS] = pk(h);
    const f2 y1 = cmul(y, e8), h1 = cmul(h, e128);
    if (i < 7) g[i + 1] = cmul(g[i], e128);
    if (i == 0) { e[3] = cmul(e[2], e[1]); e[4] = cmul(e[2], e[2]); }
    if (i == 1) { e[5] = cmul(e[4], e[1]); e[6] = cmul(e[3], e[3]); e[7] = cmul(e[4], e[3]); }
    if (i < 7) { TB[(2 * i + 2) * RS] = pk(y1); y = cmul(y1, e8); }
    h = h1;
  }
  TA[40 * RS] = pk(h);
#pragma unroll
  for (int k = 0; k < 8; ++k) {
    TA[k * RS] = pk(k ? cmul(g[0], e[k]) : g[0]);
    TA[(8 + k) * RS] = pk(k ? cmul(g[1], e[k]) : g[1]);
    if (k < 4) { TA[(16 + k) * RS] = pk(k ? cmul(g[2], e[2 * k]) : g[2]); TA[(20 + k) * RS] = pk(k ? cmul(g[3], e[2 * k]) : g[3]); }
    else { TA[(24 + 2 * (k - 4)) * RS] = pk(g[k]); TA[(25 + 2 * (k - 4)) * RS] = pk(cmul(g[k], e[4])); }
  }
}
DI void pro_kgemm(const Ctx& x) {
  const int ch = x.w >> 1, j = x.lane & 15, g = x.lane >> 4, odd = x.w & 1;
  unsigned ao = KG_A + (ch * 48 + 16 * odd + j) * KG_ROW + g * 16, a2o = KG_A + (ch * 48 + 32 + j) * KG_ROW + g * 16;
  unsigned bo = KG_B + (ch * 16 + j) * KG_ROW + g * 16;
  asm volatile("" : "+v"(ao), "+v"(a2o), "+v"(bo));
  u4 a0[8], a2[8], b[8];
#pragma unroll
  for (int ks = 0; ks < 8; ++ks) { b[ks] = *(LAS u4*)(x.L + bo + ks * 64); a0[ks] = *(LAS u4*)(x.L + ao + ks * 64); a2[ks] = *(LAS u4*)(x.L + a2o + ks * 64); }
  SB();
  f4 acc0 = {0, 0, 0, 0}, acc2 = acc0;
#pragma unroll
  for (int ks = 0; ks < 8; ++ks) { acc0 = mfma32(a0[ks], b[ks], acc0); acc2 = mfma32(a2[ks], b[ks], acc2); }
  const int pe = 128 * (g >> 1) + 4 * (g & 1), po = g < 2 ? 256 + 128 * g : 512 + 256 * (g - 2);
  const unsigned t0 = KG_T + (ch * 2056 + 8 * j + (odd ? po : pe)) * 4;
  const int s1 = odd ? (g < 2 ? 8 : 16) : 4, s2 = odd ? (g < 2 ? 16 : 512) : 8;
  *(LAS float*)(x.L + t0) = acc0[0]; *(LAS float*)(x.L + t0 + s1) = acc0[1];
  *(LAS float*)(x.L + t0 + s2) = acc0[2]; *(LAS float*)(x.L + t0 + s1 + s2) = acc0[3];
  const unsigned t2 = KG_T + (ch * 2056 + 1024 + 512 * g + 8 * j) * 4, dummy = KG_T + 2052 * 4;
#pragma unroll
  for (int r = 0; r < 4; ++r) *(LAS float*)(x.L + ((4 * g + r < 8 || (4 * g + r == 8 && j == 0)) ? t2 + 512 * r : dummy)) = acc2[r];
}
constexpr int KG_D = 0, KG_DCH = 4096 + 64;
DI void pro_expand(const Ctx& x) {
  unsigned tb = KG_T, db = KG_D + x.tid * 4;
  f2 l0 = {(float)(2047 - 2 * x.tid), (float)(2046 - 2 * x.tid)};
  asm volatile("" : "+v"(tb), "+v"(db), "+v"(l0));
#pragma unroll
  for (int i = 0; i < 4; ++i) {
    unsigned r = pk(l0 - (float)(512 * i));
    asm("" : "+v"(r));
    const unsigned a0 = tb + 4 * (int)blo(r), a1 = tb + 4 * (int)bhi(r);
#pragma unroll
    for (int ch = 0; ch < 2; ++ch)
      *(LAS unsigned*)(x.L + db + ch * KG_DCH + i * 1024) = pack2(*(LAS float*)(x.L + a0 + ch * (2056 * 4)), *(LAS float*)(x.L + a1 + ch * (2056 * 4)));
  }
}
DI void pro_take_k(const Ctx& x, u4 (&kd)[3]) {
  const int ch = x.tid >> 7, q = x.tid & 127;
  lds_t* a = x.L + KG_D + ch * KG_DCH + 32 * q;
  kd[0] = *(LAS u4*)a; kd[1] = *(LAS u4*)(a + 16); kd[2] = *(LAS u4*)(a + 32);
  const unsigned off = WS_KREV + ((2 * x.cp + ch) * 2048 + 16 * q) * 2;
  __builtin_amdgcn_raw_buffer_store_b128(kd[0], x.ws, off, 0, 0);
  __builtin_amdgcn_raw_buffer_store_b128(kd[1], x.ws, off + 16, 0, 0);
}
DI void pro_wait(const Ctx& x, unsigned flags, unsigned tag, unsigned first) {
  unsigned v = first;
  for (unsigned s = 0;; ++s) {
    const unsigned ok = __all(v == tag);
    if (x.lane == 0) ((LAS unsigned*)(x.L + LD_FLAG))[x.w] = ok;
    __syncthreads();
    const u4 r = *(LAS u4*)(x.L + LD_FLAG);
    if (r.x & r.y & r.z & r.w) break;
    if (s > SPIN_CAP) { if (x.tid == 0) spin_fail(x); break; }
    v = __builtin_amdgcn_raw_buffer_load_b32(x.ws, flags + x.tid * 64, 0, AUX_SC1);
    __syncthreads();
  }
}

DI u4 pro_peek_wave(const Ctx& x, unsigned flags) {
  u4 v;
#pragma unroll
  for (int i = 0; i < 4; ++i) v[i] = __builtin_amdgcn_raw_buffer_load_b32(x.ws, flags + (64 * i + x.lane) * 64, 0, AUX_SC1);
  return v;
}
DI void pro_wait_wave(const Ctx& x, unsigned flags, unsigned tag, u4 v) {
  for (unsigned s = 0; !__all(v.x == tag && v.y == tag && v.z == tag && v.w == tag); ++s) {
    if (s > SPIN_CAP) { spin_fail(x); break; }
    asm volatile("" ::: "memory");
    v = pro_peek_wave(x, flags);
  }
}

DI void q_wsum(const u4 (&wf)[48], f4 (&ws)[3]) {
  const u4 ones = {0x3f803f80u, 0x3f803f80u, 0x3f803f80u, 0x3f803f80u};
  ws[0] = f4{0, 0, 0, 0}; ws[1] = ws[0]; ws[2] = ws[0];
#pragma unroll
  for (int ks = 0; ks < 16; ++ks)
#pragma unroll
    for (int m = 0; m < 3; ++m) ws[m] = mfma32(wf[16 * m + ks], ones, ws[m]);
}
struct QDma { unsigned v0, v1, base, lds; int layer; unsigned char* wsp; };
DI QDma q_dma_setup(const Ctx& x) {
  const int row = x.lane >> 4, pc = x.lane & 15;
  return QDma{(unsigned)(row * 256 + ((pc ^ (row << 1)) * 16)), (unsigned)(row * 256 + ((pc ^ ((4 + row) << 1)) * 16)),
              WS_X1 + x.layer * X1_BYTES + x.rb * (512 * 256) + x.w * 4096, (unsigned)(LD_XT + x.w * 4096), x.layer, x.wsp};
}
template <int OFF> DI void dma2_sc1(rsrc_t r, const QDma& d) {
  unsigned t;
  asm volatile("s_add_u32 m0, %5, %6\n\ts_add_u32 %0, %4, %6\n\t"
               "buffer_load_dwordx4 %1, %3, %0 offen sc1 lds\n\tbuffer_load_dwordx4 %2, %3, %0 offen offset:1024 sc1 lds"
               : "=&s"(t) : "v"(d.v0), "v"(d.v1), "s"(r), "s"(d.base), "s"(d.lds), "n"(OFF) : "memory", "scc");
}
template <int OFF> DI void dma1_sc1(rsrc_t r, const QDma& d) {
  unsigned t;
  asm volatile("s_add_u32 m0, %4, %5\n\ts_add_u32 %0, %3, %5\n\tbuffer_load_dwordx4 %1, %2, %0 offen sc1 lds"
               : "=&s"(t) : "v"((OFF & 1024) ? d.v1 : d.v0), "s"(r), "s"(d.base), "s"(d.lds), "n"(OFF) : "memory", "scc");
}
struct QAddr { lds_t* lo[4]; lds_t* hi[4]; };
DI void q_frag_addr(const Ctx& x, QAddr& ba) {
  const int g = x.lane >> 4, q = (x.lane & 15) >> 2, pp = x.lane & 3, ph = x.w >> 1;
  const int r7 = 4 * (g & 1) + q;
#pragma unroll
  for (int pt = 0; pt < 4; ++pt) {
    const int lc = 8 * ph + 2 * (pt ^ (2 * (x.w & 1))) + (pp >> 1);
    unsigned o = LD_XT + (4 * g + q) * 256 + ((lc ^ (r7 << 1)) * 16) + 8 * (pp & 1), o2 = o + 65536;
    asm volatile("" : "+v"(o), "+v"(o2));
    ba.lo[pt] = x.L + o; ba.hi[pt] = x.L + o2;
  }
}
template <int KS> DI u4 q_ldb1(const QAddr& ba, int pt) {
  lds_t* p = (KS < 8 ? ba.lo[pt] : ba.hi[pt]) + (KS & 7) * 8192;
  return cat(lds_tr(p), lds_tr(p + 4096));
}
template <int KS> DI void q_ldb(const QAddr& ba, u4 (&b)[4]) {
#pragma unroll
  for (int pt = 0; pt < 4; ++pt) b[pt] = q_ldb1<KS>(ba, pt);
}
template <int N> DI void wait_vm_barrier() { asm volatile("s_waitcnt vmcnt(%0)\n\ts_barrier" :: "n"(N) : "memory"); }

DI f4 bcast4(float r) {
  const f2 xy = swap16(r, r), ab = swap32(xy.x, xy.x), cd = swap32(xy.y, xy.y);
  return f4{ab.x, ab.y, cd.x, cd.y};
}
template <class F> DI void softmax4(const f4 (&t)[4], const float (&cs)[4], float csr, lds_t* scr_w, lds_t* scr_r, u4 (&out)[4], F mid) {
  float mx[4], mn[4];
#pragma unroll
  for (int pt = 0; pt < 4; ++pt) {
    mx[pt] = fmax2(fmax2(fmax2(t[pt][0], t[pt][1]), t[pt][2]), t[pt][3]);
    mn[pt] = fmin2(fmin2(fmin2(t[pt][0], t[pt][1]), t[pt][2]), t[pt][3]);
  }
  const f2 M = xrow4_maxmin(mx[0], mx[1], mx[2], mx[3], mn[0], mn[1], mn[2], mn[3]);
  *(LAS f2*)scr_w = M * f2{-csr, csr};
  f2 bb[4];
#pragma unroll
  for (int pt = 0; pt < 4; ++pt) bb[pt] = *(LAS f2*)(scr_r + 128 * pt);
  SB();
  mid();
  SB();
  f2 ep[4][2], en[4][2];
  float sp[4], sn[4];
#pragma unroll
  for (int pt = 0; pt < 4; ++pt) {
    const f2 b = bb[pt];
    const f2 t0 = {t[pt][0], t[pt][1]}, t1 = {t[pt][2], t[pt][3]};
    const f2 p0 = t0 * cs[pt] + b.x, p1 = t1 * cs[pt] + b.x, n0 = b.y - t0 * cs[pt], n1 = b.y - t1 * cs[pt];
    ep[pt][0] = f2{__builtin_amdgcn_exp2f(p0.x), __builtin_amdgcn_exp2f(p0.y)}; ep[pt][1] = f2{__builtin_amdgcn_exp2f(p1.x), __builtin_amdgcn_exp2f(p1.y)};
    en[pt][0] = f2{__builtin_amdgcn_exp2f(n0.x), __builtin_amdgcn_exp2f(n0.y)}; en[pt][1] = f2{__builtin_amdgcn_exp2f(n1.x), __builtin_amdgcn_exp2f(n1.y)};
    const f2 a = ep[pt][0] + ep[pt][1], c = en[pt][0] + en[pt][1];
    sp[pt] = a.x + a.y; sn[pt] = c.x + c.y;
  }
  const f4 ip = bcast4(__builtin_amdgcn_rcpf(xrow4<2>(sp[0], sp[1], sp[2], sp[3])));
  const f4 in = bcast4(__builtin_amdgcn_rcpf(xrow4<2>(sn[0], sn[1], sn[2], sn[3])));
#pragma unroll
  for (int pt = 0; pt < 4; ++pt)
    out[pt] = u4{pk(ep[pt][0] * ip[pt]), pk(ep[pt][1] * ip[pt]), pk(en[pt][0] * in[pt]), pk(en[pt][1] * in[pt])};
}

struct QState { u4 pq[4]; u2 res[4]; u4 pk8[8], az; };
DI void o_load_partials(const Ctx& x, QState& st) {
  const int h = x.w & 1, ph = x.w >> 1, hh = 2 * x.hp + h;
  const unsigned ko = WS_KVP + x.layer * KVP_BYTES + (hh * 16 + 8 * ph) * 1024 + x.lane * 16;
#pragma unroll
  for (int i = 0; i < 8; ++i) st.pk8[i] = ld16(x.ws, ko + i * 1024);
  st.az = ld16(x.ws, WS_ZP + x.layer * ZP_BYTES + (hh * 16 + (x.lane & 15)) * 64 + (x.lane >> 4) * 16);
}

template <int KS> struct QPass1 {
  static DI void run(rsrc_t ws, const QDma& d, const QAddr& ba, const u4 (&wf)[48], u4 (&b)[4], f4 (&ak)[4], f4 (&av)[4], f4 (&ag)[2]) {
    if (KS < 15 && (KS & 1)) wait_vm_barrier<(KS < 15 ? 8 + 2 * (KS < 10 ? KS : 10) - 4 * ((KS + 1) / 2) : 0)>();
    const u4 g0 = b[0], g1 = b[1];
#pragma unroll
    for (int pt = 0; pt < 4; ++pt) {
      const u4 cur = b[pt];
      lds_t* p = ((KS + 1) < 8 ? ba.lo[pt] : ba.hi[pt]) + ((KS + 1) & 7) * 8192;
      u2 t0, t1;
      if (KS == 0) mfma_va0(ak[pt], wf[16 + KS], cur); else mfma_va(ak[pt], wf[16 + KS], cur);
      if (KS < 15) t0 = lds_tr(p);
      SB();
      if (KS == 0) mfma_va0(av[pt], wf[32 + KS], cur); else mfma_va(av[pt], wf[32 + KS], cur);
      if (KS < 15) { t1 = lds_tr(p + 4096); b[pt] = cat(t0, t1); }
      SB();
    }
    if (KS == 0) mfma_vv0(ag[0], g0, g0); else mfma_vv(ag[0], g0, g0);
    SB();
    if (KS < 10) dma1_sc1<(3 + KS / 2) * 16384 + (KS & 1) * 2048>(ws, d);
    SB();
    if (KS == 0) mfma_vv0(ag[1], g1, g1); else mfma_vv(ag[1], g1, g1);
    SB();
    if (KS < 10) dma1_sc1<(3 + KS / 2) * 16384 + (KS & 1) * 2048 + 1024>(ws, d);
    SB();
    QPass1<KS + 1>::run(ws, d, ba, wf, b, ak, av, ag);
  }
};
template <> struct QPass1<16> { static DI void run(rsrc_t, const QDma&, const QAddr&, const u4 (&)[48], u4 (&)[4], f4 (&)[4], f4 (&)[4], f4 (&)[2]) {} };
template <int KS> struct QPass2 {
  static DI void run(const QAddr& ba, const u4 (&wf)[48], u4 (&b)[3][4], f4 (&aq)[4]) {
#pragma unroll
    for (int pt = 0; pt < 4; ++pt) {
      if (KS == 0) mfma_va0(aq[pt], wf[KS], b[0][pt]); else mfma_va(aq[pt], wf[KS], b[KS % 3][pt]);
      if (KS < 14) b[(KS + 2) % 3][pt] = q_ldb1<(KS < 14 ? KS + 2 : 15)>(ba, pt);
      SB();
    }
    QPass2<KS + 1>::run(ba, wf, b, aq);
  }
};
template <> struct QPass2<16> { static DI void run(const QAddr&, const u4 (&)[48], u4 (&)[3][4], f4 (&)[4]) {} };

DI void o_dma_wo(const Ctx& x, const Params& p) {
  const rsrc_t r = make_rsrc(p.wo, 512 * 512 * 2);
  const int row = x.lane >> 3, pc = x.lane & 7, cb = x.hp >> 1;
  const int b1 = (row >> 1) & 1;
  const unsigned v0 = row * 1024 + ((pc ^ (b1 << 1)) * 16), v1 = row * 1024 + ((pc ^ ((b1 | 2) << 1)) * 16) + 7168;
#pragma unroll 1
  for (int i = 4 * x.w; i < 4 * x.w + 4; ++i) dma4(r, v0, v1, v0 + 2 * 7168, v1 + 2 * 7168, i * 32768 + cb * 128, LD_XT + i * 4096);
}

DI void q_phase(const Ctx& x, const Params& p, const u4 (&wf)[48], const f4 (&wsum)[3], QState& st) {
  const QDma dm = q_dma_setup(x);
  dma2_sc1<0>(x.ws, dm); dma2_sc1<2048>(x.ws, dm); dma2_sc1<16384>(x.ws, dm); dma2_sc1<16384 + 2048>(x.ws, dm);
  dma2_sc1<32768>(x.ws, dm); dma2_sc1<32768 + 2048>(x.ws, dm);
  QAddr ba;
  q_frag_addr(x, ba);
  const int h = x.w & 1, ph = x.w >> 1, g = x.lane >> 4, cl = x.lane & 15;
  f4 ak[4], av[4], ag[2];
  STAMP(x.wsp, 9 + 16 * x.layer);
  {
    u4 b[4];
    wait_vm_barrier<8>();
    q_ldb<0>(ba, b);
    SB();
    QPass1<0>::run(x.ws, dm, ba, wf, b, ak, av, ag);
    mfma_done();
  }
  STAMP(x.wsp, 10 + 16 * x.layer);
  {
    const int r = cl & 3;
    const unsigned so = LD_STAT + 4 * (64 * ph + 32 * h + cl);
    const unsigned a0 = g == 0 ? so : LD_STAT + 1024, a1 = g == (cl >> 2) ? so + 512 : LD_STAT + 1024;
#pragma unroll
    for (int i = 0; i < 2; ++i) {
      const float dq = r == 0 ? ag[i][0] : r == 1 ? ag[i][1] : r == 2 ? ag[i][2] : ag[i][3];
      *(LAS float*)(x.L + a0 + 64 * i) = ak[i][0];
      *(LAS float*)(x.L + a1 + 64 * i) = dq;
    }
  }
  __syncthreads();
  float mean[4], rs[4], cs[4];
#pragma unroll
  for (int pt = 0; pt < 4; ++pt) {
    LAS float* S = (LAS float*)(x.L + LD_STAT) + 64 * ph + 16 * (pt ^ (2 * h)) + cl;
    mean[pt] = S[0] * (1.f / DM);
    rs[pt] = __builtin_amdgcn_rsqf(__builtin_fabsf((S[128] * (1.f / DM) + LN_EPS) - mean[pt] * mean[pt]));
    cs[pt] = rs[pt] * LOG2E;
  }
  const float csr = g == 0 ? cs[0] : g == 1 ? cs[2] : g == 2 ? cs[1] : cs[3];
  lds_t* scr_r = x.L + LD_SCR + x.w * 512 + cl * 8;
  lds_t* scr_w = scr_r + 128 * ((g >> 1) | ((g & 1) << 1));
  lds_t* pv = x.L + LD_PV + x.w * PV_WAVE;
  {
    f4 t[4];
#pragma unroll
    for (int pt = 0; pt < 4; ++pt) t[pt] = ak[pt] - mean[pt] * wsum[1];
    u4 pk4[4];
    softmax4(t, cs, csr, scr_w, scr_r, pk4, [&]() {
#pragma unroll
      for (int pt = 0; pt < 4; ++pt) {
        const f4 v = (av[pt] - mean[pt] * wsum[2]) * rs[pt];
        *(LAS u2*)(pv + (16 * pt + cl) * PV_ROW + 8 * g + 64) = u2{pack2(v[0], v[1]), pack2(v[2], v[3])};
      }
    });
#pragma unroll
    for (int pt = 0; pt < 4; ++pt) {
      lds_t* row = pv + (16 * pt + cl) * PV_ROW + 8 * g;
      *(LAS u2*)(row) = u2{pk4[pt].x, pk4[pt].y}; *(LAS u2*)(row + 32) = u2{pk4[pt].z, pk4[pt].w};
    }
  }
  f4 kv0 = {0, 0, 0, 0}, kv1 = kv0, z0 = kv0, z1 = kv0;
  {
    const u4 ones = {0x3f803f80u, 0x3f803f80u, 0x3f803f80u, 0x3f803f80u};
    const int q = cl >> 2, pp = cl & 3;
#pragma unroll
    for (int k2 = 0; k2 < 2; ++k2) {
      lds_t* a = pv + (32 * k2 + 8 * g + q) * PV_ROW + 8 * pp;
      const u4 a0 = cat(lds_tr(a), lds_tr(a + 4 * PV_ROW));
      const u4 a1 = cat(lds_tr(a + 32), lds_tr(a + 4 * PV_ROW + 32));
      const u4 bv = cat(lds_tr(a + 64), lds_tr(a + 4 * PV_ROW + 64));
      kv0 = mfma32(a0, bv, kv0); kv1 = mfma32(a1, bv, kv1); z0 = mfma32(a0, ones, z0); z1 = mfma32(a1, ones, z1);
    }
  }
  STAMP(x.wsp, 11 + 16 * x.layer);
  {
    lds_t* mine = x.L + LD_PV + x.w * PV_WAVE + x.lane * 16;
    if (ph == 1) { *(LAS f4*)(mine) = kv0; *(LAS f4*)(mine + 1024) = kv1; *(LAS f4*)(mine + 2048) = z0; *(LAS f4*)(mine + 3072) = z1; }
    __syncthreads();
    if (ph == 0) {
      lds_t* o = x.L + LD_PV + (x.w + 2) * PV_WAVE + x.lane * 16;
      kv0 += *(LAS f4*)(o); kv1 += *(LAS f4*)(o + 1024); z0 += *(LAS f4*)(o + 2048); z1 += *(LAS f4*)(o + 3072);
      const int hr = (2 * x.hp + h) * 16 + x.rb;
      st16(x.ws, WS_KVP + x.layer * KVP_BYTES + hr * 1024 + x.lane * 16,
           u4{pack2(kv0[0], kv0[1]), pack2(kv0[2], kv0[3]), pack2(kv1[0], kv1[1]), pack2(kv1[2], kv1[3])});
      if (cl == 0) st16(x.ws, WS_ZP + x.layer * ZP_BYTES + hr * 64 + g * 16,
                        u4{pack2(z0[0], z0[1]), pack2(z0[2], z0[3]), pack2(z1[0], z1[1]), pack2(z1[2], z1[3])});
    }
  }
  STAMP(x.wsp, 12 + 16 * x.layer);
  {
    {
      const int cb = x.hp >> 1, q = cl >> 2, pos = 64 * (x.hp & 1) + 16 * x.w + 4 * (cl & 3);
      const int row = 64 * cb + 4 * g + q;
      unsigned ro = LD_XT + row * 256 + (((pos >> 3) ^ ((row & 7) << 1)) * 16) + (pos & 4) * 2;
      asm volatile("" : "+v"(ro));
#pragma unroll
      for (int ct = 0; ct < 4; ++ct) st.res[ct] = lds_tr(x.L + ro + ct * 4096);
    }
    f4 aq[4];
    u4 b[3][4];
    q_ldb<0>(ba, b[0]); q_ldb<1>(ba, b[1]);
    SB();
    QPass2<0>::run(ba, wf, b, aq);
    mfma_done();
    f4 t[4];
#pragma unroll
    for (int pt = 0; pt < 4; ++pt) t[pt] = aq[pt] - mean[pt] * wsum[0];
    softmax4(t, cs, csr, scr_w, scr_r, st.pq, [&]() { o_load_partials(x, st); });
  }
  STAMP(x.wsp, 13 + 16 * x.layer);
}

DI void o_compute(const Ctx& x, QState& st) {
  const int h = x.w & 1, ph = x.w >> 1, g = x.lane >> 4, cl = x.lane & 15, hh = 2 * x.hp + h;
  STAMP(x.wsp, 14 + 16 * x.layer);
  const unsigned one = g == (cl >> 2) ? ((cl & 1) ? 0x3f800000u : 0x3f80u) : 0u;
  const unsigned e0 = (cl & 2) ? 0u : one, e1 = (cl & 2) ? one : 0u;
  const u4 sel0 = {e0, e1, 0u, 0u}, sel1 = {0u, 0u, e0, e1};
  f4 k0, k1, den[4];
  for (unsigned spin = 0;; ++spin) {
    k0 = f4{0, 0, 0, 0}; k1 = k0;
#pragma unroll
    for (int i = 0; i < 8; ++i) { k0 = mfma32(sel0, st.pk8[i], k0); k1 = mfma32(sel1, st.pk8[i], k1); }
    const u2 ones = {0x3f803f80u, 0x3f803f80u};
    f4 dp[4];
#pragma unroll
    for (int pt = 0; pt < 4; ++pt) dp[pt] = mfma32(st.az, st.pq[pt], f4{0, 0, 0, 0});
#pragma unroll
    for (int pt = 0; pt < 4; ++pt) den[pt] = mfma16(ones, u2{pack2(dp[pt][0], dp[pt][1]), pack2(dp[pt][2], dp[pt][3])}, f4{1e-6f, 1e-6f, 1e-6f, 1e-6f});
    const float chk = (k0[0] + k1[0]) + den[0][0];
    if (!__any(chk != chk)) break;
    if (spin > 4096u) { spin_fail(x); break; }
    o_load_partials(x, st);
  }
  lds_t* mine = x.L + LD_PV + x.w * PV_WAVE + (x.w >> 1) * 4096 + x.lane * 16;
  *(LAS f4*)(mine) = k0; *(LAS f4*)(mine + 1024) = k1;
  __syncthreads();
  STAMP(x.wsp, 15 + 16 * x.layer);
  lds_t* o = x.L + LD_PV + (x.w ^ 2) * PV_WAVE + ((x.w ^ 2) >> 1) * 4096 + x.lane * 16;
  k0 += *(LAS f4*)(o); k1 += *(LAS f4*)(o + 1024);
  const u4 akv = {pack2(k0[0], k0[1]), pack2(k0[2], k0[3]), pack2(k1[0], k1[1]), pack2(k1[2], k1[3])};
  f4 num[4];
#pragma unroll
  for (int pt = 0; pt < 4; ++pt) num[pt] = mfma32(akv, st.pq[pt], f4{0, 0, 0, 0});
  const unsigned oo = WS_OB + x.layer * OB_BYTES + ((2 * x.rb + ph) * 32 + hh) * 2048 + (16 * ((g & 1) ^ (2 * h)) + cl) * 32 + 16 * (g >> 1);
#pragma unroll
  for (int pt = 0; pt < 4; pt += 2) {
    const f4 va = num[pt] * __builtin_amdgcn_rcpf(den[pt][0]), vb = num[pt + 1] * __builtin_amdgcn_rcpf(den[pt + 1][0]);
    const auto s0 = __builtin_amdgcn_permlane16_swap(pack2(va[0], va[1]), pack2(vb[0], vb[1]), false, false);
    const auto s1 = __builtin_amdgcn_permlane16_swap(pack2(va[2], va[3]), pack2(vb[2], vb[3]), false, false);
    st16(x.ws, oo ^ (pt * 512), u4{s0[0], s1[0], s0[1], s1[1]});
  }
}

struct OAddr { lds_t* p[4]; };
template <int KS> DI void o_ldw(const OAddr& wb, u4 (&b)[4]) {
#pragma unroll
  for (int ct = 0; ct < 4; ++ct) b[ct] = cat(lds_tr(wb.p[ct] + KS * 4096), lds_tr(wb.p[ct] + KS * 4096 + 512));
}
DI unsigned umax3(unsigned a, unsigned b, unsigned c) { const unsigned m = a > b ? a : b; return m > c ? m : c; }
template <int KS> struct OGemm {
  static DI void run(const OAddr& wb, const u4 (&of)[16], u4 (&b)[3][4], f4 (&acc)[4], unsigned& top) {
#pragma unroll
    for (int ct = 0; ct < 4; ++ct) {
      if (KS == 0) mfma_vv0(acc[ct], b[0][ct], of[KS]); else mfma_vv(acc[ct], b[KS % 3][ct], of[KS]);
      if (KS < 14) b[(KS + 2) % 3][ct] = cat(lds_tr(wb.p[ct] + (KS + 2) * 4096), lds_tr(wb.p[ct] + (KS + 2) * 4096 + 512));
      if (ct == 1) top = umax3(top, of[KS].x, of[KS].y);
      if (ct == 3) top = umax3(top, of[KS].z, of[KS].w);
      SB();
    }
    OGemm<KS + 1>::run(wb, of, b, acc, top);
  }
};
template <> struct OGemm<16> { static DI void run(const OAddr&, const u4 (&)[16], u4 (&)[3][4], f4 (&)[4], unsigned&) {} };

DI void o_gemm(const Ctx& x, const Params& p, const QState& st, u4 (&kd)[3]) {
  const int g = x.lane >> 4, cl = x.lane & 15, pb = 2 * x.rb + (x.hp & 1), cb = x.hp >> 1;
  const unsigned oo = WS_OB + x.layer * OB_BYTES + ((pb * 32 + (g >> 1)) * 64 + 16 * x.w + cl) * 32 + 16 * (g & 1);
  OAddr wb;
  {
    const int q = cl >> 2, pp = cl & 3, f = ((q >> 1) & 1) | ((g & 1) << 1);
#pragma unroll
    for (int ct = 0; ct < 4; ++ct) wb.p[ct] = x.L + LD_XT + (8 * g + q) * 128 + (((2 * ct + (pp >> 1)) ^ (f << 1)) * 16) + 8 * (pp & 1);
  }
  f4 acc[4];
  o_dma_wo(x, p);
  for (unsigned spin = 0;; ++spin) {
    u4 of[16];
#pragma unroll
    for (int ks = 0; ks < 16; ++ks) of[ks] = ld16(x.ws, oo + ks * 4096);
    if (spin == 0) wait_vm_barrier<16>();
    STAMP(x.wsp, 17 + 16 * x.layer);
    u4 b[3][4];
    o_ldw<0>(wb, b[0]); o_ldw<1>(wb, b[1]);
    SB();
    unsigned top = 0;
    OGemm<0>::run(wb, of, b, acc, top);
    mfma_done();
    if (!__any(top == ~0u)) break;
    if (spin > 4096u) { spin_fail(x); break; }
  }
  STAMP(x.wsp, 18 + 16 * x.layer);
  f2 s2 = {0.f, 0.f}, q2 = s2;
  unsigned so = LD_OST + (16 * x.w + cl) * 144 + 8 * g;
  asm volatile("" : "+v"(so));
#pragma unroll
  for (int ct = 0; ct < 4; ++ct) {
    const f2 a = unpk(st.res[ct].x) + f2{acc[ct][0], acc[ct][1]}, b2 = unpk(st.res[ct].y) + f2{acc[ct][2], acc[ct][3]};
    s2 += a + b2; q2 += a * a; q2 += b2 * b2;
    *(LAS u2*)(x.L + so + 32 * ct) = u2{pk(a), pk(b2)};
  }
  if (x.layer == 0) {
    const f2 sq = xrow_sum2(s2.x + s2.y, q2.x + q2.y);
    if (g < 2) {
      GAS float* d = (GAS float*)(p.ws + WS_ST + 2048 * 8) + g * 2048 + 64 * pb + 16 * x.w + cl;
      __hip_atomic_fetch_add(d, g ? sq.y : sq.x, RLX_AGENT);
    }
    conv_load_k(x, kd);
  }
  __syncthreads();
  if (x.layer == 0) {
    const int qq = cl >> 2, pp = cl & 3;
#pragma unroll
    for (int j = 0; j < 2; ++j) {
      lds_t* a = x.L + LD_OST + (8 * (g + 4 * j) + qq) * 144 + (16 * x.w + 4 * pp) * 2;
      const u4 v = cat(lds_tr(a), lds_tr(a + 4 * 144));
      st16(x.ws, WS_XT + XT_BYTES + ((64 * cb + 16 * x.w + cl) * XT_STRIDE + 64 * pb + 8 * (g + 4 * j)) * 2, v);
    }
  } else {
    const rsrc_t ro = make_rsrc(p.out, SEQ * DM * 2);
#pragma unroll
    for (int i = 0; i < 2; ++i) {
      const int k = i * 256 + x.tid, pos = k >> 3, q8 = k & 7;
      __builtin_amdgcn_raw_buffer_store_b128(*(LAS u4*)(x.L + LD_OST + pos * 144 + q8 * 16), ro, ((64 * pb + pos) * DM + 64 * cb + 8 * q8) * 2, 0, 0);
    }
  }
  STAMP(x.wsp, 19 + 16 * x.layer);
}

DI Ctx make_ctx(const Params& p, unsigned char* smem) {
  Ctx x;
  x.L = (lds_t*)smem; x.ws = make_rsrc(p.ws, WS_END); x.cw = (GAS unsigned*)(p.ws + WS_CTRL); x.wsp = p.ws;
  x.tid = threadIdx.x; x.lane = x.tid & 63; x.w = __builtin_amdgcn_readfirstlane(x.tid >> 6); x.c = blockIdx.x;
  x.cp = (x.c & 7) * 32 + (x.c >> 3);
  x.rb = 2 * (x.c & 7) + (x.c >> 7); x.hp = (x.c >> 3) & 15; x.layer = 0;
  return x;
}

DI Ctx fresh(const Ctx& x, int layer) { Ctx y = x; int t = x.tid; asm volatile("" : "+v"(t)); y.tid = t; y.lane = t & 63; y.layer = layer; return y; }

__global__ void __launch_bounds__(256, 1) k_main(Params p) {
  extern __shared__ __attribute__((aligned(16))) unsigned char smem[];
  const unsigned char* code0;
  asm volatile("s_getpc_b64 %0" : "=s"(code0));
  const Ctx x0 = make_ctx(p, smem);
  STAMP(p.ws, 0);
  u4 wf[48];
  f4 wsum[3];
  const float dsk = bf2f(p.dsk[2 * x0.cp + (x0.w >> 1)]);
  unsigned tag;
  {
    const Ctx x = fresh(x0, 0);
    u4 kd[3], a[32];
    ProIn pin;
    Warm wm;
    wm.a = pro_warm_code(x, code0);
    pro_load(x, p, pin);
    pro_init(x);
    tag = pin.epoch + 1u;
    pro_publish_x(x, pin);
    SB();
    pro_fold_w(x, 6 * x.c + x.w, pin.w[0]);
    if (x.w < 2) { pro_fold_w(x, 6 * x.c + 4 + x.w, pin.w[1]); asm volatile("s_waitcnt vmcnt(2)" ::: "memory"); }
    else asm volatile("s_waitcnt vmcnt(1)" ::: "memory");
    __syncthreads();
    if (x.tid == 0) __builtin_amdgcn_raw_buffer_store_b32(tag, x.ws, WS_FLAG + x.c * 64, 0, AUX_SC1);
    STAMP(p.ws, 1);
    pro_tables(x, pin);
    drain();
    __syncthreads();
    if (x.tid == 0) __builtin_amdgcn_raw_buffer_store_b32(tag, x.ws, WS_FLAG2 + x.c * 64, 0, AUX_SC1);
    STAMP(p.ws, 2);
    const unsigned fl = __builtin_amdgcn_raw_buffer_load_b32(x.ws, WS_FLAG + x.tid * 64, 0, AUX_SC1);
    pro_kgemm(x);
    pro_wait(x, WS_FLAG, tag, fl);
    STAMP(p.ws, 3);
    ConvIn in;
    conv_load_in(x, 0, in);
    SB();
    pro_expand(x);
    __syncthreads();
    pro_take_k(x, kd);
    conv_build_k(x, kd);
    STAMP(p.ws, 4);
    conv_build_u(x, in);
    const u4 fl2 = pro_peek_wave(x, WS_FLAG2);
    __syncthreads();
    STAMP(p.ws, 5);
    pro_wait_wave(x, WS_FLAG2, tag, fl2);
    STAMP3(x, 36);
    pro_warm(x, p, wm);
    if (x.w & 1) conv_wave<1, true>(x, dsk, a); else conv_wave<0, true>(x, dsk, a);
    pro_warm_retire(wm);
    a2a_post(fresh(x0, 0), 0);
    poison_o(fresh(x0, 0), 1);
    q_take_w(fresh(x0, 0), wf);
    q_wsum(wf, wsum);
  }
#pragma unroll 1
  for (int layer = 0; layer < 2; ++layer) {
    Ctx x = fresh(x0, layer);
    a2a_wait(x, 2 * layer);
    STAMP(p.ws, 8 + 16 * layer);
    QState st;
    x = fresh(x0, layer);
    q_phase(x, p, wf, wsum, st);
    x = fresh(x0, layer);
    o_compute(x, st);
    STAMP(p.ws, 16 + 16 * layer);
    x = fresh(x0, layer);
    u4 kd[3];
    o_gemm(x, p, st, kd);
    if (layer == 0) {
      u4 a[32];
      conv_build_k(x, kd);
      a2a_signal(x, 1);
      x = fresh(x0, 1);
      if (x.w & 1) conv_load_a<1>(x, a); else conv_load_a<0>(x, a);
      SB();
      a2a_wait(x, 1);
      STAMP(p.ws, 20);
      ConvIn in;
      conv_load_in(x, 1, in);
      conv_build_u(x, in);
        __syncthreads();
      STAMP(p.ws, 21);
      if (x.w & 1) conv_wave<1, false>(x, dsk, a); else conv_wave<0, false>(x, dsk, a);
      a2a_post(fresh(x0, 1), 2);
    }
  }
  if (x0.c == 0 && x0.tid == 0) __builtin_amdgcn_raw_buffer_store_b32(tag, x0.ws, WS_EPOCH, 0, AUX_SC1);
}

extern "C" void kernel_launch(void* const* d_in, const int* in_sizes, int n_in, void* d_out, int out_size, void* d_ws, size_t ws_size,
                              hipStream_t stream) {
  Params p;
  p.u = (const unsigned short*)d_in[0]; p.lar = (const unsigned short*)d_in[1]; p.aim = (const unsigned short*)d_in[2];
  p.cre = (const unsigned short*)d_in[3]; p.cim = (const unsigned short*)d_in[4]; p.ldt = (const unsigned short*)d_in[5];
  p.dsk = (const unsigned short*)d_in[6]; p.wqkv = (const unsigned short*)d_in[8]; p.wphi = (const unsigned short*)d_in[9];
  p.wo = (const unsigned short*)d_in[10]; p.out = (unsigned short*)d_out; p.ws = (unsigned char*)d_ws;
  (void)hipFuncSetAttribute((const void*)k_main, hipFuncAttributeMaxDynamicSharedMemorySize, LDS_BYTES);
  k_main<<<256, 256, LDS_BYTES, stream>>>(p);
}
```

```cpp
#include <hip/hip_runtime.h>
#include <hip/hip_bf16.h>
#include <stdint.h>

#define MK_STAMP 0
#define STAMP(ws, i) do { } while (0)
#define STAMP2(x, i) do { } while (0)
#define STAMP3(x, i) do { } while (0)
typedef short s4 __attribute__((ext_vector_type(4)));
typedef short s8 __attribute__((ext_vector_type(8)));
typedef float f2 __attribute__((ext_vector_type(2)));
typedef float f4 __attribute__((ext_vector_type(4)));
typedef unsigned u2 __attribute__((ext_vector_type(2)));
typedef unsigned u4 __attribute__((ext_vector_type(4)));
typedef __bf16 bf2 __attribute__((ext_vector_type(2)));
#define GAS __attribute__((address_space(1)))
#define LAS __attribute__((address_space(3)))
#define DI __device__ __forceinline__
#define RLX_AGENT __ATOMIC_RELAXED, __HIP_MEMORY_SCOPE_AGENT
typedef GAS unsigned gu32;
typedef LAS unsigned char lds_t;
typedef __amdgpu_buffer_rsrc_t rsrc_t;

constexpr int SEQ = 2048, DM = 512, NST = 128, NH = 32;
constexpr float LOG2E = 1.4426950408889634f;
constexpr float LN_EPS = 1e-5f;
constexpr int AUX_SC1 = 16;

constexpr int XT_STRIDE = 2112;
constexpr unsigned WS_CTRL = 0;
constexpr unsigned WS_KREV = 262144;
constexpr unsigned WS_WFRAG = WS_KREV + 512u * 2048 * 2;
constexpr unsigned WS_XT = WS_WFRAG + 32u * 3 * 16 * 1024;
constexpr unsigned XT_BYTES = 512u * XT_STRIDE * 2;
constexpr unsigned WS_ST = WS_XT + 2 * XT_BYTES;
constexpr unsigned WS_X1 = WS_ST + 2u * 2048 * 8;
constexpr unsigned X1_BYTES = 2048u * 512 * 2;
constexpr unsigned WS_KVP = WS_X1 + 2 * X1_BYTES;
constexpr unsigned KVP_BYTES = 32u * 16 * 1024;
constexpr unsigned WS_ZP = WS_KVP + 2 * KVP_BYTES;
constexpr unsigned ZP_BYTES = 32u * 16 * 64;
constexpr unsigned WS_OB = WS_ZP + 2 * ZP_BYTES;
constexpr unsigned OB_BYTES = 2048u * 512 * 2;
constexpr unsigned WS_END = WS_OB + 2 * OB_BYTES;
constexpr int CW_TMO = 0;
#define A2A_NS 16
#define A2A_REP 8
constexpr int NS = A2A_NS, REP = A2A_REP;
constexpr int NCW_A2A = 3 * REP * NS;
constexpr int CW_STRIDE = 64;
constexpr int CWI_A2A = 1, CWI_END = CWI_A2A + NCW_A2A;
constexpr unsigned WS_EPOCH = 131072, WS_FLAG = WS_EPOCH + 256, WS_FLAG2 = WS_FLAG + 256 * 64;
static_assert(CWI_END <= 512 && CWI_END * CW_STRIDE * 4 <= WS_EPOCH && WS_FLAG2 + 256 * 64 <= WS_KREV, "control block");

struct Params {
  const unsigned short* u;
  const unsigned short* lar;
  const unsigned short* aim;
  const unsigned short* cre;
  const unsigned short* cim;
  const unsigned short* ldt;
  const unsigned short* dsk;
  const unsigned short* wqkv;
  const unsigned short* wphi;
  const unsigned short* wo;
  unsigned short* out;
  unsigned char* ws;
};

DI float bf2f(unsigned short b) { return __builtin_bit_cast(float, (unsigned)b << 16); }
DI float blo(unsigned x) { return __builtin_bit_cast(float, x << 16); }
DI float bhi(unsigned x) { return __builtin_bit_cast(float, x & 0xffff0000u); }
DI unsigned pack2(float a, float b) { bf2 v = {(__bf16)a, (__bf16)b}; return __builtin_bit_cast(unsigned, v); }
DI float rbf(float a) { return blo(pack2(a, a)); }
DI f4 mfma32(u4 a, u4 b, f4 c) { return __builtin_amdgcn_mfma_f32_16x16x32_bf16(__builtin_bit_cast(s8, a), __builtin_bit_cast(s8, b), c, 0, 0, 0); }
DI f4 mfma16(u2 a, u2 b, f4 c) { return __builtin_amdgcn_mfma_f32_16x16x16bf16_1k(__builtin_bit_cast(s4, a), __builtin_bit_cast(s4, b), c, 0, 0, 0); }
DI void mfma_va(f4& acc, const u4& a, const u4& b) { asm volatile("v_mfma_f32_16x16x32_bf16 %0, %1, %2, %0" : "+v"(acc) : "a"(a), "v"(b)); }
DI void mfma_va0(f4& acc, const u4& a, const u4& b) { asm volatile("v_mfma_f32_16x16x32_bf16 %0, %1, %2, 0" : "=&v"(acc) : "a"(a), "v"(b)); }
DI void mfma_vv(f4& acc, const u4& a, const u4& b) { asm volatile("v_mfma_f32_16x16x32_bf16 %0, %1, %2, %0" : "+v"(acc) : "v"(a), "v"(b)); }
DI void mfma_vv0(f4& acc, const u4& a, const u4& b) { asm volatile("v_mfma_f32_16x16x32_bf16 %0, %1, %2, 0" : "=&v"(acc) : "v"(a), "v"(b)); }
DI void mfma16_vvc(f4& acc, const u2& a, const u2& b) { asm volatile("v_mfma_f32_16x16x16_bf16 %0, %1, %2, %0" : "+v"(acc) : "v"(a), "v"(b)); }
DI void valu_to_mfma() { asm volatile("s_nop 1"); }
DI void mfma_done() { asm volatile("s_nop 7" ::: "memory"); __builtin_amdgcn_sched_barrier(0); }
DI u2 lds_tr(lds_t* p) { return __builtin_bit_cast(u2, __builtin_amdgcn_ds_read_tr16_b64_v4i16((LAS s4*)p)); }
DI u4 cat(u2 a, u2 b) { return u4{a.x, a.y, b.x, b.y}; }
DI rsrc_t make_rsrc(const void* p, unsigned bytes) { return __builtin_amdgcn_make_buffer_rsrc((void*)p, 0, (int)bytes, 0x00020000); }
DI u4 ld16(rsrc_t r, unsigned off) { return __builtin_amdgcn_raw_buffer_load_b128(r, off, 0, AUX_SC1); }
DI void st16(rsrc_t r, unsigned off, u4 v) { __builtin_amdgcn_raw_buffer_store_b128(v, r, off, 0, AUX_SC1); }
DI void drain() { asm volatile("s_waitcnt vmcnt(0)" ::: "memory"); }
DI void settle() { __builtin_amdgcn_s_waitcnt(0x0F70); }
DI void dma4_sc1(rsrc_t r, unsigned v0, unsigned v1, unsigned soff, unsigned lds) {
  asm volatile("s_mov_b32 m0, %4\n\ts_nop 0\n\t"
               "buffer_load_dwordx4 %0, %2, %3 offen sc1 lds\n\tbuffer_load_dwordx4 %1, %2, %3 offen offset:1024 sc1 lds\n\t"
               "buffer_load_dwordx4 %0, %2, %3 offen offset:2048 sc1 lds\n\tbuffer_load_dwordx4 %1, %2, %3 offen offset:3072 sc1 lds"
               :: "v"(v0), "v"(v1), "s"(r), "s"(soff), "s"(lds) : "memory");
}
DI void dma4(rsrc_t r, unsigned v0, unsigned v1, unsigned v2, unsigned v3, unsigned soff, unsigned lds) {
  asm volatile("s_mov_b32 m0, %6\n\ts_nop 0\n\t"
               "buffer_load_dwordx4 %0, %4, %5 offen lds\n\tbuffer_load_dwordx4 %1, %4, %5 offen offset:1024 lds\n\t"
               "buffer_load_dwordx4 %2, %4, %5 offen offset:2048 lds\n\tbuffer_load_dwordx4 %3, %4, %5 offen offset:3072 lds"
               :: "v"(v0), "v"(v1), "v"(v2), "v"(v3), "s"(r), "s"(soff), "s"(lds) : "memory");
}
#define SB() __builtin_amdgcn_sched_barrier(0)
#define SGB(mask, n) __builtin_amdgcn_sched_group_barrier(mask, n, 0)
constexpr int SG_MFMA = 0x8, SG_DSR = 0x100;
template <int CTRL> DI float dpp_f(float v) { return __builtin_bit_cast(float, __builtin_amdgcn_mov_dpp(__builtin_bit_cast(int, v), CTRL, 0xf, 0xf, true)); }
DI float row_sum(float v) { v += dpp_f<0x128>(v); v += dpp_f<0x124>(v); v += dpp_f<0x122>(v); v += dpp_f<0x121>(v); return v; }
DI f2 swap32(float a, float b) { auto r = __builtin_amdgcn_permlane32_swap(__builtin_bit_cast(unsigned, a), __builtin_bit_cast(unsigned, b), false, false);
  const unsigned r0 = r[0], r1 = r[1];
  return f2{__builtin_bit_cast(float, r0), __builtin_bit_cast(float, r1)}; }
DI f2 swap16(float a, float b) { auto r = __builtin_amdgcn_permlane16_swap(__builtin_bit_cast(unsigned, a), __builtin_bit_cast(unsigned, b), false, false);
  const unsigned r0 = r[0], r1 = r[1];
  return f2{__builtin_bit_cast(float, r0), __builtin_bit_cast(float, r1)}; }
DI float fmax2(float a, float b) { return __builtin_fmaxf(a, b); }
DI float fmin2(float a, float b) { return __builtin_fminf(a, b); }
template <int OP> DI float op2(float a, float b) { return OP == 0 ? fmax2(a, b) : OP == 1 ? fmin2(a, b) : a + b; }
template <int OP> DI float xrow4(float a, float b, float c, float d) {
  f2 s = swap32(a, b); const float ab = op2<OP>(s.x, s.y);
  s = swap32(c, d); const float cd = op2<OP>(s.x, s.y);
  s = swap16(ab, cd); return op2<OP>(s.x, s.y);
}
DI f2 xrow4_maxmin(float a0, float a1, float a2, float a3, float b0, float b1, float b2, float b3) {
  asm("s_nop 1\n\t"
      "v_permlane32_swap_b32 %0, %1\n\tv_permlane32_swap_b32 %2, %3\n\tv_permlane32_swap_b32 %4, %5\n\tv_permlane32_swap_b32 %6, %7\n\t"
      "v_max_f32 %0, %0, %1\n\tv_max_f32 %2, %2, %3\n\tv_min_f32 %4, %4, %5\n\tv_min_f32 %6, %6, %7\n\t"
      "v_permlane16_swap_b32 %0, %2\n\ts_nop 0\n\tv_permlane16_swap_b32 %4, %6\n\t"
      "v_max_f32 %0, %0, %2\n\tv_min_f32 %4, %4, %6"
      : "+v"(a0), "+v"(a1), "+v"(a2), "+v"(a3), "+v"(b0), "+v"(b1), "+v"(b2), "+v"(b3));
  return f2{a0, b0};
}
DI f2 xrow_max2(float a, float b) { f2 s = swap32(a, b); float c = fmaxf(s.x, s.y); s = swap16(c, c); c = fmaxf(s.x, s.y); return swap32(c, c); }
DI f2 xrow_sum2(float a, float b) { f2 s = swap32(a, b); float c = s.x + s.y; s = swap16(c, c); c = s.x + s.y; return swap32(c, c); }
DI float wave_sum(float v) { v = row_sum(v); f2 s = swap16(v, v); v = s.x + s.y; s = swap32(v, v); return s.x + s.y; }

constexpr int LD_XT = 0;
constexpr int LD_KC = 65536;
constexpr int KC_COPY = 4160, KC_CH = 4 * KC_COPY;
constexpr int LD_U = LD_KC + 2 * KC_CH;
constexpr int U_CH = 4608;
constexpr int LD_X = LD_U + 2 * U_CH;
constexpr int LD_EX = LD_KC;
constexpr int LD_CONV_END = LD_X + 8192;
constexpr int LD_WA = 0, LD_WB = LD_CONV_END;
constexpr int LD_MISC = 131072;
constexpr int LD_PV = LD_MISC;
constexpr int PV_ROW = 104, PV_WAVE = 64 * PV_ROW;
constexpr int LD_STAT = LD_PV + 4 * PV_WAVE;
constexpr int LD_SCR = LD_STAT + 1024 + 256;
constexpr int LD_FLAG = LD_SCR + 2048;
constexpr int LD_OST = LD_PV;
constexpr int LDS_BYTES = 163840;
static_assert(LD_CONV_END <= LD_MISC && LD_FLAG + 64 <= LDS_BYTES, "LDS map");

struct Ctx {
  lds_t* L; rsrc_t ws; GAS unsigned* cw; unsigned char* wsp;
  int tid, lane, w, c, cp, rb, hp, layer;
};

DI void spin_fail(const Ctx& x) { __hip_atomic_store(x.cw + CW_TMO, 1u, RLX_AGENT); }
constexpr unsigned SPIN_CAP = 1u << 18;
DI void a2a_post(const Ctx& x, int e) {
  __syncthreads();
  if (x.tid < REP) __hip_atomic_fetch_add(x.cw + (CWI_A2A + (e * REP + x.tid) * NS + (x.c >> 3) % NS) * CW_STRIDE, 1u, RLX_AGENT);
}
DI void a2a_signal(const Ctx& x, int e) { drain(); a2a_post(x, e); }
DI GAS unsigned* a2a_word(const Ctx& x, int e) { return x.cw + (CWI_A2A + (e * REP + (x.c & 7) % REP) * NS + x.lane % NS) * CW_STRIDE; }
DI unsigned a2a_peek(const Ctx& x, int e) { return x.w == 0 ? __hip_atomic_load(a2a_word(x, e), RLX_AGENT) : 0u; }
DI void a2a_wait(const Ctx& x, int e, unsigned first) {
  if (x.w == 0) {
    if (!__all(first >= 256u / NS)) {
      GAS unsigned* q = a2a_word(x, e);
      for (unsigned s = 0;; ++s) {
        if (__all(__hip_atomic_load(q, RLX_AGENT) >= 256u / NS)) break;
        if (s > SPIN_CAP) { spin_fail(x); break; }
        __builtin_amdgcn_s_sleep(1);
      }
    }
    __builtin_amdgcn_fence(__ATOMIC_ACQUIRE, "wavefront");
  }
  settle();
  __syncthreads();
}

DI void conv_load_k(const Ctx& x, u4 (&kd)[3]) {
  const int ch = x.tid >> 7, q = x.tid & 127;
  const unsigned off = WS_KREV + ((2 * x.cp + ch) * 2048 + 16 * q) * 2;
  kd[0] = __builtin_amdgcn_raw_buffer_load_b128(x.ws, off, 0, 0);
  kd[1] = __builtin_amdgcn_raw_buffer_load_b128(x.ws, off + 16, 0, 0);
  kd[2] = __builtin_amdgcn_raw_buffer_load_b128(x.ws, off + 32, 0, 0);
}
DI void conv_build_k(const Ctx& x, const u4 (&kd)[3]) {
  const int ch = x.tid >> 7, q = x.tid & 127;
  const unsigned d8 = q == 127 ? 0u : kd[2].x, d9 = q == 127 ? 0u : kd[2].y;
  const unsigned d[10] = {kd[0].x, kd[0].y, kd[0].z, kd[0].w, kd[1].x, kd[1].y, kd[1].z, kd[1].w, d8, d9};
  lds_t* b = x.L + LD_KC + ch * KC_CH + 32 * q;
  *(LAS u4*)(b) = u4{d[0], d[1], d[2], d[3]}; *(LAS u4*)(b + 16) = u4{d[4], d[5], d[6], d[7]};
  *(LAS u4*)(b + 2 * KC_COPY) = u4{d[1], d[2], d[3], d[4]}; *(LAS u4*)(b + 2 * KC_COPY + 16) = u4{d[5], d[6], d[7], d[8]};
  unsigned e[9];
#pragma unroll
  for (int i = 0; i < 9; ++i) e[i] = __builtin_amdgcn_alignbit(d[i + 1], d[i], 16);
  *(LAS u4*)(b + KC_COPY) = u4{e[0], e[1], e[2], e[3]}; *(LAS u4*)(b + KC_COPY + 16) = u4{e[4], e[5], e[6], e[7]};
  *(LAS u4*)(b + 3 * KC_COPY) = u4{e[1], e[2], e[3], e[4]}; *(LAS u4*)(b + 3 * KC_COPY + 16) = u4{e[5], e[6], e[7], e[8]};
  if (x.tid < 16) {
    *(LAS u4*)(x.L + LD_KC + (x.tid >> 1) * KC_COPY + 4096 + (x.tid & 1) * 16) = u4{0, 0, 0, 0};
  }
  if (x.tid >= 64 && x.tid < 128) {
    const int i = x.tid - 64;
    *(LAS u4*)(x.L + LD_U + (i >> 5) * U_CH + (i & 31) * 16) = u4{0, 0, 0, 0};
  }
}
template <int PAR> struct Mu { static constexpr int v(int k) { return PAR ? (k == 0 ? 1 : k == 1 ? 2 : k == 2 ? 5 : 6) : (k == 0 ? 0 : k == 1 ? 3 : k == 2 ? 4 : 7); } };
template <int PAR> constexpr bool conv_first(int tau, int i, int k) {
  if (i) return false;
  for (int t2 = 0; t2 < tau; ++t2)
    for (int k2 = 0; k2 < 4; ++k2)
      if (Mu<PAR>::v(k2) + t2 == Mu<PAR>::v(k) + tau) return false;
  return true;
}
template <int PAR> DI lds_t* conv_a_base(const Ctx& x) {
  const int b = x.lane & 15, kq = x.lane >> 4, ch = x.w >> 1;
  const int s0 = 2047 - 16 * (kq >> 1) - b + 8 * (kq & 1), cc = s0 & 3;
  unsigned bo = LD_KC + ch * KC_CH + cc * KC_COPY + 2 * (s0 - cc) - 64 * 63;
  asm volatile("" : "+v"(bo));
  return x.L + bo;
}
template <int PAR> DI u4 conv_a_frag(lds_t* base, int k, int i) {
  const int m = 8 * Mu<PAR>::v(k) + i;
  const u2 lo = *(volatile LAS u2*)(base + 64 * (63 - m)), hi = *(volatile LAS u2*)(base + 64 * (63 - m) + 8);
  return cat(lo, hi);
}
template <int PAR, int K0> DI void conv_load_a(const Ctx& x, u4 (&a)[32]) {
  lds_t* base = conv_a_base<PAR>(x);
#pragma unroll
  for (int k = K0; k < K0 + 2; ++k)
#pragma unroll
    for (int i = 0; i < 8; ++i) a[8 * k + i] = conv_a_frag<PAR>(base, k, i);
}
struct WDma { unsigned lane16, soff, lo, hi; };
DI WDma q_dma_w_setup(const Ctx& x) {
  const unsigned lo = x.w < 3 ? LD_WA + x.w * 24576 : LD_WB + 8192, hi = x.w < 2 ? lo : x.w == 2 ? LD_WB - 16384 : lo;
  return WDma{(unsigned)x.lane * 16, WS_WFRAG + x.hp * (96 * 1024) + x.w * 24576, lo, hi};
}
template <int I> DI void q_dma_w1(const Ctx& x, const WDma& d) {
  unsigned t;
  asm volatile("s_add_u32 m0, %4, %5\n\ts_add_u32 %0, %3, %5\n\tbuffer_load_dwordx4 %1, %2, %0 offen sc1 lds"
               : "=&s"(t) : "v"(d.lane16), "s"(x.ws), "s"(d.soff), "s"(I < 16 ? d.lo : d.hi), "n"(I * 1024) : "memory", "scc");
}
DI void q_take_w(const Ctx& x, u4 (&wf)[48]) {
  const int h = x.w & 1;
  const unsigned a0 = LD_WA + h * 49152 + x.lane * 16, a1 = (h ? LD_WB - 16384 : LD_WA) + x.lane * 16;
#pragma unroll
  for (int i = 0; i < 48; ++i)
    asm volatile("ds_read_b128 %0, %1 offset:%2" : "=a"(wf[i]) : "v"(i < 16 ? a0 : a1), "n"(i * 1024));
  asm volatile("s_waitcnt lgkmcnt(0)" ::: "memory");
  SB();
}
template <int TAU, int I> DI void conv_dma_w(const Ctx& x, const WDma& d) { if ((I & 1) && TAU < 6) { q_dma_w1<(TAU < 6 ? 4 * TAU + (I >> 1) : 0)>(x, d); SB(); } }
template <int PAR, bool FIRST> DI void conv_mfma(const Ctx& x, u4 (&a)[32], f4 (&acc)[8]) {
  const WDma wd = q_dma_w_setup(x);
  const int al = x.lane & 15, kq = x.lane >> 4, ch = x.w >> 1;
  unsigned bo = LD_U + ch * U_CH + 2 * (256 + 16 * al - 16 * (kq >> 1) + 8 * (kq & 1)) - 64 * 7;
  asm volatile("" : "+v"(bo));
  lds_t* base = x.L + bo;
  constexpr int NT = 8 - Mu<PAR>::v(0);
  u4 b[8];
  if (FIRST) {
    STAMP3(x, 37);
    lds_t* abase = conv_a_base<PAR>(x);
#pragma unroll
    for (int i = 0; i < 8; ++i) { b[i] = *(LAS u4*)(base + 64 * (7 - i)); a[i] = conv_a_frag<PAR>(abase, 0, i); }
    SB();
#pragma unroll
    for (int k = 0; k < 4; ++k) {
#pragma unroll
      for (int i = 0; i < 8; ++i) {
        if (i == 0) mfma_vv0(acc[Mu<PAR>::v(k)], a[8 * k + i], b[i]); else mfma_vv(acc[Mu<PAR>::v(k)], a[8 * k + i], b[i]);
        if (k < 3) a[8 * (k + 1) + i] = conv_a_frag<PAR>(abase, k + 1, i);
        else b[i] = *(LAS u4*)(base + 64 * (8 - i + 7));
        SB();
      }
      switch (k) {
        case 0: q_dma_w1<0>(x, wd); break; case 1: q_dma_w1<1>(x, wd); break;
        case 2: q_dma_w1<2>(x, wd); break; default: q_dma_w1<3>(x, wd); break;
      }
      SB();
    }
    STAMP3(x, 38);
  } else {
#pragma unroll
    for (int i = 0; i < 8; ++i) b[i] = *(LAS u4*)(base + 64 * (7 - i));
    SB();
  }
#pragma unroll
  for (int tau = FIRST ? 1 : 0; tau < 8; ++tau) {
    if (FIRST && tau == 4) STAMP3(x, 39);
    if (tau < NT) {
#pragma unroll
      for (int i = 0; i < 8; ++i) {
#pragma unroll
        for (int k = 0; k < 4; ++k)
          if (Mu<PAR>::v(k) + tau <= 7) {
            if (conv_first<PAR>(tau, i, k)) mfma_vv0(acc[Mu<PAR>::v(k) + tau], a[8 * k + i], b[i]);
            else mfma_vv(acc[Mu<PAR>::v(k) + tau], a[8 * k + i], b[i]);
          }
        if (tau + 1 < NT) b[i] = *(LAS u4*)(base + 64 * (8 * (tau + 1) - i + 7));
        SB();
        if (FIRST) {
          switch (8 * tau + i) {
#define W_(t, j) case 8 * t + j: conv_dma_w<t, j>(x, wd); break;
#define WT_(t) W_(t, 1) W_(t, 3) W_(t, 5) W_(t, 7)
            WT_(1) WT_(2) WT_(3) WT_(4) WT_(5)
#undef WT_
#undef W_
            default: break;
          }
        }
      }
    }
  }
}

struct ConvIn { u4 x0, x1, s[4]; };
DI void conv_load_in(const Ctx& x, int layer, ConvIn& in) {
  const unsigned xo = WS_XT + layer * XT_BYTES + (2 * x.cp * XT_STRIDE + 8 * x.tid) * 2;
  const unsigned so = WS_ST + layer * 2048 * 8 + x.tid * 32;
  in.s[0] = ld16(x.ws, so); in.s[1] = ld16(x.ws, so + 16); in.s[2] = ld16(x.ws, so + 8192); in.s[3] = ld16(x.ws, so + 8192 + 16);
  in.x0 = ld16(x.ws, xo); in.x1 = ld16(x.ws, xo + XT_STRIDE * 2);
}
DI f2 unpk(unsigned v) { return f2{blo(v), bhi(v)}; }
DI unsigned pk(f2 v) { return pack2(v.x, v.y); }
DI void conv_build_u(const Ctx& x, const ConvIn& in) {
  const f4 sa = __builtin_bit_cast(f4, in.s[0]), sb = __builtin_bit_cast(f4, in.s[1]), qa = __builtin_bit_cast(f4, in.s[2]), qb = __builtin_bit_cast(f4, in.s[3]);
  const f2 sv[4] = {f2{sa[0], sa[1]}, f2{sa[2], sa[3]}, f2{sb[0], sb[1]}, f2{sb[2], sb[3]}};
  const f2 qv[4] = {f2{qa[0], qa[1]}, f2{qa[2], qa[3]}, f2{qb[0], qb[1]}, f2{qb[2], qb[3]}};
  u4 u0, u1;
#pragma unroll
  for (int i = 0; i < 4; ++i) {
    const f2 m = sv[i] * (1.f / DM);
    const f2 ve = (qv[i] * (1.f / DM) + LN_EPS) - m * m;
    const f2 r = {__builtin_amdgcn_rsqf(__builtin_fabsf(ve.x)), __builtin_amdgcn_rsqf(__builtin_fabsf(ve.y))};
    const f2 nmr = -m * r;
    u0[i] = pk(unpk(in.x0[i]) * r + nmr);
    u1[i] = pk(unpk(in.x1[i]) * r + nmr);
  }
  *(LAS u4*)(x.L + LD_U + 512 + 16 * x.tid) = u0; *(LAS u4*)(x.L + LD_U + U_CH + 512 + 16 * x.tid) = u1;
  *(LAS u4*)(x.L + LD_X + 16 * x.tid) = in.x0; *(LAS u4*)(x.L + LD_X + 4096 + 16 * x.tid) = in.x1;
}
DI void poison_o(const Ctx& x, int layer) {
#pragma unroll
  for (int i = 0; i < 2; ++i) st16(x.ws, WS_OB + layer * OB_BYTES + (x.c * 2 + i) * 4096 + x.tid * 16, u4{~0u, ~0u, ~0u, ~0u});
}
template <int PAR, bool LOADW> DI void conv_wave(const Ctx& x, float dsk, u4 (&a)[32]) {
  const int ch = x.w >> 1;
  f4 acc[8];
  if (PAR) acc[0] = f4{0, 0, 0, 0};
  conv_mfma<PAR, LOADW>(x, a, acc);
  mfma_done();
  if (LOADW) __syncthreads();
  STAMP(x.wsp, 6 + 16 * x.layer);
  const int al = x.lane & 15, g = x.lane >> 4;
  unsigned eo = LD_EX + x.lane * 16, uo = LD_U + ch * U_CH + 512 + 2 * (1024 * PAR + 16 * al + 4 * g);
  asm volatile("" : "+v"(eo), "+v"(uo));
  lds_t* mine = x.L + eo + x.w * 4096;
  lds_t* other = x.L + eo + (x.w ^ 1) * 4096;
#pragma unroll
  for (int t = 0; t < 4; ++t) *(LAS f4*)(mine + t * 1024) = acc[PAR ? t : 4 + t];
  f2 v[4][2];
#pragma unroll
  for (int t = 0; t < 4; ++t) {
    const u2 uu = *(LAS u2*)(x.L + uo + 512 * t), xx = *(LAS u2*)(x.L + uo + (LD_X - LD_U - 512) + ch * (4096 - U_CH) + 512 * t);
    const f4 own = acc[PAR ? 4 + t : t];
    v[t][0] = unpk(uu.x) * dsk + (unpk(xx.x) + f2{own[0], own[1]});
    v[t][1] = unpk(uu.y) * dsk + (unpk(xx.y) + f2{own[2], own[3]});
  }
  __syncthreads();
  STAMP(x.wsp, 7 + 16 * x.layer);
  const unsigned so = WS_X1 + x.layer * X1_BYTES + (((8 * PAR + 2 * (g & 1) + (al >> 3)) * 512 + 2 * x.cp + ch) * 128 + 16 * (al & 7) + 8 * (g >> 1)) * 2;
#pragma unroll
  for (int t = 0; t < 4; t += 2) {
    const f4 o0 = *(LAS f4*)(other + t * 1024), o1 = *(LAS f4*)(other + (t + 1) * 1024);
    const unsigned a0 = pk(v[t][0] + f2{o0[0], o0[1]}), a1 = pk(v[t][1] + f2{o0[2], o0[3]});
    const unsigned b0 = pk(v[t + 1][0] + f2{o1[0], o1[1]}), b1 = pk(v[t + 1][1] + f2{o1[2], o1[3]});
    const auto s0 = __builtin_amdgcn_permlane16_swap(a0, b0, false, false), s1 = __builtin_amdgcn_permlane16_swap(a1, b1, false, false);
    st16(x.ws, so + t * (2 * 512 * 256), u4{s0[0], s1[0], s0[1], s1[1]});
  }
  if (LOADW) poison_o(x, 0);
  drain();
}

DI f2 cmul(f2 a, f2 b) { return f2{a.x, a.x} * b + f2{a.y, a.y} * f2{-b.y, b.x}; }
DI f2 cexp_p(float wr2, float wrev, float p) {
  const float e = __builtin_amdgcn_exp2f(wr2 * p);
  const float a = __builtin_amdgcn_fractf(wrev * p);
  return f2{e * __builtin_amdgcn_cosf(a), e * __builtin_amdgcn_sinf(a)};
}
constexpr int KG_ROW = 528;
constexpr int KG_A = 0;
constexpr int KG_X = 2 * 48 * KG_ROW;
constexpr int KG_T = LD_CONV_END;
constexpr int KG_B = KG_T + 2 * 2056 * 4;
static_assert(KG_X + 64 * 136 <= LD_KC && KG_B + 2 * 16 * KG_ROW <= LDS_BYTES, "prologue LDS map");

struct ProW { u2 a0, a1; unsigned short b[4]; };
struct ProIn { unsigned short lar, aim, cre, cim, ldt; u4 xt[2], xr[2]; ProW w[2]; unsigned epoch; };
DI void pro_load_w(const Params& p, int F, int lane, ProW& w) {
  const int fh = F / 48, fm = (F % 48) >> 4, fks = F & 15, row = lane & 15, kq = lane >> 4;
  w.a0 = *(const u2*)(p.wqkv + (32 * fks + row) * 1536 + fm * 512 + 16 * fh + 4 * kq);
  w.a1 = *(const u2*)(p.wqkv + (32 * fks + 16 + row) * 1536 + fm * 512 + 16 * fh + 4 * kq);
  const unsigned short* q = p.wphi + fh * 256 + (4 * kq) * 16 + row;
#pragma unroll
  for (int j = 0; j < 4; ++j) w.b[j] = q[16 * j];
}
DI void pro_fold_w(const Ctx& x, int F, const ProW& w) {
  const bool fold = ((F % 48) >> 4) < 2;
  const int dd = (x.lane & 15) - 4 * (x.lane >> 4);
  const u2 wb = fold ? u2{w.b[0] | ((unsigned)w.b[1] << 16), w.b[2] | ((unsigned)w.b[3] << 16)}
                     : u2{(dd == 0 ? 0x3f80u : 0u) | (dd == 1 ? 0x3f800000u : 0u), (dd == 2 ? 0x3f80u : 0u) | (dd == 3 ? 0x3f800000u : 0u)};
  const f4 d0 = mfma16(w.a0, wb, f4{0, 0, 0, 0}), d1 = mfma16(w.a1, wb, f4{0, 0, 0, 0});
  float e[8];
#pragma unroll
  for (int r = 0; r < 4; ++r) {
    const float b0 = dpp_f<0x150>(d0[r]), b1 = dpp_f<0x150>(d1[r]);
    e[r] = d0[r] - (fold ? b0 : 0.f); e[4 + r] = d1[r] - (fold ? b1 : 0.f);
  }
  u4 o = {pack2(e[0], e[1]), pack2(e[2], e[3]), pack2(e[4], e[5]), pack2(e[6], e[7])};
  if (fold && (x.lane & 15) == 0) o = u4{0x3f803f80u, 0x3f803f80u, 0x3f803f80u, 0x3f803f80u};
  st16(x.ws, WS_WFRAG + F * 1024 + x.lane * 16, o);
}
DI void pro_load(const Ctx& x, const Params& p, ProIn& in) {
#pragma unroll
  for (int i = 0; i < 2; ++i) {
    in.xt[i] = *(const u4*)(p.u + (64 * (x.c >> 3) + 32 * i + (x.tid >> 3)) * DM + 64 * (x.c & 7) + 8 * (x.tid & 7));
    in.xr[i] = *(const u4*)(p.u + (8 * x.c + 2 * x.w + i) * DM + 8 * x.lane);
  }
  in.epoch = __builtin_amdgcn_raw_buffer_load_b32(x.ws, WS_EPOCH, 0, AUX_SC1);
  const int d = 2 * x.cp + (x.tid >> 7), n = x.tid & 127;
  in.lar = p.lar[d * NST + n]; in.aim = p.aim[d * NST + n]; in.cre = p.cre[d * NST + n]; in.cim = p.cim[d * NST + n]; in.ldt = p.ldt[d];
  SB();
}
DI void pro_load_w2(const Ctx& x, const Params& p, ProIn& in) {
  pro_load_w(p, 6 * x.c + x.w, x.lane, in.w[0]);
  pro_load_w(p, 6 * x.c + 4 + (x.w & 1), x.lane, in.w[1]);
  SB();
}
constexpr int CODE_KB = 44;
struct Warm { unsigned a, b, c; };
DI unsigned pro_warm_code(const Ctx& x, const unsigned char* code) {
  unsigned v = 0;
  if (x.w == 3 && x.lane < CODE_KB * 8 / 32) v = *(const GAS unsigned*)(code + ((x.c >> 3) * (CODE_KB * 8 / 32) + x.lane) * 128);
  return v;
}
DI void pro_warm(const Ctx& x, const Params& p, Warm& wm) {
  const int j = x.c >> 3;
  wm.b = wm.c = 0;
  if (x.w == 3) {
    wm.b = *(const GAS unsigned*)((const unsigned char*)p.wo + (j * 128 + x.lane) * 128);
    wm.c = *(const GAS unsigned*)((const unsigned char*)p.wo + (j * 128 + 64 + x.lane) * 128);
  }
}
DI void pro_warm_retire(const Warm& wm) { asm volatile("" :: "v"(wm.a), "v"(wm.b), "v"(wm.c)); }
DI void pro_init(const Ctx& x) {
  const rsrc_t ws = x.ws;
  if (x.c < 2) { const int i = x.c * 256 + x.tid; if (i < CWI_END) __builtin_amdgcn_raw_buffer_store_b32(0u, ws, i * (CW_STRIDE * 4), 0, AUX_SC1); }
  if (x.c >= 8 && x.c < 12) st16(ws, WS_ST + 2048 * 8 + ((x.c - 8) * 256 + x.tid) * 16, u4{0, 0, 0, 0});
  static_assert(WS_ZP == WS_KVP + 2 * KVP_BYTES && (2 * KVP_BYTES + 2 * ZP_BYTES) == 256 * 272 * 16, "poisoned block");
  st16(ws, WS_KVP + (x.c * 272 + x.tid) * 16, u4{~0u, ~0u, ~0u, ~0u});
  if (x.tid < 16) st16(ws, WS_KVP + (x.c * 272 + 256 + x.tid) * 16, u4{~0u, ~0u, ~0u, ~0u});
}
DI void pro_publish_x(const Ctx& x, const ProIn& in) {
#pragma unroll
  for (int i = 0; i < 2; ++i) {
    lds_t* a = x.L + KG_X + (32 * i + (x.tid >> 3)) * 136 + (x.tid & 7) * 16;
    *(LAS u2*)a = u2{in.xt[i].x, in.xt[i].y}; *(LAS u2*)(a + 8) = u2{in.xt[i].z, in.xt[i].w};
  }
  {
    f2 a[2], q[2];
#pragma unroll
    for (int i = 0; i < 2; ++i) {
      a[i] = unpk(in.xr[i][0]); q[i] = a[i] * a[i];
#pragma unroll
      for (int k = 1; k < 4; ++k) { const f2 v = unpk(in.xr[i][k]); a[i] += v; q[i] += v * v; }
    }
    const float t = xrow4<2>(row_sum(a[0].x + a[0].y), row_sum(q[0].x + q[0].y), row_sum(a[1].x + a[1].y), row_sum(q[1].x + q[1].y));
    const int g = x.lane >> 4;
    if ((x.lane & 15) == 0)
      __builtin_amdgcn_raw_buffer_store_b32(__builtin_bit_cast(unsigned, t), x.ws, WS_ST + (g >> 1) * 8192 + (8 * x.c + 2 * x.w + (g & 1)) * 4, 0, AUX_SC1);
  }
  __syncthreads();
  const int g = x.lane >> 4, cl = x.lane & 15;
#pragma unroll
  for (int j = 0; j < 2; ++j) {
    lds_t* a = x.L + KG_X + (8 * (g + 4 * j) + (cl >> 2)) * 136 + (16 * x.w + 4 * (cl & 3)) * 2;
    st16(x.ws, WS_XT + ((64 * (x.c & 7) + 16 * x.w + cl) * XT_STRIDE + 64 * (x.c >> 3) + 8 * (g + 4 * j)) * 2, cat(lds_tr(a), lds_tr(a + 4 * 136)));
  }
}
DI void pro_tables(const Ctx& x, const ProIn& in) {
  const float dt = rbf(__expf(bf2f(in.ldt)));
  const float ar = -rbf(__expf(bf2f(in.lar)));
  const float wr2 = (ar * dt) * LOG2E, wrev = (bf2f(in.aim) * dt) * 0.15915494309189535f;
  const f2 c2 = {2.0f * (bf2f(in.cre) * dt), -2.0f * (bf2f(in.cim) * dt)};
  const int chl = x.tid >> 7, n = x.tid & 127;
  LAS unsigned* TA = (LAS unsigned*)(x.L + KG_A + chl * 48 * KG_ROW) + n;
  LAS unsigned* TB = (LAS unsigned*)(x.L + KG_B + chl * 16 * KG_ROW) + n;
  constexpr int RS = KG_ROW / 4;
  const f2 e8 = cexp_p(wr2, wrev, 8.f), e128 = cexp_p(wr2, -wrev, 128.f), e1 = cexp_p(wr2, -wrev, 1.f);
  f2 y = e8, h = cmul(c2, cexp_p(wr2, -wrev, 1024.f)), g[8], e[8];
  g[0] = c2; e[1] = e1; e[2] = cmul(e1, e1);
  TB[0] = 0x00003f80u;
#pragma unroll
  for (int i = 0; i < 8; ++i) {
    TB[(2 * i + 1) * RS] = pk(y);
    TA[(32 + i) * RS] = pk(h);
    const f2 y1 = cmul(y, e8), h1 = cmul(h, e128);
    if (i < 7) g[i + 1] = cmul(g[i], e128);
    if (i == 0) { e[3] = cmul(e[2], e[1]); e[4] = cmul(e[2], e[2]); }
    if (i == 1) { e[5] = cmul(e[4], e[1]); e[6] = cmul(e[3], e[3]); e[7] = cmul(e[4], e[3]); }
    if (i < 7) { TB[(2 * i + 2) * RS] = pk(y1); y = cmul(y1, e8); }
    h = h1;
  }
  TA[40 * RS] = pk(h);
#pragma unroll
  for (int k = 0; k < 8; ++k) {
    TA[k * RS] = pk(k ? cmul(g[0], e[k]) : g[0]);
    TA[(8 + k) * RS] = pk(k ? cmul(g[1], e[k]) : g[1]);
    if (k < 4) { TA[(16 + k) * RS] = pk(k ? cmul(g[2], e[2 * k]) : g[2]); TA[(20 + k) * RS] = pk(k ? cmul(g[3], e[2 * k]) : g[3]); }
    else { TA[(24 + 2 * (k - 4)) * RS] = pk(g[k]); TA[(25 + 2 * (k - 4)) * RS] = pk(cmul(g[k], e[4])); }
  }
}
DI void pro_kgemm(const Ctx& x) {
  const int ch = x.w >> 1, j = x.lane & 15, g = x.lane >> 4, odd = x.w & 1;
  unsigned ao = KG_A + (ch * 48 + 16 * odd + j) * KG_ROW + g * 16, a2o = KG_A + (ch * 48 + 32 + j) * KG_ROW + g * 16;
  unsigned bo = KG_B + (ch * 16 + j) * KG_ROW + g * 16;
  asm volatile("" : "+v"(ao), "+v"(a2o), "+v"(bo));
  u4 a0[8], a2[8], b[8];
#pragma unroll
  for (int ks = 0; ks < 8; ++ks) { b[ks] = *(LAS u4*)(x.L + bo + ks * 64); a0[ks] = *(LAS u4*)(x.L + ao + ks * 64); a2[ks] = *(LAS u4*)(x.L + a2o + ks * 64); }
  SB();
  f4 acc0 = {0, 0, 0, 0}, acc2 = acc0;
#pragma unroll
  for (int ks = 0; ks < 8; ++ks) { acc0 = mfma32(a0[ks], b[ks], acc0); acc2 = mfma32(a2[ks], b[ks], acc2); }
  const int pe = 128 * (g >> 1) + 4 * (g & 1), po = g < 2 ? 256 + 128 * g : 512 + 256 * (g - 2);
  const unsigned t0 = KG_T + (ch * 2056 + 8 * j + (odd ? po : pe)) * 4;
  const int s1 = odd ? (g < 2 ? 8 : 16) : 4, s2 = odd ? (g < 2 ? 16 : 512) : 8;
  *(LAS float*)(x.L + t0) = acc0[0]; *(LAS float*)(x.L + t0 + s1) = acc0[1];
  *(LAS float*)(x.L + t0 + s2) = acc0[2]; *(LAS float*)(x.L + t0 + s1 + s2) = acc0[3];
  const unsigned t2 = KG_T + (ch * 2056 + 1024 + 512 * g + 8 * j) * 4, dummy = KG_T + 2052 * 4;
#pragma unroll
  for (int r = 0; r < 4; ++r) *(LAS float*)(x.L + ((4 * g + r < 8 || (4 * g + r == 8 && j == 0)) ? t2 + 512 * r : dummy)) = acc2[r];
}
constexpr int KG_D = 0, KG_DCH = 4096 + 64;
DI void pro_expand(const Ctx& x) {
  unsigned tb = KG_T, db = KG_D + x.tid * 4;
  f2 l0 = {(float)(2047 - 2 * x.tid), (float)(2046 - 2 * x.tid)};
  asm volatile("" : "+v"(tb), "+v"(db), "+v"(l0));
#pragma unroll
  for (int i = 0; i < 4; ++i) {
    unsigned r = pk(l0 - (float)(512 * i));
    asm("" : "+v"(r));
    const unsigned a0 = tb + 4 * (int)blo(r), a1 = tb + 4 * (int)bhi(r);
#pragma unroll
    for (int ch = 0; ch < 2; ++ch)
      *(LAS unsigned*)(x.L + db + ch * KG_DCH + i * 1024) = pack2(*(LAS float*)(x.L + a0 + ch * (2056 * 4)), *(LAS float*)(x.L + a1 + ch * (2056 * 4)));
  }
}
DI void pro_take_k(const Ctx& x, u4 (&kd)[3]) {
  const int ch = x.tid >> 7, q = x.tid & 127;
  lds_t* a = x.L + KG_D + ch * KG_DCH + 32 * q;
  kd[0] = *(LAS u4*)a; kd[1] = *(LAS u4*)(a + 16); kd[2] = *(LAS u4*)(a + 32);
  const unsigned off = WS_KREV + ((2 * x.cp + ch) * 2048 + 16 * q) * 2;
  __builtin_amdgcn_raw_buffer_store_b128(kd[0], x.ws, off, 0, 0);
  __builtin_amdgcn_raw_buffer_store_b128(kd[1], x.ws, off + 16, 0, 0);
}
DI bool pro_all(const Ctx& x, bool mine) {
  const unsigned ok = __all(mine);
  if (x.lane == 0) ((LAS unsigned*)(x.L + LD_FLAG))[x.w] = ok;
  __syncthreads();
  const u4 r = *(LAS u4*)(x.L + LD_FLAG);
  return r.x & r.y & r.z & r.w;
}
DI void pro_wait(const Ctx& x, unsigned flags, unsigned tag, unsigned first) {
  if (pro_all(x, first == tag)) return;
  for (unsigned s = 0;; ++s) {
    const unsigned v = __builtin_amdgcn_raw_buffer_load_b32(x.ws, flags + x.tid * 64, 0, AUX_SC1);
    __syncthreads();
    if (pro_all(x, v == tag)) break;
    if (s > SPIN_CAP) { if (x.tid == 0) spin_fail(x); break; }
  }
  settle();
}
DI u4 pro_peek_wave(const Ctx& x, unsigned flags) {
  u4 v;
#pragma unroll
  for (int i = 0; i < 4; ++i) v[i] = __builtin_amdgcn_raw_buffer_load_b32(x.ws, flags + (64 * i + x.lane) * 64, 0, AUX_SC1);
  return v;
}
DI void pro_wait_wave(const Ctx& x, unsigned flags, unsigned tag, u4 v) {
  for (unsigned s = 0; !__all(v.x == tag && v.y == tag && v.z == tag && v.w == tag); ++s) {
    if (s > SPIN_CAP) { spin_fail(x); break; }
    asm volatile("" ::: "memory");
    v = pro_peek_wave(x, flags);
  }
  settle();
}

DI void q_wsum(const u4 (&wf)[48], f4 (&ws)[3]) {
  const u4 ones = {0x3f803f80u, 0x3f803f80u, 0x3f803f80u, 0x3f803f80u};
  ws[0] = f4{0, 0, 0, 0}; ws[1] = ws[0]; ws[2] = ws[0];
#pragma unroll
  for (int ks = 0; ks < 16; ++ks)
#pragma unroll
    for (int m = 0; m < 3; ++m) ws[m] = mfma32(wf[16 * m + ks], ones, ws[m]);
}
struct QDma { unsigned v0, v1, base, lds; int layer; unsigned char* wsp; };
DI QDma q_dma_setup(const Ctx& x) {
  const int row = x.lane >> 4, pc = x.lane & 15;
  return QDma{(unsigned)(row * 256 + ((pc ^ (row << 1)) * 16)), (unsigned)(row * 256 + ((pc ^ ((4 + row) << 1)) * 16)),
              WS_X1 + x.layer * X1_BYTES + x.rb * (512 * 256) + x.w * 4096, (unsigned)(LD_XT + x.w * 4096), x.layer, x.wsp};
}
template <int OFF> DI void dma2_sc1(rsrc_t r, const QDma& d) {
  unsigned t;
  asm volatile("s_add_u32 m0, %5, %6\n\ts_add_u32 %0, %4, %6\n\t"
               "buffer_load_dwordx4 %1, %3, %0 offen sc1 lds\n\tbuffer_load_dwordx4 %2, %3, %0 offen offset:1024 sc1 lds"
               : "=&s"(t) : "v"(d.v0), "v"(d.v1), "s"(r), "s"(d.base), "s"(d.lds), "n"(OFF) : "memory", "scc");
}
template <int OFF> DI void dma1_sc1(rsrc_t r, const QDma& d) {
  unsigned t;
  asm volatile("s_add_u32 m0, %4, %5\n\ts_add_u32 %0, %3, %5\n\tbuffer_load_dwordx4 %1, %2, %0 offen sc1 lds"
               : "=&s"(t) : "v"((OFF & 1024) ? d.v1 : d.v0), "s"(r), "s"(d.base), "s"(d.lds), "n"(OFF) : "memory", "scc");
}
struct QAddr { lds_t* lo[4]; lds_t* hi[4]; };
DI void q_frag_addr(const Ctx& x, QAddr& ba) {
  const int g = x.lane >> 4, q = (x.lane & 15) >> 2, pp = x.lane & 3, ph = x.w >> 1;
  const int r7 = 4 * (g & 1) + q;
#pragma unroll
  for (int pt = 0; pt < 4; ++pt) {
    const int lc = 8 * ph + 2 * (pt ^ (2 * (x.w & 1))) + (pp >> 1);
    unsigned o = LD_XT + (4 * g + q) * 256 + ((lc ^ (r7 << 1)) * 16) + 8 * (pp & 1), o2 = o + 65536;
    asm volatile("" : "+v"(o), "+v"(o2));
    ba.lo[pt] = x.L + o; ba.hi[pt] = x.L + o2;
  }
}
template <int KS> DI u4 q_ldb1(const QAddr& ba, int pt) {
  lds_t* p = (KS < 8 ? ba.lo[pt] : ba.hi[pt]) + (KS & 7) * 8192;
  return cat(lds_tr(p), lds_tr(p + 4096));
}
template <int KS> DI void q_ldb(const QAddr& ba, u4 (&b)[4]) {
#pragma unroll
  for (int pt = 0; pt < 4; ++pt) b[pt] = q_ldb1<KS>(ba, pt);
}
template <int N> DI void wait_vm_barrier() { asm volatile("s_waitcnt vmcnt(%0)\n\ts_barrier" :: "n"(N) : "memory"); }

DI f4 bcast4(float r) {
  const f2 xy = swap16(r, r), ab = swap32(xy.x, xy.x), cd = swap32(xy.y, xy.y);
  return f4{ab.x, ab.y, cd.x, cd.y};
}
template <class F> DI void softmax4(const f4 (&t)[4], const float (&cs)[4], float csr, lds_t* scr_w, lds_t* scr_r, u4 (&out)[4], F mid) {
  float mx[4], mn[4];
#pragma unroll
  for (int pt = 0; pt < 4; ++pt) {
    mx[pt] = fmax2(fmax2(fmax2(t[pt][0], t[pt][1]), t[pt][2]), t[pt][3]);
    mn[pt] = fmin2(fmin2(fmin2(t[pt][0], t[pt][1]), t[pt][2]), t[pt][3]);
  }
  const f2 M = xrow4_maxmin(mx[0], mx[1], mx[2], mx[3], mn[0], mn[1], mn[2], mn[3]);
  *(LAS f2*)scr_w = M * f2{-csr, csr};
  f2 bb[4];
#pragma unroll
  for (int pt = 0; pt < 4; ++pt) bb[pt] = *(LAS f2*)(scr_r + 128 * pt);
  SB();
  mid();
  SB();
  f2 ep[4][2], en[4][2];
  float sp[4], sn[4];
#pragma unroll
  for (int pt = 0; pt < 4; ++pt) {
    const f2 b = bb[pt];
    const f2 t0 = {t[pt][0], t[pt][1]}, t1 = {t[pt][2], t[pt][3]};
    const f2 p0 = t0 * cs[pt] + b.x, p1 = t1 * cs[pt] + b.x, n0 = b.y - t0 * cs[pt], n1 = b.y - t1 * cs[pt];
    ep[pt][0] = f2{__builtin_amdgcn_exp2f(p0.x), __builtin_amdgcn_exp2f(p0.y)}; ep[pt][1] = f2{__builtin_amdgcn_exp2f(p1.x), __builtin_amdgcn_exp2f(p1.y)};
    en[pt][0] = f2{__builtin_amdgcn_exp2f(n0.x), __builtin_amdgcn_exp2f(n0.y)}; en[pt][1] = f2{__builtin_amdgcn_exp2f(n1.x), __builtin_amdgcn_exp2f(n1.y)};
    const f2 a = ep[pt][0] + ep[pt][1], c = en[pt][0] + en[pt][1];
    sp[pt] = a.x + a.y; sn[pt] = c.x + c.y;
  }
  const f4 ip = bcast4(__builtin_amdgcn_rcpf(xrow4<2>(sp[0], sp[1], sp[2], sp[3])));
  const f4 in = bcast4(__builtin_amdgcn_rcpf(xrow4<2>(sn[0], sn[1], sn[2], sn[3])));
#pragma unroll
  for (int pt = 0; pt < 4; ++pt)
    out[pt] = u4{pk(ep[pt][0] * ip[pt]), pk(ep[pt][1] * ip[pt]), pk(en[pt][0] * in[pt]), pk(en[pt][1] * in[pt])};
}

struct QState { u4 pq[4]; u2 res[4]; u4 pk8[8], az; };
DI void o_load_partials(const Ctx& x, QState& st) {
  const int h = x.w & 1, ph = x.w >> 1, hh = 2 * x.hp + h;
  const unsigned ko = WS_KVP + x.layer * KVP_BYTES + (hh * 16 + 8 * ph) * 1024 + x.lane * 16;
#pragma unroll
  for (int i = 0; i < 8; ++i) st.pk8[i] = ld16(x.ws, ko + i * 1024);
  st.az = ld16(x.ws, WS_ZP + x.layer * ZP_BYTES + (hh * 16 + (x.lane & 15)) * 64 + (x.lane >> 4) * 16);
}

#define Q_UP 4
constexpr int UP = Q_UP, NIN = 2 * (8 - UP);
template <int KS> struct QPass1 {
  static DI void run(rsrc_t ws, const QDma& d, const QAddr& ba, const u4 (&wf)[48], u4 (&b)[4], f4 (&ak)[4], f4 (&av)[4], f4 (&ag)[2]) {
    if (KS < 15 && (KS & 1)) wait_vm_barrier<(KS < 15 ? 4 * UP + 2 * (KS < NIN ? KS : NIN) - 4 * ((KS + 1) / 2 + 1) : 0)>();
    const u4 g0 = b[0], g1 = b[1];
#pragma unroll
    for (int pt = 0; pt < 4; ++pt) {
      const u4 cur = b[pt];
      lds_t* p = ((KS + 1) < 8 ? ba.lo[pt] : ba.hi[pt]) + ((KS + 1) & 7) * 8192;
      u2 t0, t1;
      if (KS == 0) mfma_va0(ak[pt], wf[16 + KS], cur); else mfma_va(ak[pt], wf[16 + KS], cur);
      if (KS < 15) t0 = lds_tr(p);
      SB();
      if (KS == 0) mfma_va0(av[pt], wf[32 + KS], cur); else mfma_va(av[pt], wf[32 + KS], cur);
      if (KS < 15) { t1 = lds_tr(p + 4096); b[pt] = cat(t0, t1); }
      SB();
    }
    if (KS == 0) mfma_vv0(ag[0], g0, g0); else mfma_vv(ag[0], g0, g0);
    SB();
    if (KS < NIN) dma1_sc1<(KS < NIN ? (UP + KS / 2) * 16384 + (KS & 1) * 2048 : 0)>(ws, d);
    SB();
    if (KS == 0) mfma_vv0(ag[1], g1, g1); else mfma_vv(ag[1], g1, g1);
    SB();
    if (KS < NIN) dma1_sc1<(KS < NIN ? (UP + KS / 2) * 16384 + (KS & 1) * 2048 + 1024 : 0)>(ws, d);
    SB();
    QPass1<KS + 1>::run(ws, d, ba, wf, b, ak, av, ag);
  }
};
template <> struct QPass1<16> { static DI void run(rsrc_t, const QDma&, const QAddr&, const u4 (&)[48], u4 (&)[4], f4 (&)[4], f4 (&)[4], f4 (&)[2]) {} };
template <int KS> struct QPass2 {
  static DI void run(const QAddr& ba, const u4 (&wf)[48], u4 (&b)[3][4], f4 (&aq)[4]) {
#pragma unroll
    for (int pt = 0; pt < 4; ++pt) {
      if (KS == 0) mfma_va0(aq[pt], wf[KS], b[0][pt]); else mfma_va(aq[pt], wf[KS], b[KS % 3][pt]);
      if (KS < 14) b[(KS + 2) % 3][pt] = q_ldb1<(KS < 14 ? KS + 2 : 15)>(ba, pt);
      SB();
    }
    QPass2<KS + 1>::run(ba, wf, b, aq);
  }
};
template <> struct QPass2<16> { static DI void run(const QAddr&, const u4 (&)[48], u4 (&)[3][4], f4 (&)[4]) {} };

DI void o_dma_wo(const Ctx& x, const Params& p) {
  const rsrc_t r = make_rsrc(p.wo, 512 * 512 * 2);
  const int row = x.lane >> 3, pc = x.lane & 7, cb = x.hp >> 1;
  const int b1 = (row >> 1) & 1;
  const unsigned v0 = row * 1024 + ((pc ^ (b1 << 1)) * 16), v1 = row * 1024 + ((pc ^ ((b1 | 2) << 1)) * 16) + 7168;
#pragma unroll 1
  for (int i = 4 * x.w; i < 4 * x.w + 4; ++i) dma4(r, v0, v1, v0 + 2 * 7168, v1 + 2 * 7168, i * 32768 + cb * 128, LD_XT + i * 4096);
}

DI void q_phase(const Ctx& x, const Params& p, const u4 (&wf)[48], const f4 (&wsum)[3], QState& st) {
  const QDma dm = q_dma_setup(x);
  dma2_sc1<0>(x.ws, dm); dma2_sc1<2048>(x.ws, dm); dma2_sc1<16384>(x.ws, dm); dma2_sc1<16384 + 2048>(x.ws, dm);
  if (UP > 2) { dma2_sc1<32768>(x.ws, dm); dma2_sc1<32768 + 2048>(x.ws, dm); }
  if (UP > 3) { dma2_sc1<49152>(x.ws, dm); dma2_sc1<49152 + 2048>(x.ws, dm); }
  if (UP > 4) { dma2_sc1<65536>(x.ws, dm); dma2_sc1<65536 + 2048>(x.ws, dm); }
  if (UP > 5) { dma2_sc1<81920>(x.ws, dm); dma2_sc1<81920 + 2048>(x.ws, dm); }
  QAddr ba;
  q_frag_addr(x, ba);
  const int h = x.w & 1, ph = x.w >> 1, g = x.lane >> 4, cl = x.lane & 15;
  f4 ak[4], av[4], ag[2];
  STAMP(x.wsp, 9 + 16 * x.layer);
  {
    u4 b[4];
    wait_vm_barrier<4 * UP - 4>();
    q_ldb<0>(ba, b);
    SB();
    QPass1<0>::run(x.ws, dm, ba, wf, b, ak, av, ag);
    mfma_done();
  }
  STAMP(x.wsp, 10 + 16 * x.layer);
  {
    const int r = cl & 3;
    const unsigned so = LD_STAT + 4 * (64 * ph + 32 * h + cl);
    const unsigned a0 = g == 0 ? so : LD_STAT + 1024, a1 = g == (cl >> 2) ? so + 512 : LD_STAT + 1024;
#pragma unroll
    for (int i = 0; i < 2; ++i) {
      const float dq = r == 0 ? ag[i][0] : r == 1 ? ag[i][1] : r == 2 ? ag[i][2] : ag[i][3];
      *(LAS float*)(x.L + a0 + 64 * i) = ak[i][0];
      *(LAS float*)(x.L + a1 + 64 * i) = dq;
    }
  }
  __syncthreads();
  float mean[4], rs[4], cs[4];
#pragma unroll
  for (int pt = 0; pt < 4; ++pt) {
    LAS float* S = (LAS float*)(x.L + LD_STAT) + 64 * ph + 16 * (pt ^ (2 * h)) + cl;
    mean[pt] = S[0] * (1.f / DM);
    rs[pt] = __builtin_amdgcn_rsqf(__builtin_fabsf((S[128] * (1.f / DM) + LN_EPS) - mean[pt] * mean[pt]));
    cs[pt] = rs[pt] * LOG2E;
  }
  const float csr = g == 0 ? cs[0] : g == 1 ? cs[2] : g == 2 ? cs[1] : cs[3];
  lds_t* scr_r = x.L + LD_SCR + x.w * 512 + cl * 8;
  lds_t* scr_w = scr_r + 128 * ((g >> 1) | ((g & 1) << 1));
  lds_t* pv = x.L + LD_PV + x.w * PV_WAVE;
  {
    f4 t[4];
#pragma unroll
    for (int pt = 0; pt < 4; ++pt) t[pt] = ak[pt] - mean[pt] * wsum[1];
    u4 pk4[4];
    softmax4(t, cs, csr, scr_w, scr_r, pk4, [&]() {
#pragma unroll
      for (int pt = 0; pt < 4; ++pt) {
        const f4 v = (av[pt] - mean[pt] * wsum[2]) * rs[pt];
        *(LAS u2*)(pv + (16 * pt + cl) * PV_ROW + 8 * g + 64) = u2{pack2(v[0], v[1]), pack2(v[2], v[3])};
      }
    });
#pragma unroll
    for (int pt = 0; pt < 4; ++pt) {
      lds_t* row = pv + (16 * pt + cl) * PV_ROW + 8 * g;
      *(LAS u2*)(row) = u2{pk4[pt].x, pk4[pt].y}; *(LAS u2*)(row + 32) = u2{pk4[pt].z, pk4[pt].w};
    }
  }
  f4 kv0, kv1, z0, z1;
  {
    u4 ones = {0x3f803f80u, 0x3f803f80u, 0x3f803f80u, 0x3f803f80u};
    asm volatile("" : "+v"(ones));
    const int q = cl >> 2, pp = cl & 3;
    u4 a0[2], a1[2], bv[2];
#pragma unroll
    for (int k2 = 0; k2 < 2; ++k2) {
      lds_t* a = pv + (32 * k2 + 8 * g + q) * PV_ROW + 8 * pp;
      a0[k2] = cat(lds_tr(a), lds_tr(a + 4 * PV_ROW));
      bv[k2] = cat(lds_tr(a + 64), lds_tr(a + 4 * PV_ROW + 64));
      a1[k2] = cat(lds_tr(a + 32), lds_tr(a + 4 * PV_ROW + 32));
    }
    SB(); valu_to_mfma();
    mfma_vv0(kv0, a0[0], bv[0]); mfma_vv0(z0, a0[0], ones); mfma_vv0(kv1, a1[0], bv[0]); mfma_vv0(z1, a1[0], ones);
    mfma_vv(kv0, a0[1], bv[1]); mfma_vv(z0, a0[1], ones); mfma_vv(kv1, a1[1], bv[1]); mfma_vv(z1, a1[1], ones);
    mfma_done();
  }
  STAMP(x.wsp, 11 + 16 * x.layer);
  {
    lds_t* mine = x.L + LD_PV + x.w * PV_WAVE + x.lane * 16;
    if (ph == 1) { *(LAS f4*)(mine) = kv0; *(LAS f4*)(mine + 1024) = kv1; *(LAS f4*)(mine + 2048) = z0; *(LAS f4*)(mine + 3072) = z1; }
    __syncthreads();
    if (ph == 0) {
      lds_t* o = x.L + LD_PV + (x.w + 2) * PV_WAVE + x.lane * 16;
      kv0 += *(LAS f4*)(o); kv1 += *(LAS f4*)(o + 1024); z0 += *(LAS f4*)(o + 2048); z1 += *(LAS f4*)(o + 3072);
      const int hr = (2 * x.hp + h) * 16 + x.rb;
      st16(x.ws, WS_KVP + x.layer * KVP_BYTES + hr * 1024 + x.lane * 16,
           u4{pack2(kv0[0], kv0[1]), pack2(kv0[2], kv0[3]), pack2(kv1[0], kv1[1]), pack2(kv1[2], kv1[3])});
      if (cl == 0) st16(x.ws, WS_ZP + x.layer * ZP_BYTES + hr * 64 + g * 16,
                        u4{pack2(z0[0], z0[1]), pack2(z0[2], z0[3]), pack2(z1[0], z1[1]), pack2(z1[2], z1[3])});
    }
  }
  STAMP(x.wsp, 12 + 16 * x.layer);
  {
    {
      const int cb = x.hp >> 1, q = cl >> 2, pos = 64 * (x.hp & 1) + 16 * x.w + 4 * (cl & 3);
      const int row = 64 * cb + 4 * g + q;
      unsigned ro = LD_XT + row * 256 + (((pos >> 3) ^ ((row & 7) << 1)) * 16) + (pos & 4) * 2;
      asm volatile("" : "+v"(ro));
#pragma unroll
      for (int ct = 0; ct < 4; ++ct) st.res[ct] = lds_tr(x.L + ro + ct * 4096);
    }
    f4 aq[4];
    u4 b[3][4];
    q_ldb<0>(ba, b[0]); q_ldb<1>(ba, b[1]);
    SB();
    QPass2<0>::run(ba, wf, b, aq);
    mfma_done();
    f4 t[4];
#pragma unroll
    for (int pt = 0; pt < 4; ++pt) t[pt] = aq[pt] - mean[pt] * wsum[0];
    softmax4(t, cs, csr, scr_w, scr_r, st.pq, [&]() { o_load_partials(x, st); });
  }
  STAMP(x.wsp, 13 + 16 * x.layer);
}

DI void o_compute(const Ctx& x, QState& st) {
  const int h = x.w & 1, ph = x.w >> 1, g = x.lane >> 4, cl = x.lane & 15, hh = 2 * x.hp + h;
  STAMP(x.wsp, 14 + 16 * x.layer);
  const unsigned one = g == (cl >> 2) ? ((cl & 1) ? 0x3f800000u : 0x3f80u) : 0u;
  const unsigned e0 = (cl & 2) ? 0u : one, e1 = (cl & 2) ? one : 0u;
  const u4 sel0 = {e0, e1, 0u, 0u}, sel1 = {0u, 0u, e0, e1};
  f4 k0, k1, den[4];
  for (unsigned spin = 0;; ++spin) {
    f4 dp[4];
    SB(); valu_to_mfma();
#pragma unroll
    for (int pt = 0; pt < 4; ++pt) mfma_vv0(dp[pt], st.az, st.pq[pt]);
    mfma_vv0(k0, sel0, st.pk8[0]); mfma_vv0(k1, sel1, st.pk8[0]);
#pragma unroll
    for (int i = 1; i < 8; ++i) { mfma_vv(k0, sel0, st.pk8[i]); mfma_vv(k1, sel1, st.pk8[i]); }
    SB();
    u2 dq[4];
#pragma unroll
    for (int pt = 0; pt < 4; ++pt) { dq[pt] = u2{pack2(dp[pt][0], dp[pt][1]), pack2(dp[pt][2], dp[pt][3])}; den[pt] = f4{1e-6f, 1e-6f, 1e-6f, 1e-6f}; }
    const u2 ones = {0x3f803f80u, 0x3f803f80u};
    u2 o2 = ones;
    asm volatile("" : "+v"(o2));
    SB(); valu_to_mfma();
#pragma unroll
    for (int pt = 0; pt < 4; ++pt) mfma16_vvc(den[pt], o2, dq[pt]);
    mfma_done();
    const float chk = (k0[0] + k1[0]) + den[0][0];
    if (!__any(chk != chk)) break;
    if (spin > 4096u) { spin_fail(x); break; }
    o_load_partials(x, st);
  }
  lds_t* mine = x.L + LD_PV + x.w * PV_WAVE + (x.w >> 1) * 4096 + x.lane * 16;
  *(LAS f4*)(mine) = k0; *(LAS f4*)(mine + 1024) = k1;
  __syncthreads();
  STAMP(x.wsp, 15 + 16 * x.layer);
  lds_t* o = x.L + LD_PV + (x.w ^ 2) * PV_WAVE + ((x.w ^ 2) >> 1) * 4096 + x.lane * 16;
  k0 += *(LAS f4*)(o); k1 += *(LAS f4*)(o + 1024);
  const u4 akv = {pack2(k0[0], k0[1]), pack2(k0[2], k0[3]), pack2(k1[0], k1[1]), pack2(k1[2], k1[3])};
  f4 num[4];
  SB(); valu_to_mfma();
#pragma unroll
  for (int pt = 0; pt < 4; ++pt) mfma_vv0(num[pt], akv, st.pq[pt]);
  mfma_done();
  const unsigned oo = WS_OB + x.layer * OB_BYTES + ((2 * x.rb + ph) * 32 + hh) * 2048 + (16 * ((g & 1) ^ (2 * h)) + cl) * 32 + 16 * (g >> 1);
#pragma unroll
  for (int pt = 0; pt < 4; pt += 2) {
    const f4 va = num[pt] * __builtin_amdgcn_rcpf(den[pt][0]), vb = num[pt + 1] * __builtin_amdgcn_rcpf(den[pt + 1][0]);
    const auto s0 = __builtin_amdgcn_permlane16_swap(pack2(va[0], va[1]), pack2(vb[0], vb[1]), false, false);
    const auto s1 = __builtin_amdgcn_permlane16_swap(pack2(va[2], va[3]), pack2(vb[2], vb[3]), false, false);
    st16(x.ws, oo ^ (pt * 512), u4{s0[0], s1[0], s0[1], s1[1]});
  }
}

struct OAddr { lds_t* p[4]; };
template <int KS> DI void o_ldw(const OAddr& wb, u4 (&b)[4]) {
#pragma unroll
  for (int ct = 0; ct < 4; ++ct) b[ct] = cat(lds_tr(wb.p[ct] + KS * 4096), lds_tr(wb.p[ct] + KS * 4096 + 512));
}
DI unsigned umax3(unsigned a, unsigned b, unsigned c) { const unsigned m = a > b ? a : b; return m > c ? m : c; }
template <int KS> struct OGemm {
  static DI void run(const OAddr& wb, const u4 (&of)[16], u4 (&b)[3][4], f4 (&acc)[4], unsigned& top) {
#pragma unroll
    for (int ct = 0; ct < 4; ++ct) {
      if (KS == 0) mfma_vv0(acc[ct], b[0][ct], of[KS]); else mfma_vv(acc[ct], b[KS % 3][ct], of[KS]);
      if (KS < 14) b[(KS + 2) % 3][ct] = cat(lds_tr(wb.p[ct] + (KS + 2) * 4096), lds_tr(wb.p[ct] + (KS + 2) * 4096 + 512));
      if (ct == 1) top = umax3(top, of[KS].x, of[KS].y);
      if (ct == 3) top = umax3(top, of[KS].z, of[KS].w);
      SB();
    }
    OGemm<KS + 1>::run(wb, of, b, acc, top);
  }
};
template <> struct OGemm<16> { static DI void run(const OAddr&, const u4 (&)[16], u4 (&)[3][4], f4 (&)[4], unsigned&) {} };

DI void o_gemm(const Ctx& x, const Params& p, const QState& st, u4 (&kd)[3]) {
  const int g = x.lane >> 4, cl = x.lane & 15, pb = 2 * x.rb + (x.hp & 1), cb = x.hp >> 1;
  const unsigned oo = WS_OB + x.layer * OB_BYTES + ((pb * 32 + (g >> 1)) * 64 + 16 * x.w + cl) * 32 + 16 * (g & 1);
  OAddr wb;
  {
    const int q = cl >> 2, pp = cl & 3, f = ((q >> 1) & 1) | ((g & 1) << 1);
#pragma unroll
    for (int ct = 0; ct < 4; ++ct) wb.p[ct] = x.L + LD_XT + (8 * g + q) * 128 + (((2 * ct + (pp >> 1)) ^ (f << 1)) * 16) + 8 * (pp & 1);
  }
  f4 acc[4];
  o_dma_wo(x, p);
  for (unsigned spin = 0;; ++spin) {
    u4 of[16];
#pragma unroll
    for (int ks = 0; ks < 16; ++ks) of[ks] = ld16(x.ws, oo + ks * 4096);
    if (spin == 0) wait_vm_barrier<16>();
    STAMP(x.wsp, 17 + 16 * x.layer);
    u4 b[3][4];
    o_ldw<0>(wb, b[0]); o_ldw<1>(wb, b[1]);
    SB();
    unsigned top = 0;
    OGemm<0>::run(wb, of, b, acc, top);
    mfma_done();
    if (!__any(top == ~0u)) break;
    if (spin > 4096u) { spin_fail(x); break; }
  }
  STAMP(x.wsp, 18 + 16 * x.layer);
  f2 s2 = {0.f, 0.f}, q2 = s2;
  unsigned so = LD_OST + (16 * x.w + cl) * 144 + 8 * g;
  asm volatile("" : "+v"(so));
#pragma unroll
  for (int ct = 0; ct < 4; ++ct) {
    const f2 a = unpk(st.res[ct].x) + f2{acc[ct][0], acc[ct][1]}, b2 = unpk(st.res[ct].y) + f2{acc[ct][2], acc[ct][3]};
    s2 += a + b2; q2 += a * a; q2 += b2 * b2;
    *(LAS u2*)(x.L + so + 32 * ct) = u2{pk(a), pk(b2)};
  }
  if (x.layer == 0) {
    const f2 sq = xrow_sum2(s2.x + s2.y, q2.x + q2.y);
    if (g < 2) {
      GAS float* d = (GAS float*)(p.ws + WS_ST + 2048 * 8) + g * 2048 + 64 * pb + 16 * x.w + cl;
      __hip_atomic_fetch_add(d, g ? sq.y : sq.x, RLX_AGENT);
    }
  }
  conv_load_k(x, kd);
  __syncthreads();
  if (x.layer == 0) {
    const int qq = cl >> 2, pp = cl & 3;
#pragma unroll
    for (int j = 0; j < 2; ++j) {
      lds_t* a = x.L + LD_OST + (8 * (g + 4 * j) + qq) * 144 + (16 * x.w + 4 * pp) * 2;
      const u4 v = cat(lds_tr(a), lds_tr(a + 4 * 144));
      st16(x.ws, WS_XT + XT_BYTES + ((64 * cb + 16 * x.w + cl) * XT_STRIDE + 64 * pb + 8 * (g + 4 * j)) * 2, v);
    }
  } else {
    const rsrc_t ro = make_rsrc(p.out, SEQ * DM * 2);
#pragma unroll
    for (int i = 0; i < 2; ++i) {
      const int k = i * 256 + x.tid, pos = k >> 3, q8 = k & 7;
      __builtin_amdgcn_raw_buffer_store_b128(*(LAS u4*)(x.L + LD_OST + pos * 144 + q8 * 16), ro, ((64 * pb + pos) * DM + 64 * cb + 8 * q8) * 2, 0, 0);
    }
  }
  STAMP(x.wsp, 19 + 16 * x.layer);
}

DI Ctx make_ctx(const Params& p, unsigned char* smem) {
  Ctx x;
  x.L = (lds_t*)smem; x.ws = make_rsrc(p.ws, WS_END); x.cw = (GAS unsigned*)(p.ws + WS_CTRL); x.wsp = p.ws;
  x.tid = threadIdx.x; x.lane = x.tid & 63; x.w = __builtin_amdgcn_readfirstlane(x.tid >> 6); x.c = blockIdx.x;
  x.cp = (x.c & 7) * 32 + (x.c >> 3);
  x.rb = 2 * (x.c & 7) + (x.c >> 7); x.hp = (x.c >> 3) & 15; x.layer = 0;
  return x;
}

DI Ctx fresh(const Ctx& x, int layer) { Ctx y = x; int t = x.tid; asm volatile("" : "+v"(t)); y.tid = t; y.lane = t & 63; y.layer = layer; return y; }

__global__ void __launch_bounds__(256, 1) k_main(Params p) {
  extern __shared__ __attribute__((aligned(16))) unsigned char smem[];
  const unsigned char* code0;
  asm volatile("s_getpc_b64 %0" : "=s"(code0));
  const Ctx x0 = make_ctx(p, smem);
  STAMP(p.ws, 0);
  u4 wf[48];
  f4 wsum[3];
  const float dsk = bf2f(p.dsk[2 * x0.cp + (x0.w >> 1)]);
  unsigned tag;
  {
    const Ctx x = fresh(x0, 0);
    u4 kd[3], a[32];
    ProIn pin;
    pro_load(x, p, pin);
    pro_init(x);
    tag = pin.epoch + 1u;
    pro_publish_x(x, pin);
    SB();
    pro_load_w2(x, p, pin);
    asm volatile("s_waitcnt vmcnt(12)" ::: "memory");
    __syncthreads();
    if (x.w == 0) __builtin_amdgcn_raw_buffer_store_b32(tag, x.ws, WS_FLAG + x.c * 64, 0, AUX_SC1);
    STAMP(p.ws, 1);
    unsigned fl = __builtin_amdgcn_raw_buffer_load_b32(x.ws, WS_FLAG + x.tid * 64, 0, AUX_SC1);
    pro_tables(x, pin);
    pro_fold_w(x, 6 * x.c + x.w, pin.w[0]);
    if (x.w < 2) pro_fold_w(x, 6 * x.c + 4 + x.w, pin.w[1]);
    drain();
    asm volatile("" : "+v"(fl));
    __syncthreads();
    STAMP(p.ws, 2);
    fl = __builtin_amdgcn_raw_buffer_load_b32(x.ws, WS_FLAG + x.tid * 64, 0, AUX_SC1) == tag ? tag : fl;
    __builtin_amdgcn_raw_buffer_store_b32(tag, x.ws, WS_FLAG2 + x.c * 64, 0, AUX_SC1);
    pro_kgemm(x);
    pro_wait(x, WS_FLAG, tag, fl);
    STAMP(p.ws, 3);
    ConvIn in;
    conv_load_in(x, 0, in);
    SB();
    pro_expand(x);
    __syncthreads();
    pro_take_k(x, kd);
    conv_build_k(x, kd);
    STAMP(p.ws, 4);
    conv_build_u(x, in);
    const u4 fl2 = pro_peek_wave(x, WS_FLAG2);
    __syncthreads();
    STAMP(p.ws, 5);
    pro_wait_wave(x, WS_FLAG2, tag, fl2);
    STAMP3(x, 36);
    Warm wm;
    wm.a = pro_warm_code(x, code0);
    pro_warm(x, p, wm);
    if (x.w & 1) conv_wave<1, true>(x, dsk, a); else conv_wave<0, true>(x, dsk, a);
    pro_warm_retire(wm);
    a2a_post(fresh(x0, 0), 0);
    poison_o(fresh(x0, 0), 1);
    q_take_w(fresh(x0, 0), wf);
    q_wsum(wf, wsum);
  }
#pragma unroll 1
  for (int layer = 0; layer < 2; ++layer) {
    Ctx x = fresh(x0, layer);
    a2a_wait(x, 2 * layer, 0u);
    STAMP(p.ws, 8 + 16 * layer);
    QState st;
    x = fresh(x0, layer);
    q_phase(x, p, wf, wsum, st);
    x = fresh(x0, layer);
    o_compute(x, st);
    STAMP(p.ws, 16 + 16 * layer);
    x = fresh(x0, layer);
    u4 kd[3];
    o_gemm(x, p, st, kd);
    if (layer == 0) {
      u4 a[32];
      conv_build_k(x, kd);
      a2a_signal(x, 1);
      x = fresh(x0, 1);
      unsigned first;
      if (x.w & 1) { conv_load_a<1, 0>(x, a); first = a2a_peek(x, 1); conv_load_a<1, 2>(x, a); }
      else { conv_load_a<0, 0>(x, a); first = a2a_peek(x, 1); conv_load_a<0, 2>(x, a); }
      SB();
      a2a_wait(x, 1, first);
      STAMP(p.ws, 20);
      ConvIn in;
      conv_load_in(x, 1, in);
      conv_build_u(x, in);
        __syncthreads();
      STAMP(p.ws, 21);
      if (x.w & 1) conv_wave<1, false>(x, dsk, a); else conv_wave<0, false>(x, dsk, a);
      a2a_post(fresh(x0, 1), 2);
    }
  }
  if (x0.c == 0 && x0.tid == 0) __builtin_amdgcn_raw_buffer_store_b32(tag, x0.ws, WS_EPOCH, 0, AUX_SC1);
}

extern "C" void kernel_launch(void* const* d_in, const int* in_sizes, int n_in, void* d_out, int out_size, void* d_ws, size_t ws_size,
                              hipStream_t stream) {
  Params p;
  p.u = (const unsigned short*)d_in[0]; p.lar = (const unsigned short*)d_in[1]; p.aim = (const unsigned short*)d_in[2];
  p.cre = (const unsigned short*)d_in[3]; p.cim = (const unsigned short*)d_in[4]; p.ldt = (const unsigned short*)d_in[5];
  p.dsk = (const unsigned short*)d_in[6]; p.wqkv = (const unsigned short*)d_in[8]; p.wphi = (const unsigned short*)d_in[9];
  p.wo = (const unsigned short*)d_in[10]; p.out = (unsigned short*)d_out; p.ws = (unsigned char*)d_ws;
  (void)hipFuncSetAttribute((const void*)k_main, hipFuncAttributeMaxDynamicSharedMemorySize, LDS_BYTES);
  k_main<<<256, 256, LDS_BYTES, stream>>>(p);
}
```

```cpp
#include <hip/hip_runtime.h>
#include <hip/hip_bf16.h>
#include <stdint.h>


typedef short s4 __attribute__((ext_vector_type(4)));
typedef short s8 __attribute__((ext_vector_type(8)));
typedef float f2 __attribute__((ext_vector_type(2)));
typedef float f4 __attribute__((ext_vector_type(4)));
typedef unsigned u2 __attribute__((ext_vector_type(2)));
typedef unsigned u4 __attribute__((ext_vector_type(4)));
typedef __bf16 bf2 __attribute__((ext_vector_type(2)));
#define GAS __attribute__((address_space(1)))
#define LAS __attribute__((address_space(3)))
#define DI __device__ __forceinline__
#define RLX_AGENT __ATOMIC_RELAXED, __HIP_MEMORY_SCOPE_AGENT
typedef GAS unsigned gu32;
typedef LAS unsigned char lds_t;
typedef __amdgpu_buffer_rsrc_t rsrc_t;

constexpr int SEQ = 2048, DM = 512, NST = 128;
constexpr float LOG2E = 1.4426950408889634f;
constexpr float LN_EPS = 1e-5f;
constexpr int AUX_SC1 = 16;

constexpr int XT_STRIDE = 2112;
constexpr unsigned WS_CTRL = 0;
constexpr unsigned WS_KREV = 262144;
constexpr unsigned WS_WFRAG = WS_KREV + 512u * 2048 * 2;
constexpr unsigned WS_XT = WS_WFRAG + 32u * 3 * 16 * 1024;
constexpr unsigned XT_BYTES = 512u * XT_STRIDE * 2;
constexpr unsigned WS_ST = WS_XT + 2 * XT_BYTES;
constexpr unsigned WS_X1 = WS_ST + 2u * 2048 * 8;
constexpr unsigned X1_BYTES = 2048u * 512 * 2;
constexpr unsigned WS_KVP = WS_X1 + 2 * X1_BYTES;
constexpr unsigned KVP_BYTES = 32u * 16 * 1024;
constexpr unsigned WS_ZP = WS_KVP + 2 * KVP_BYTES;
constexpr unsigned ZP_BYTES = 32u * 16 * 64;
constexpr unsigned WS_OB = WS_ZP + 2 * ZP_BYTES;
constexpr unsigned OB_BYTES = 2048u * 512 * 2;
constexpr unsigned WS_END = WS_OB + 2 * OB_BYTES;
constexpr int CW_TMO = 0;
constexpr int NS = 16, REP = 8;
constexpr int NCW_A2A = 3 * REP * NS;
constexpr int CW_STRIDE = 64;
constexpr int CWI_A2A = 1, CWI_END = CWI_A2A + NCW_A2A;
constexpr unsigned WS_EPOCH = 131072, WS_FLAG = WS_EPOCH + 256, WS_FLAG2 = WS_FLAG + 256 * 64;
static_assert(CWI_END <= 512 && CWI_END * CW_STRIDE * 4 <= WS_EPOCH && WS_FLAG2 + 256 * 64 <= WS_KREV, "control block");

struct Params {
  const unsigned short* u;
  const unsigned short* lar;
  const unsigned short* aim;
  const unsigned short* cre;
  const unsigned short* cim;
  const unsigned short* ldt;
  const unsigned short* dsk;
  const unsigned short* wqkv;
  const unsigned short* wphi;
  const unsigned short* wo;
  unsigned short* out;
  unsigned char* ws;
};

DI float bf2f(unsigned short b) { return __builtin_bit_cast(float, (unsigned)b << 16); }
DI float blo(unsigned x) { return __builtin_bit_cast(float, x << 16); }
DI float bhi(unsigned x) { return __builtin_bit_cast(float, x & 0xffff0000u); }
DI unsigned pack2(float a, float b) { bf2 v = {(__bf16)a, (__bf16)b}; return __builtin_bit_cast(unsigned, v); }
DI float rbf(float a) { return blo(pack2(a, a)); }
DI f4 mfma32(u4 a, u4 b, f4 c) { return __builtin_amdgcn_mfma_f32_16x16x32_bf16(__builtin_bit_cast(s8, a), __builtin_bit_cast(s8, b), c, 0, 0, 0); }
DI f4 mfma16(u2 a, u2 b, f4 c) { return __builtin_amdgcn_mfma_f32_16x16x16bf16_1k(__builtin_bit_cast(s4, a), __builtin_bit_cast(s4, b), c, 0, 0, 0); }
DI void mfma_va(f4& acc, const u4& a, const u4& b) { asm volatile("v_mfma_f32_16x16x32_bf16 %0, %1, %2, %0" : "+v"(acc) : "a"(a), "v"(b)); }
DI void mfma_va0(f4& acc, const u4& a, const u4& b) { asm volatile("v_mfma_f32_16x16x32_bf16 %0, %1, %2, 0" : "=&v"(acc) : "a"(a), "v"(b)); }
DI void mfma_vv(f4& acc, const u4& a, const u4& b) { asm volatile("v_mfma_f32_16x16x32_bf16 %0, %1, %2, %0" : "+v"(acc) : "v"(a), "v"(b)); }
DI void mfma_vv0(f4& acc, const u4& a, const u4& b) { asm volatile("v_mfma_f32_16x16x32_bf16 %0, %1, %2, 0" : "=&v"(acc) : "v"(a), "v"(b)); }
DI void mfma16_vvc(f4& acc, const u2& a, const u2& b) { asm volatile("v_mfma_f32_16x16x16_bf16 %0, %1, %2, %0" : "+v"(acc) : "v"(a), "v"(b)); }
DI void valu_to_mfma() { asm volatile("s_nop 1"); }
DI void mfma_done() { asm volatile("s_nop 7" ::: "memory"); __builtin_amdgcn_sched_barrier(0); }
DI u2 lds_tr(lds_t* p) { return __builtin_bit_cast(u2, __builtin_amdgcn_ds_read_tr16_b64_v4i16((LAS s4*)p)); }
DI u4 cat(u2 a, u2 b) { return u4{a.x, a.y, b.x, b.y}; }
DI rsrc_t make_rsrc(const void* p, unsigned bytes) { return __builtin_amdgcn_make_buffer_rsrc((void*)p, 0, (int)bytes, 0x00020000); }
DI u4 ld16(rsrc_t r, unsigned off) { return __builtin_amdgcn_raw_buffer_load_b128(r, off, 0, AUX_SC1); }
DI void st16(rsrc_t r, unsigned off, u4 v) { __builtin_amdgcn_raw_buffer_store_b128(v, r, off, 0, AUX_SC1); }
DI void drain() { asm volatile("s_waitcnt vmcnt(0)" ::: "memory"); }
DI void settle() { __builtin_amdgcn_s_waitcnt(0x0F70); }
DI void dma4(rsrc_t r, unsigned v0, unsigned v1, unsigned v2, unsigned v3, unsigned soff, unsigned lds) {
  asm volatile("s_mov_b32 m0, %6\n\ts_nop 0\n\t"
               "buffer_load_dwordx4 %0, %4, %5 offen lds\n\tbuffer_load_dwordx4 %1, %4, %5 offen offset:1024 lds\n\t"
               "buffer_load_dwordx4 %2, %4, %5 offen offset:2048 lds\n\tbuffer_load_dwordx4 %3, %4, %5 offen offset:3072 lds"
               :: "v"(v0), "v"(v1), "v"(v2), "v"(v3), "s"(r), "s"(soff), "s"(lds) : "memory");
}
#define SB() __builtin_amdgcn_sched_barrier(0)
template <int CTRL> DI float dpp_f(float v) { return __builtin_bit_cast(float, __builtin_amdgcn_mov_dpp(__builtin_bit_cast(int, v), CTRL, 0xf, 0xf, true)); }
DI float row_sum(float v) { v += dpp_f<0x128>(v); v += dpp_f<0x124>(v); v += dpp_f<0x122>(v); v += dpp_f<0x121>(v); return v; }
DI f2 swap32(float a, float b) { auto r = __builtin_amdgcn_permlane32_swap(__builtin_bit_cast(unsigned, a), __builtin_bit_cast(unsigned, b), false, false);
  const unsigned r0 = r[0], r1 = r[1];
  return f2{__builtin_bit_cast(float, r0), __builtin_bit_cast(float, r1)}; }
DI f2 swap16(float a, float b) { auto r = __builtin_amdgcn_permlane16_swap(__builtin_bit_cast(unsigned, a), __builtin_bit_cast(unsigned, b), false, false);
  const unsigned r0 = r[0], r1 = r[1];
  return f2{__builtin_bit_cast(float, r0), __builtin_bit_cast(float, r1)}; }
DI float fmax2(float a, float b) { return __builtin_fmaxf(a, b); }
DI float fmin2(float a, float b) { return __builtin_fminf(a, b); }
DI float xrow4_sum(float a, float b, float c, float d) {
  f2 s = swap32(a, b); const float ab = s.x + s.y;
  s = swap32(c, d); const float cd = s.x + s.y;
  s = swap16(ab, cd); return s.x + s.y;
}
DI f2 xrow4_maxmin(float a0, float a1, float a2, float a3, float b0, float b1, float b2, float b3) {
  asm("s_nop 1\n\t"
      "v_permlane32_swap_b32 %0, %1\n\tv_permlane32_swap_b32 %2, %3\n\tv_permlane32_swap_b32 %4, %5\n\tv_permlane32_swap_b32 %6, %7\n\t"
      "v_max_f32 %0, %0, %1\n\tv_max_f32 %2, %2, %3\n\tv_min_f32 %4, %4, %5\n\tv_min_f32 %6, %6, %7\n\t"
      "v_permlane16_swap_b32 %0, %2\n\ts_nop 0\n\tv_permlane16_swap_b32 %4, %6\n\t"
      "v_max_f32 %0, %0, %2\n\tv_min_f32 %4, %4, %6"
      : "+v"(a0), "+v"(a1), "+v"(a2), "+v"(a3), "+v"(b0), "+v"(b1), "+v"(b2), "+v"(b3));
  return f2{a0, b0};
}
DI f2 xrow_sum2(float a, float b) { f2 s = swap32(a, b); float c = s.x + s.y; s = swap16(c, c); c = s.x + s.y; return swap32(c, c); }

constexpr int LD_XT = 0;
constexpr int LD_KC = 65536;
constexpr int KC_COPY = 4160, KC_CH = 4 * KC_COPY;
constexpr int LD_U = LD_KC + 2 * KC_CH;
constexpr int U_CH = 4608;
constexpr int LD_X = LD_U + 2 * U_CH;
constexpr int LD_EX = LD_KC;
constexpr int LD_CONV_END = LD_X + 8192;
constexpr int LD_WA = 0, LD_WB = LD_CONV_END;
constexpr int LD_MISC = 131072;
constexpr int LD_PV = LD_MISC;
constexpr int PV_ROW = 104, PV_WAVE = 64 * PV_ROW;
constexpr int LD_STAT = LD_PV + 4 * PV_WAVE;
constexpr int LD_SCR = LD_STAT + 1024 + 256;
constexpr int LD_FLAG = LD_SCR + 2048;
constexpr int LD_OST = LD_PV;
constexpr int LDS_BYTES = 163840;
static_assert(LD_CONV_END <= LD_MISC && LD_FLAG + 64 <= LDS_BYTES, "LDS map");

struct Ctx {
  lds_t* L; rsrc_t ws; GAS unsigned* cw;
  int tid, lane, w, c, cp, rb, hp, layer;
};

DI void spin_fail(const Ctx& x) { __hip_atomic_store(x.cw + CW_TMO, 1u, RLX_AGENT); }
constexpr unsigned SPIN_CAP = 1u << 18;
DI void a2a_post(const Ctx& x, int e) {
  __syncthreads();
  if (x.tid < REP) __hip_atomic_fetch_add(x.cw + (CWI_A2A + (e * REP + x.tid) * NS + (x.c >> 3) % NS) * CW_STRIDE, 1u, RLX_AGENT);
}
DI void a2a_signal(const Ctx& x, int e) { drain(); a2a_post(x, e); }
DI GAS unsigned* a2a_word(const Ctx& x, int e) { return x.cw + (CWI_A2A + (e * REP + (x.c & 7) % REP) * NS + x.lane % NS) * CW_STRIDE; }
DI unsigned a2a_peek(const Ctx& x, int e) { return x.w == 0 ? __hip_atomic_load(a2a_word(x, e), RLX_AGENT) : 0u; }
DI void a2a_wait(const Ctx& x, int e, unsigned first) {
  if (x.w == 0) {
    if (!__all(first >= 256u / NS)) {
      GAS unsigned* q = a2a_word(x, e);
      for (unsigned s = 0;; ++s) {
        if (__all(__hip_atomic_load(q, RLX_AGENT) >= 256u / NS)) break;
        if (s > SPIN_CAP) { spin_fail(x); break; }
        __builtin_amdgcn_s_sleep(1);
      }
    }
    __builtin_amdgcn_fence(__ATOMIC_ACQUIRE, "wavefront");
  }
  settle();
  __syncthreads();
}

DI void conv_load_k(const Ctx& x, u4 (&kd)[3]) {
  const int ch = x.tid >> 7, q = x.tid & 127;
  const unsigned off = WS_KREV + ((2 * x.cp + ch) * 2048 + 16 * q) * 2;
  kd[0] = __builtin_amdgcn_raw_buffer_load_b128(x.ws, off, 0, 0);
  kd[1] = __builtin_amdgcn_raw_buffer_load_b128(x.ws, off + 16, 0, 0);
  kd[2] = __builtin_amdgcn_raw_buffer_load_b128(x.ws, off + (q == 127 ? 16 : 32), 0, 0);
}
DI void conv_build_k(const Ctx& x, const u4 (&kd)[3]) {
  const int ch = x.tid >> 7, q = x.tid & 127;
  const unsigned d8 = q == 127 ? 0u : kd[2].x, d9 = q == 127 ? 0u : kd[2].y;
  const unsigned d[10] = {kd[0].x, kd[0].y, kd[0].z, kd[0].w, kd[1].x, kd[1].y, kd[1].z, kd[1].w, d8, d9};
  lds_t* b = x.L + LD_KC + ch * KC_CH + 32 * q;
  *(LAS u4*)(b) = u4{d[0], d[1], d[2], d[3]}; *(LAS u4*)(b + 16) = u4{d[4], d[5], d[6], d[7]};
  *(LAS u4*)(b + 2 * KC_COPY) = u4{d[1], d[2], d[3], d[4]}; *(LAS u4*)(b + 2 * KC_COPY + 16) = u4{d[5], d[6], d[7], d[8]};
  unsigned e[9];
#pragma unroll
  for (int i = 0; i < 9; ++i) e[i] = __builtin_amdgcn_alignbit(d[i + 1], d[i], 16);
  *(LAS u4*)(b + KC_COPY) = u4{e[0], e[1], e[2], e[3]}; *(LAS u4*)(b + KC_COPY + 16) = u4{e[4], e[5], e[6], e[7]};
  *(LAS u4*)(b + 3 * KC_COPY) = u4{e[1], e[2], e[3], e[4]}; *(LAS u4*)(b + 3 * KC_COPY + 16) = u4{e[5], e[6], e[7], e[8]};
  if (x.tid < 16) {
    *(LAS u4*)(x.L + LD_KC + (x.tid >> 1) * KC_COPY + 4096 + (x.tid & 1) * 16) = u4{0, 0, 0, 0};
  }
  if (x.tid >= 64 && x.tid < 128) {
    const int i = x.tid - 64;
    *(LAS u4*)(x.L + LD_U + (i >> 5) * U_CH + (i & 31) * 16) = u4{0, 0, 0, 0};
  }
}
template <int PAR> struct Mu { static constexpr int v(int k) { return PAR ? (k == 0 ? 1 : k == 1 ? 2 : k == 2 ? 5 : 6) : (k == 0 ? 0 : k == 1 ? 3 : k == 2 ? 4 : 7); } };
template <int PAR> constexpr bool conv_first(int tau, int i, int k) {
  if (i) return false;
  for (int t2 = 0; t2 < tau; ++t2)
    for (int k2 = 0; k2 < 4; ++k2)
      if (Mu<PAR>::v(k2) + t2 == Mu<PAR>::v(k) + tau) return false;
  return true;
}
template <int PAR> DI lds_t* conv_a_base(const Ctx& x) {
  const int b = x.lane & 15, kq = x.lane >> 4, ch = x.w >> 1;
  const int s0 = 2047 - 16 * (kq >> 1) - b + 8 * (kq & 1), cc = s0 & 3;
  unsigned bo = LD_KC + ch * KC_CH + cc * KC_COPY + 2 * (s0 - cc) - 64 * 63;
  asm volatile("" : "+v"(bo));
  return x.L + bo;
}
template <int PAR> DI u4 conv_a_frag(lds_t* base, int k, int i) {
  const int m = 8 * Mu<PAR>::v(k) + i;
  const u2 lo = *(volatile LAS u2*)(base + 64 * (63 - m)), hi = *(volatile LAS u2*)(base + 64 * (63 - m) + 8);
  return cat(lo, hi);
}
template <int PAR, int K0> DI void conv_load_a(const Ctx& x, u4 (&a)[32]) {
  lds_t* base = conv_a_base<PAR>(x);
#pragma unroll
  for (int k = K0; k < K0 + 2; ++k)
#pragma unroll
    for (int i = 0; i < 8; ++i) a[8 * k + i] = conv_a_frag<PAR>(base, k, i);
}
struct WDma { unsigned lane16, soff, lo, hi; };
DI WDma q_dma_w_setup(const Ctx& x) {
  const unsigned lo = x.w < 3 ? LD_WA + x.w * 24576 : LD_WB + 8192, hi = x.w < 2 ? lo : x.w == 2 ? LD_WB - 16384 : lo;
  return WDma{(unsigned)x.lane * 16, WS_WFRAG + x.hp * (96 * 1024) + x.w * 24576, lo, hi};
}
template <int I> DI void q_dma_w1(const Ctx& x, const WDma& d) {
  unsigned t;
  asm volatile("s_add_u32 m0, %4, %5\n\ts_add_u32 %0, %3, %5\n\tbuffer_load_dwordx4 %1, %2, %0 offen sc1 lds"
               : "=&s"(t) : "v"(d.lane16), "s"(x.ws), "s"(d.soff), "s"(I < 16 ? d.lo : d.hi), "n"(I * 1024) : "memory", "scc");
}
DI void q_take_w(const Ctx& x, u4 (&wf)[48]) {
  const int h = x.w & 1;
  const unsigned a0 = LD_WA + h * 49152 + x.lane * 16, a1 = (h ? LD_WB - 16384 : LD_WA) + x.lane * 16;
#pragma unroll
  for (int i = 0; i < 48; ++i)
    asm volatile("ds_read_b128 %0, %1 offset:%2" : "=a"(wf[i]) : "v"(i < 16 ? a0 : a1), "n"(i * 1024));
  asm volatile("s_waitcnt lgkmcnt(0)" ::: "memory");
  SB();
}
template <int TAU, int I> DI void conv_dma_w(const Ctx& x, const WDma& d) { if ((I & 1) && TAU < 6) { q_dma_w1<(TAU < 6 ? 4 * TAU + (I >> 1) : 0)>(x, d); SB(); } }
template <int PAR, bool FIRST> DI void conv_mfma(const Ctx& x, u4 (&a)[32], f4 (&acc)[8]) {
  const WDma wd = q_dma_w_setup(x);
  const int al = x.lane & 15, kq = x.lane >> 4, ch = x.w >> 1;
  unsigned bo = LD_U + ch * U_CH + 2 * (256 + 16 * al - 16 * (kq >> 1) + 8 * (kq & 1)) - 64 * 7;
  asm volatile("" : "+v"(bo));
  lds_t* base = x.L + bo;
  constexpr int NT = 8 - Mu<PAR>::v(0);
  u4 b[8];
  if (FIRST) {
    lds_t* abase = conv_a_base<PAR>(x);
#pragma unroll
    for (int i = 0; i < 8; ++i) { b[i] = *(LAS u4*)(base + 64 * (7 - i)); a[i] = conv_a_frag<PAR>(abase, 0, i); }
    SB();
#pragma unroll
    for (int k = 0; k < 4; ++k) {
#pragma unroll
      for (int i = 0; i < 8; ++i) {
        if (i == 0) mfma_vv0(acc[Mu<PAR>::v(k)], a[8 * k + i], b[i]); else mfma_vv(acc[Mu<PAR>::v(k)], a[8 * k + i], b[i]);
        if (k < 3) a[8 * (k + 1) + i] = conv_a_frag<PAR>(abase, k + 1, i);
        else b[i] = *(LAS u4*)(base + 64 * (8 - i + 7));
        SB();
      }
      switch (k) {
        case 0: q_dma_w1<0>(x, wd); break; case 1: q_dma_w1<1>(x, wd); break;
        case 2: q_dma_w1<2>(x, wd); break; default: q_dma_w1<3>(x, wd); break;
      }
      SB();
    }
  } else {
#pragma unroll
    for (int i = 0; i < 8; ++i) b[i] = *(LAS u4*)(base + 64 * (7 - i));
    SB();
  }
#pragma unroll
  for (int tau = FIRST ? 1 : 0; tau < 8; ++tau) {
    if (tau < NT) {
#pragma unroll
      for (int i = 0; i < 8; ++i) {
#pragma unroll
        for (int k = 0; k < 4; ++k)
          if (Mu<PAR>::v(k) + tau <= 7) {
            if (conv_first<PAR>(tau, i, k)) mfma_vv0(acc[Mu<PAR>::v(k) + tau], a[8 * k + i], b[i]);
            else mfma_vv(acc[Mu<PAR>::v(k) + tau], a[8 * k + i], b[i]);
          }
        if (tau + 1 < NT) b[i] = *(LAS u4*)(base + 64 * (8 * (tau + 1) - i + 7));
        SB();
        if (FIRST) {
          switch (8 * tau + i) {
#define W_(t, j) case 8 * t + j: conv_dma_w<t, j>(x, wd); break;
#define WT_(t) W_(t, 1) W_(t, 3) W_(t, 5) W_(t, 7)
            WT_(1) WT_(2) WT_(3) WT_(4) WT_(5)
#undef WT_
#undef W_
            default: break;
          }
        }
      }
    }
  }
}

struct ConvIn { u4 x0, x1, s[4]; };
DI void conv_load_in(const Ctx& x, int layer, ConvIn& in) {
  const unsigned xo = WS_XT + layer * XT_BYTES + (2 * x.cp * XT_STRIDE + 8 * x.tid) * 2;
  const unsigned so = WS_ST + layer * 2048 * 8 + x.tid * 32;
  in.s[0] = ld16(x.ws, so); in.s[1] = ld16(x.ws, so + 16); in.s[2] = ld16(x.ws, so + 8192); in.s[3] = ld16(x.ws, so + 8192 + 16);
  in.x0 = ld16(x.ws, xo); in.x1 = ld16(x.ws, xo + XT_STRIDE * 2);
}
DI f2 unpk(unsigned v) { return f2{blo(v), bhi(v)}; }
DI unsigned pk(f2 v) { return pack2(v.x, v.y); }
DI void conv_build_u(const Ctx& x, const ConvIn& in) {
  const f4 sa = __builtin_bit_cast(f4, in.s[0]), sb = __builtin_bit_cast(f4, in.s[1]), qa = __builtin_bit_cast(f4, in.s[2]), qb = __builtin_bit_cast(f4, in.s[3]);
  const f2 sv[4] = {f2{sa[0], sa[1]}, f2{sa[2], sa[3]}, f2{sb[0], sb[1]}, f2{sb[2], sb[3]}};
  const f2 qv[4] = {f2{qa[0], qa[1]}, f2{qa[2], qa[3]}, f2{qb[0], qb[1]}, f2{qb[2], qb[3]}};
  u4 u0, u1;
#pragma unroll
  for (int i = 0; i < 4; ++i) {
    const f2 m = sv[i] * (1.f / DM);
    const f2 ve = (qv[i] * (1.f / DM) + LN_EPS) - m * m;
    const f2 r = {__builtin_amdgcn_rsqf(__builtin_fabsf(ve.x)), __builtin_amdgcn_rsqf(__builtin_fabsf(ve.y))};
    const f2 nmr = -m * r;
    u0[i] = pk(unpk(in.x0[i]) * r + nmr);
    u1[i] = pk(unpk(in.x1[i]) * r + nmr);
  }
  *(LAS u4*)(x.L + LD_U + 512 + 16 * x.tid) = u0; *(LAS u4*)(x.L + LD_U + U_CH + 512 + 16 * x.tid) = u1;
  *(LAS u4*)(x.L + LD_X + 16 * x.tid) = in.x0; *(LAS u4*)(x.L + LD_X + 4096 + 16 * x.tid) = in.x1;
}
DI void poison_o(const Ctx& x, int layer) {
#pragma unroll
  for (int i = 0; i < 2; ++i) st16(x.ws, WS_OB + layer * OB_BYTES + (x.c * 2 + i) * 4096 + x.tid * 16, u4{~0u, ~0u, ~0u, ~0u});
}
template <int PAR, bool LOADW> DI void conv_wave(const Ctx& x, float dsk, u4 (&a)[32]) {
  const int ch = x.w >> 1;
  f4 acc[8];
  if (PAR) acc[0] = f4{0, 0, 0, 0};
  conv_mfma<PAR, LOADW>(x, a, acc);
  mfma_done();
  if (LOADW) __syncthreads();
  const int al = x.lane & 15, g = x.lane >> 4;
  unsigned eo = LD_EX + x.lane * 16, uo = LD_U + ch * U_CH + 512 + 2 * (1024 * PAR + 16 * al + 4 * g);
  asm volatile("" : "+v"(eo), "+v"(uo));
  lds_t* mine = x.L + eo + x.w * 4096;
  lds_t* other = x.L + eo + (x.w ^ 1) * 4096;
#pragma unroll
  for (int t = 0; t < 4; ++t) *(LAS f4*)(mine + t * 1024) = acc[PAR ? t : 4 + t];
  f2 v[4][2];
#pragma unroll
  for (int t = 0; t < 4; ++t) {
    const u2 uu = *(LAS u2*)(x.L + uo + 512 * t), xx = *(LAS u2*)(x.L + uo + (LD_X - LD_U - 512) + ch * (4096 - U_CH) + 512 * t);
    const f4 own = acc[PAR ? 4 + t : t];
    v[t][0] = unpk(uu.x) * dsk + (unpk(xx.x) + f2{own[0], own[1]});
    v[t][1] = unpk(uu.y) * dsk + (unpk(xx.y) + f2{own[2], own[3]});
  }
  __syncthreads();
  const unsigned so = WS_X1 + x.layer * X1_BYTES + (((8 * PAR + 2 * (g & 1) + (al >> 3)) * 512 + 2 * x.cp + ch) * 128 + 16 * (al & 7) + 8 * (g >> 1)) * 2;
#pragma unroll
  for (int t = 0; t < 4; t += 2) {
    const f4 o0 = *(LAS f4*)(other + t * 1024), o1 = *(LAS f4*)(other + (t + 1) * 1024);
    const unsigned a0 = pk(v[t][0] + f2{o0[0], o0[1]}), a1 = pk(v[t][1] + f2{o0[2], o0[3]});
    const unsigned b0 = pk(v[t + 1][0] + f2{o1[0], o1[1]}), b1 = pk(v[t + 1][1] + f2{o1[2], o1[3]});
    const auto s0 = __builtin_amdgcn_permlane16_swap(a0, b0, false, false), s1 = __builtin_amdgcn_permlane16_swap(a1, b1, false, false);
    st16(x.ws, so + t * (2 * 512 * 256), u4{s0[0], s1[0], s0[1], s1[1]});
  }
  if (LOADW) poison_o(x, 0);
  drain();
}

DI f2 cmul(f2 a, f2 b) { return f2{a.x, a.x} * b + f2{a.y, a.y} * f2{-b.y, b.x}; }
DI f2 cexp_p(float wr2, float wrev, float p) {
  const float e = __builtin_amdgcn_exp2f(wr2 * p);
  const float a = __builtin_amdgcn_fractf(wrev * p);
  return f2{e * __builtin_amdgcn_cosf(a), e * __builtin_amdgcn_sinf(a)};
}
constexpr int KG_ROW = 528;
constexpr int KG_A = 0;
constexpr int KG_X = 2 * 48 * KG_ROW;
constexpr int KG_T = LD_CONV_END;
constexpr int KG_B = KG_T + 2 * 2056 * 4;
static_assert(KG_X + 64 * 136 <= LD_KC && KG_B + 2 * 16 * KG_ROW <= LDS_BYTES, "prologue LDS map");

struct ProW { u2 a0, a1; unsigned short b[4]; };
struct ProIn { unsigned short lar, aim, cre, cim, ldt; u4 xt[2], xr[2]; ProW w[2]; unsigned epoch; };
DI void pro_load_w(const Params& p, int F, int lane, ProW& w) {
  const int fh = F / 48, fm = (F % 48) >> 4, fks = F & 15, row = lane & 15, kq = lane >> 4;
  w.a0 = *(const u2*)(p.wqkv + (32 * fks + row) * 1536 + fm * 512 + 16 * fh + 4 * kq);
  w.a1 = *(const u2*)(p.wqkv + (32 * fks + 16 + row) * 1536 + fm * 512 + 16 * fh + 4 * kq);
  const unsigned short* q = p.wphi + fh * 256 + (4 * kq) * 16 + row;
#pragma unroll
  for (int j = 0; j < 4; ++j) w.b[j] = q[16 * j];
}
DI void pro_fold_w(const Ctx& x, int F, const ProW& w) {
  const bool fold = ((F % 48) >> 4) < 2;
  const int dd = (x.lane & 15) - 4 * (x.lane >> 4);
  const u2 wb = fold ? u2{w.b[0] | ((unsigned)w.b[1] << 16), w.b[2] | ((unsigned)w.b[3] << 16)}
                     : u2{(dd == 0 ? 0x3f80u : 0u) | (dd == 1 ? 0x3f800000u : 0u), (dd == 2 ? 0x3f80u : 0u) | (dd == 3 ? 0x3f800000u : 0u)};
  const f4 d0 = mfma16(w.a0, wb, f4{0, 0, 0, 0}), d1 = mfma16(w.a1, wb, f4{0, 0, 0, 0});
  float e[8];
#pragma unroll
  for (int r = 0; r < 4; ++r) {
    const float b0 = dpp_f<0x150>(d0[r]), b1 = dpp_f<0x150>(d1[r]);
    e[r] = d0[r] - (fold ? b0 : 0.f); e[4 + r] = d1[r] - (fold ? b1 : 0.f);
  }
  u4 o = {pack2(e[0], e[1]), pack2(e[2], e[3]), pack2(e[4], e[5]), pack2(e[6], e[7])};
  if (fold && (x.lane & 15) == 0) o = u4{0x3f803f80u, 0x3f803f80u, 0x3f803f80u, 0x3f803f80u};
  st16(x.ws, WS_WFRAG + F * 1024 + x.lane * 16, o);
}
DI void pro_load(const Ctx& x, const Params& p, ProIn& in) {
#pragma unroll
  for (int i = 0; i < 2; ++i) {
    in.xt[i] = *(const u4*)(p.u + (64 * (x.c >> 3) + 32 * i + (x.tid >> 3)) * DM + 64 * (x.c & 7) + 8 * (x.tid & 7));
    in.xr[i] = *(const u4*)(p.u + (8 * x.c + 2 * x.w + i) * DM + 8 * x.lane);
  }
  in.epoch = __builtin_amdgcn_raw_buffer_load_b32(x.ws, WS_EPOCH, 0, AUX_SC1);
  const int d = 2 * x.cp + (x.tid >> 7), n = x.tid & 127;
  in.lar = p.lar[d * NST + n]; in.aim = p.aim[d * NST + n]; in.cre = p.cre[d * NST + n]; in.cim = p.cim[d * NST + n]; in.ldt = p.ldt[d];
  SB();
}
DI void pro_load_w2(const Ctx& x, const Params& p, ProIn& in) {
  pro_load_w(p, 6 * x.c + x.w, x.lane, in.w[0]);
  pro_load_w(p, 6 * x.c + 4 + (x.w & 1), x.lane, in.w[1]);
  SB();
}
constexpr int CODE_KB = 48;
struct Warm { unsigned a, b, c; };
DI unsigned pro_warm_code(const Ctx& x, const unsigned char* code) {
  unsigned v = 0;
  if (x.w == 3 && x.lane < CODE_KB * 8 / 32) v = *(const GAS unsigned*)(code + ((x.c >> 3) * (CODE_KB * 8 / 32) + x.lane) * 128);
  return v;
}
DI void pro_warm(const Ctx& x, const Params& p, Warm& wm) {
  const int j = x.c >> 3;
  wm.b = wm.c = 0;
  if (x.w == 3) {
    wm.b = *(const GAS unsigned*)((const unsigned char*)p.wo + (j * 128 + x.lane) * 128);
    wm.c = *(const GAS unsigned*)((const unsigned char*)p.wo + (j * 128 + 64 + x.lane) * 128);
  }
}
DI void pro_warm_retire(const Warm& wm) { asm volatile("" :: "v"(wm.a), "v"(wm.b), "v"(wm.c)); }
DI void pro_init(const Ctx& x) {
  const rsrc_t ws = x.ws;
  if (x.c < 2) { const int i = x.c * 256 + x.tid; if (i < CWI_END) __builtin_amdgcn_raw_buffer_store_b32(0u, ws, i * (CW_STRIDE * 4), 0, AUX_SC1); }
  if (x.c >= 8 && x.c < 12) st16(ws, WS_ST + 2048 * 8 + ((x.c - 8) * 256 + x.tid) * 16, u4{0, 0, 0, 0});
  static_assert(WS_ZP == WS_KVP + 2 * KVP_BYTES && (2 * KVP_BYTES + 2 * ZP_BYTES) == 256 * 272 * 16, "poisoned block");
  st16(ws, WS_KVP + (x.c * 272 + x.tid) * 16, u4{~0u, ~0u, ~0u, ~0u});
  if (x.tid < 16) st16(ws, WS_KVP + (x.c * 272 + 256 + x.tid) * 16, u4{~0u, ~0u, ~0u, ~0u});
}
DI void pro_publish_x(const Ctx& x, const ProIn& in) {
#pragma unroll
  for (int i = 0; i < 2; ++i) {
    lds_t* a = x.L + KG_X + (32 * i + (x.tid >> 3)) * 136 + (x.tid & 7) * 16;
    *(LAS u2*)a = u2{in.xt[i].x, in.xt[i].y}; *(LAS u2*)(a + 8) = u2{in.xt[i].z, in.xt[i].w};
  }
  {
    f2 a[2], q[2];
#pragma unroll
    for (int i = 0; i < 2; ++i) {
      a[i] = unpk(in.xr[i][0]); q[i] = a[i] * a[i];
#pragma unroll
      for (int k = 1; k < 4; ++k) { const f2 v = unpk(in.xr[i][k]); a[i] += v; q[i] += v * v; }
    }
    const float t = xrow4_sum(row_sum(a[0].x + a[0].y), row_sum(q[0].x + q[0].y), row_sum(a[1].x + a[1].y), row_sum(q[1].x + q[1].y));
    const int g = x.lane >> 4;
    if ((x.lane & 15) == 0)
      __builtin_amdgcn_raw_buffer_store_b32(__builtin_bit_cast(unsigned, t), x.ws, WS_ST + (g >> 1) * 8192 + (8 * x.c + 2 * x.w + (g & 1)) * 4, 0, AUX_SC1);
  }
  __syncthreads();
  const int g = x.lane >> 4, cl = x.lane & 15;
#pragma unroll
  for (int j = 0; j < 2; ++j) {
    lds_t* a = x.L + KG_X + (8 * (g + 4 * j) + (cl >> 2)) * 136 + (16 * x.w + 4 * (cl & 3)) * 2;
    st16(x.ws, WS_XT + ((64 * (x.c & 7) + 16 * x.w + cl) * XT_STRIDE + 64 * (x.c >> 3) + 8 * (g + 4 * j)) * 2, cat(lds_tr(a), lds_tr(a + 4 * 136)));
  }
}
DI void pro_tables(const Ctx& x, const ProIn& in) {
  const float dt = rbf(__expf(bf2f(in.ldt)));
  const float ar = -rbf(__expf(bf2f(in.lar)));
  const float wr2 = (ar * dt) * LOG2E, wrev = (bf2f(in.aim) * dt) * 0.15915494309189535f;
  const f2 c2 = {2.0f * (bf2f(in.cre) * dt), -2.0f * (bf2f(in.cim) * dt)};
  const int chl = x.tid >> 7, n = x.tid & 127;
  LAS unsigned* TA = (LAS unsigned*)(x.L + KG_A + chl * 48 * KG_ROW) + n;
  LAS unsigned* TB = (LAS unsigned*)(x.L + KG_B + chl * 16 * KG_ROW) + n;
  constexpr int RS = KG_ROW / 4;
  const f2 e8 = cexp_p(wr2, wrev, 8.f), e128 = cexp_p(wr2, -wrev, 128.f), e1 = cexp_p(wr2, -wrev, 1.f);
  f2 y = e8, h = cmul(c2, cexp_p(wr2, -wrev, 1024.f)), g[8], e[8];
  g[0] = c2; e[1] = e1; e[2] = cmul(e1, e1);
  TB[0] = 0x00003f80u;
#pragma unroll
  for (int i = 0; i < 8; ++i) {
    TB[(2 * i + 1) * RS] = pk(y);
    TA[(32 + i) * RS] = pk(h);
    const f2 y1 = cmul(y, e8), h1 = cmul(h, e128);
    if (i < 7) g[i + 1] = cmul(g[i], e128);
    if (i == 0) { e[3] = cmul(e[2], e[1]); e[4] = cmul(e[2], e[2]); }
    if (i == 1) { e[5] = cmul(e[4], e[1]); e[6] = cmul(e[3], e[3]); e[7] = cmul(e[4], e[3]); }
    if (i < 7) { TB[(2 * i + 2) * RS] = pk(y1); y = cmul(y1, e8); }
    h = h1;
  }
  TA[40 * RS] = pk(h);
#pragma unroll
  for (int k = 0; k < 8; ++k) {
    TA[k * RS] = pk(k ? cmul(g[0], e[k]) : g[0]);
    TA[(8 + k) * RS] = pk(k ? cmul(g[1], e[k]) : g[1]);
    if (k < 4) { TA[(16 + k) * RS] = pk(k ? cmul(g[2], e[2 * k]) : g[2]); TA[(20 + k) * RS] = pk(k ? cmul(g[3], e[2 * k]) : g[3]); }
    else { TA[(24 + 2 * (k - 4)) * RS] = pk(g[k]); TA[(25 + 2 * (k - 4)) * RS] = pk(cmul(g[k], e[4])); }
  }
}
DI void pro_kgemm(const Ctx& x) {
  const int ch = x.w >> 1, j = x.lane & 15, g = x.lane >> 4, odd = x.w & 1;
  unsigned ao = KG_A + (ch * 48 + 16 * odd + j) * KG_ROW + g * 16, a2o = KG_A + (ch * 48 + 32 + j) * KG_ROW + g * 16;
  unsigned bo = KG_B + (ch * 16 + j) * KG_ROW + g * 16;
  asm volatile("" : "+v"(ao), "+v"(a2o), "+v"(bo));
  u4 a0[8], a2[8], b[8];
#pragma unroll
  for (int ks = 0; ks < 8; ++ks) { b[ks] = *(LAS u4*)(x.L + bo + ks * 64); a0[ks] = *(LAS u4*)(x.L + ao + ks * 64); a2[ks] = *(LAS u4*)(x.L + a2o + ks * 64); }
  SB();
  f4 acc0 = {0, 0, 0, 0}, acc2 = acc0;
#pragma unroll
  for (int ks = 0; ks < 8; ++ks) { acc0 = mfma32(a0[ks], b[ks], acc0); acc2 = mfma32(a2[ks], b[ks], acc2); }
  const int pe = 128 * (g >> 1) + 4 * (g & 1), po = g < 2 ? 256 + 128 * g : 512 + 256 * (g - 2);
  const unsigned t0 = KG_T + (ch * 2056 + 8 * j + (odd ? po : pe)) * 4;
  const int s1 = odd ? (g < 2 ? 8 : 16) : 4, s2 = odd ? (g < 2 ? 16 : 512) : 8;
  *(LAS float*)(x.L + t0) = acc0[0]; *(LAS float*)(x.L + t0 + s1) = acc0[1];
  *(LAS float*)(x.L + t0 + s2) = acc0[2]; *(LAS float*)(x.L + t0 + s1 + s2) = acc0[3];
  const unsigned t2 = KG_T + (ch * 2056 + 1024 + 512 * g + 8 * j) * 4, dummy = KG_T + 2052 * 4;
#pragma unroll
  for (int r = 0; r < 4; ++r) *(LAS float*)(x.L + ((4 * g + r < 8 || (4 * g + r == 8 && j == 0)) ? t2 + 512 * r : dummy)) = acc2[r];
}
constexpr int KG_D = 0, KG_DCH = 4096 + 64;
DI void pro_expand(const Ctx& x) {
  unsigned tb = KG_T, db = KG_D + x.tid * 4;
  f2 l0 = {(float)(2047 - 2 * x.tid), (float)(2046 - 2 * x.tid)};
  asm volatile("" : "+v"(tb), "+v"(db), "+v"(l0));
#pragma unroll
  for (int i = 0; i < 4; ++i) {
    unsigned r = pk(l0 - (float)(512 * i));
    asm("" : "+v"(r));
    const unsigned a0 = tb + 4 * (int)blo(r), a1 = tb + 4 * (int)bhi(r);
#pragma unroll
    for (int ch = 0; ch < 2; ++ch)
      *(LAS unsigned*)(x.L + db + ch * KG_DCH + i * 1024) = pack2(*(LAS float*)(x.L + a0 + ch * (2056 * 4)), *(LAS float*)(x.L + a1 + ch * (2056 * 4)));
  }
}
DI void pro_take_k(const Ctx& x, u4 (&kd)[3]) {
  const int ch = x.tid >> 7, q = x.tid & 127;
  lds_t* a = x.L + KG_D + ch * KG_DCH + 32 * q;
  kd[0] = *(LAS u4*)a; kd[1] = *(LAS u4*)(a + 16); kd[2] = *(LAS u4*)(a + 32);
  const unsigned off = WS_KREV + ((2 * x.cp + ch) * 2048 + 16 * q) * 2;
  __builtin_amdgcn_raw_buffer_store_b128(kd[0], x.ws, off, 0, 0);
  __builtin_amdgcn_raw_buffer_store_b128(kd[1], x.ws, off + 16, 0, 0);
}
DI bool pro_all(const Ctx& x, bool mine) {
  const unsigned ok = __all(mine);
  if (x.lane == 0) ((LAS unsigned*)(x.L + LD_FLAG))[x.w] = ok;
  __syncthreads();
  const u4 r = *(LAS u4*)(x.L + LD_FLAG);
  return r.x & r.y & r.z & r.w;
}
DI void pro_wait(const Ctx& x, unsigned flags, unsigned tag, unsigned first) {
  if (pro_all(x, first == tag)) return;
  for (unsigned s = 0;; ++s) {
    const unsigned v = __builtin_amdgcn_raw_buffer_load_b32(x.ws, flags + x.tid * 64, 0, AUX_SC1);
    __syncthreads();
    if (pro_all(x, v == tag)) break;
    if (s > SPIN_CAP) { if (x.tid == 0) spin_fail(x); break; }
  }
  settle();
}
DI u4 pro_peek_wave(const Ctx& x, unsigned flags) {
  u4 v;
#pragma unroll
  for (int i = 0; i < 4; ++i) v[i] = __builtin_amdgcn_raw_buffer_load_b32(x.ws, flags + (64 * i + x.lane) * 64, 0, AUX_SC1);
  return v;
}
DI void pro_wait_wave(const Ctx& x, unsigned flags, unsigned tag, u4 v) {
  for (unsigned s = 0; !__all(v.x == tag && v.y == tag && v.z == tag && v.w == tag); ++s) {
    if (s > SPIN_CAP) { spin_fail(x); break; }
    asm volatile("" ::: "memory");
    v = pro_peek_wave(x, flags);
  }
  settle();
}

DI void q_wsum(const u4 (&wf)[48], f4 (&ws)[3]) {
  const u4 ones = {0x3f803f80u, 0x3f803f80u, 0x3f803f80u, 0x3f803f80u};
  ws[0] = f4{0, 0, 0, 0}; ws[1] = ws[0]; ws[2] = ws[0];
#pragma unroll
  for (int ks = 0; ks < 16; ++ks)
#pragma unroll
    for (int m = 0; m < 3; ++m) ws[m] = mfma32(wf[16 * m + ks], ones, ws[m]);
}
struct QDma { unsigned v0, v1, base, lds; };
DI QDma q_dma_setup(const Ctx& x) {
  const int row = x.lane >> 4, pc = x.lane & 15;
  return QDma{(unsigned)(row * 256 + ((pc ^ (row << 1)) * 16)), (unsigned)(row * 256 + ((pc ^ ((4 + row) << 1)) * 16)),
              WS_X1 + x.layer * X1_BYTES + x.rb * (512 * 256) + x.w * 4096, (unsigned)(LD_XT + x.w * 4096)};
}
template <int OFF> DI void dma2_sc1(rsrc_t r, const QDma& d) {
  unsigned t;
  asm volatile("s_add_u32 m0, %5, %6\n\ts_add_u32 %0, %4, %6\n\t"
               "buffer_load_dwordx4 %1, %3, %0 offen sc1 lds\n\tbuffer_load_dwordx4 %2, %3, %0 offen offset:1024 sc1 lds"
               : "=&s"(t) : "v"(d.v0), "v"(d.v1), "s"(r), "s"(d.base), "s"(d.lds), "n"(OFF) : "memory", "scc");
}
template <int OFF> DI void dma1_sc1(rsrc_t r, const QDma& d) {
  unsigned t;
  asm volatile("s_add_u32 m0, %4, %5\n\ts_add_u32 %0, %3, %5\n\tbuffer_load_dwordx4 %1, %2, %0 offen sc1 lds"
               : "=&s"(t) : "v"((OFF & 1024) ? d.v1 : d.v0), "s"(r), "s"(d.base), "s"(d.lds), "n"(OFF) : "memory", "scc");
}
struct QAddr { lds_t* lo[4]; lds_t* hi[4]; };
DI void q_frag_addr(const Ctx& x, QAddr& ba) {
  const int g = x.lane >> 4, q = (x.lane & 15) >> 2, pp = x.lane & 3, ph = x.w >> 1;
  const int r7 = 4 * (g & 1) + q;
#pragma unroll
  for (int pt = 0; pt < 4; ++pt) {
    const int lc = 8 * ph + 2 * (pt ^ (2 * (x.w & 1))) + (pp >> 1);
    unsigned o = LD_XT + (4 * g + q) * 256 + ((lc ^ (r7 << 1)) * 16) + 8 * (pp & 1), o2 = o + 65536;
    asm volatile("" : "+v"(o), "+v"(o2));
    ba.lo[pt] = x.L + o; ba.hi[pt] = x.L + o2;
  }
}
template <int KS> DI u4 q_ldb1(const QAddr& ba, int pt) {
  lds_t* p = (KS < 8 ? ba.lo[pt] : ba.hi[pt]) + (KS & 7) * 8192;
  return cat(lds_tr(p), lds_tr(p + 4096));
}
template <int KS> DI void q_ldb(const QAddr& ba, u4 (&b)[4]) {
#pragma unroll
  for (int pt = 0; pt < 4; ++pt) b[pt] = q_ldb1<KS>(ba, pt);
}
template <int N> DI void wait_vm_barrier() { asm volatile("s_waitcnt vmcnt(%0)\n\ts_barrier" :: "n"(N) : "memory"); }

DI f4 bcast4(float r) {
  const f2 xy = swap16(r, r), ab = swap32(xy.x, xy.x), cd = swap32(xy.y, xy.y);
  return f4{ab.x, ab.y, cd.x, cd.y};
}
template <class F> DI void softmax4(const f4 (&t)[4], const float (&cs)[4], float csr, lds_t* scr_w, lds_t* scr_r, u4 (&out)[4], F mid) {
  float mx[4], mn[4];
#pragma unroll
  for (int pt = 0; pt < 4; ++pt) {
    mx[pt] = fmax2(fmax2(fmax2(t[pt][0], t[pt][1]), t[pt][2]), t[pt][3]);
    mn[pt] = fmin2(fmin2(fmin2(t[pt][0], t[pt][1]), t[pt][2]), t[pt][3]);
  }
  const f2 M = xrow4_maxmin(mx[0], mx[1], mx[2], mx[3], mn[0], mn[1], mn[2], mn[3]);
  *(LAS f2*)scr_w = M * f2{-csr, csr};
  f2 bb[4];
#pragma unroll
  for (int pt = 0; pt < 4; ++pt) bb[pt] = *(LAS f2*)(scr_r + 128 * pt);
  SB();
  mid();
  SB();
  f2 ep[4][2], en[4][2];
  float sp[4], sn[4];
#pragma unroll
  for (int pt = 0; pt < 4; ++pt) {
    const f2 b = bb[pt];
    const f2 t0 = {t[pt][0], t[pt][1]}, t1 = {t[pt][2], t[pt][3]};
    const f2 p0 = t0 * cs[pt] + b.x, p1 = t1 * cs[pt] + b.x, n0 = b.y - t0 * cs[pt], n1 = b.y - t1 * cs[pt];
    ep[pt][0] = f2{__builtin_amdgcn_exp2f(p0.x), __builtin_amdgcn_exp2f(p0.y)}; ep[pt][1] = f2{__builtin_amdgcn_exp2f(p1.x), __builtin_amdgcn_exp2f(p1.y)};
    en[pt][0] = f2{__builtin_amdgcn_exp2f(n0.x), __builtin_amdgcn_exp2f(n0.y)}; en[pt][1] = f2{__builtin_amdgcn_exp2f(n1.x), __builtin_amdgcn_exp2f(n1.y)};
    const f2 a = ep[pt][0] + ep[pt][1], c = en[pt][0] + en[pt][1];
    sp[pt] = a.x + a.y; sn[pt] = c.x + c.y;
  }
  const f4 ip = bcast4(__builtin_amdgcn_rcpf(xrow4_sum(sp[0], sp[1], sp[2], sp[3])));
  const f4 in = bcast4(__builtin_amdgcn_rcpf(xrow4_sum(sn[0], sn[1], sn[2], sn[3])));
#pragma unroll
  for (int pt = 0; pt < 4; ++pt)
    out[pt] = u4{pk(ep[pt][0] * ip[pt]), pk(ep[pt][1] * ip[pt]), pk(en[pt][0] * in[pt]), pk(en[pt][1] * in[pt])};
}

struct QState { u4 pq[4]; u2 res[4]; u4 pk8[8], az; };
DI void o_load_partials(const Ctx& x, QState& st) {
  const int h = x.w & 1, ph = x.w >> 1, hh = 2 * x.hp + h;
  const unsigned ko = WS_KVP + x.layer * KVP_BYTES + (hh * 16 + 8 * ph) * 1024 + x.lane * 16;
#pragma unroll
  for (int i = 0; i < 8; ++i) st.pk8[i] = ld16(x.ws, ko + i * 1024);
  st.az = ld16(x.ws, WS_ZP + x.layer * ZP_BYTES + (hh * 16 + (x.lane & 15)) * 64 + (x.lane >> 4) * 16);
}

constexpr int UP = 4, NIN = 2 * (8 - UP);
template <int KS> struct QPass1 {
  static DI void run(rsrc_t ws, const QDma& d, const QAddr& ba, const u4 (&wf)[48], u4 (&b)[4], f4 (&ak)[4], f4 (&av)[4], f4 (&ag)[2]) {
    if (KS < 15 && (KS & 1)) wait_vm_barrier<(KS < 15 ? 4 * UP + 2 * (KS < NIN ? KS : NIN) - 4 * ((KS + 1) / 2 + 1) : 0)>();
    const u4 g0 = b[0], g1 = b[1];
#pragma unroll
    for (int pt = 0; pt < 4; ++pt) {
      const u4 cur = b[pt];
      lds_t* p = ((KS + 1) < 8 ? ba.lo[pt] : ba.hi[pt]) + ((KS + 1) & 7) * 8192;
      u2 t0, t1;
      if (KS == 0) mfma_va0(ak[pt], wf[16 + KS], cur); else mfma_va(ak[pt], wf[16 + KS], cur);
      if (KS < 15) t0 = lds_tr(p);
      SB();
      if (KS == 0) mfma_va0(av[pt], wf[32 + KS], cur); else mfma_va(av[pt], wf[32 + KS], cur);
      if (KS < 15) { t1 = lds_tr(p + 4096); b[pt] = cat(t0, t1); }
      SB();
    }
    if (KS == 0) mfma_vv0(ag[0], g0, g0); else mfma_vv(ag[0], g0, g0);
    SB();
    if (KS < NIN) dma1_sc1<(KS < NIN ? (UP + KS / 2) * 16384 + (KS & 1) * 2048 : 0)>(ws, d);
    SB();
    if (KS == 0) mfma_vv0(ag[1], g1, g1); else mfma_vv(ag[1], g1, g1);
    SB();
    if (KS < NIN) dma1_sc1<(KS < NIN ? (UP + KS / 2) * 16384 + (KS & 1) * 2048 + 1024 : 0)>(ws, d);
    SB();
    QPass1<KS + 1>::run(ws, d, ba, wf, b, ak, av, ag);
  }
};
template <> struct QPass1<16> { static DI void run(rsrc_t, const QDma&, const QAddr&, const u4 (&)[48], u4 (&)[4], f4 (&)[4], f4 (&)[4], f4 (&)[2]) {} };
template <int KS> struct QPass2 {
  static DI void run(const QAddr& ba, const u4 (&wf)[48], u4 (&b)[3][4], f4 (&aq)[4]) {
#pragma unroll
    for (int pt = 0; pt < 4; ++pt) {
      if (KS == 0) mfma_va0(aq[pt], wf[KS], b[0][pt]); else mfma_va(aq[pt], wf[KS], b[KS % 3][pt]);
      if (KS < 14) b[(KS + 2) % 3][pt] = q_ldb1<(KS < 14 ? KS + 2 : 15)>(ba, pt);
      SB();
    }
    QPass2<KS + 1>::run(ba, wf, b, aq);
  }
};
template <> struct QPass2<16> { static DI void run(const QAddr&, const u4 (&)[48], u4 (&)[3][4], f4 (&)[4]) {} };

DI void o_dma_wo(const Ctx& x, const Params& p) {
  const rsrc_t r = make_rsrc(p.wo, 512 * 512 * 2);
  const int row = x.lane >> 3, pc = x.lane & 7, cb = x.hp >> 1;
  const int b1 = (row >> 1) & 1;
  const unsigned v0 = row * 1024 + ((pc ^ (b1 << 1)) * 16), v1 = row * 1024 + ((pc ^ ((b1 | 2) << 1)) * 16) + 7168;
#pragma unroll 1
  for (int i = 4 * x.w; i < 4 * x.w + 4; ++i) dma4(r, v0, v1, v0 + 2 * 7168, v1 + 2 * 7168, i * 32768 + cb * 128, LD_XT + i * 4096);
}

DI void q_phase(const Ctx& x, const Params& p, const u4 (&wf)[48], const f4 (&wsum)[3], QState& st) {
  const QDma dm = q_dma_setup(x);
  dma2_sc1<0>(x.ws, dm); dma2_sc1<2048>(x.ws, dm); dma2_sc1<16384>(x.ws, dm); dma2_sc1<16384 + 2048>(x.ws, dm);
  dma2_sc1<32768>(x.ws, dm); dma2_sc1<32768 + 2048>(x.ws, dm); dma2_sc1<49152>(x.ws, dm); dma2_sc1<49152 + 2048>(x.ws, dm);
  static_assert(UP == 4, "four chunks are issued here");
  QAddr ba;
  q_frag_addr(x, ba);
  const int h = x.w & 1, ph = x.w >> 1, g = x.lane >> 4, cl = x.lane & 15;
  f4 ak[4], av[4], ag[2];
  {
    u4 b[4];
    wait_vm_barrier<4 * UP - 4>();
    q_ldb<0>(ba, b);
    SB();
    QPass1<0>::run(x.ws, dm, ba, wf, b, ak, av, ag);
    mfma_done();
  }
  {
    const int r = cl & 3;
    const unsigned so = LD_STAT + 4 * (64 * ph + 32 * h + cl);
    const unsigned a0 = g == 0 ? so : LD_STAT + 1024, a1 = g == (cl >> 2) ? so + 512 : LD_STAT + 1024;
#pragma unroll
    for (int i = 0; i < 2; ++i) {
      const float dq = r == 0 ? ag[i][0] : r == 1 ? ag[i][1] : r == 2 ? ag[i][2] : ag[i][3];
      *(LAS float*)(x.L + a0 + 64 * i) = ak[i][0];
      *(LAS float*)(x.L + a1 + 64 * i) = dq;
    }
  }
  __syncthreads();
  float mean[4], rs[4], cs[4];
#pragma unroll
  for (int pt = 0; pt < 4; ++pt) {
    LAS float* S = (LAS float*)(x.L + LD_STAT) + 64 * ph + 16 * (pt ^ (2 * h)) + cl;
    mean[pt] = S[0] * (1.f / DM);
    rs[pt] = __builtin_amdgcn_rsqf(__builtin_fabsf((S[128] * (1.f / DM) + LN_EPS) - mean[pt] * mean[pt]));
    cs[pt] = rs[pt] * LOG2E;
  }
  const float cs01 = (g & 2) ? cs[1] : cs[0], cs23 = (g & 2) ? cs[3] : cs[2], csr = (g & 1) ? cs23 : cs01;
  lds_t* scr_r = x.L + LD_SCR + x.w * 512 + cl * 8;
  lds_t* scr_w = scr_r + 128 * ((g >> 1) | ((g & 1) << 1));
  lds_t* pv = x.L + LD_PV + x.w * PV_WAVE;
  {
    f4 t[4];
#pragma unroll
    for (int pt = 0; pt < 4; ++pt) t[pt] = ak[pt] - mean[pt] * wsum[1];
    u4 pk4[4];
    softmax4(t, cs, csr, scr_w, scr_r, pk4, [&]() {
#pragma unroll
      for (int pt = 0; pt < 4; ++pt) {
        const f4 v = (av[pt] - mean[pt] * wsum[2]) * rs[pt];
        *(LAS u2*)(pv + (16 * pt + cl) * PV_ROW + 8 * g + 64) = u2{pack2(v[0], v[1]), pack2(v[2], v[3])};
      }
    });
#pragma unroll
    for (int pt = 0; pt < 4; ++pt) {
      lds_t* row = pv + (16 * pt + cl) * PV_ROW + 8 * g;
      *(LAS u2*)(row) = u2{pk4[pt].x, pk4[pt].y}; *(LAS u2*)(row + 32) = u2{pk4[pt].z, pk4[pt].w};
    }
  }
  f4 kv0, kv1, z0, z1;
  {
    u4 ones = {0x3f803f80u, 0x3f803f80u, 0x3f803f80u, 0x3f803f80u};
    asm volatile("" : "+v"(ones));
    const int q = cl >> 2, pp = cl & 3;
    u4 a0[2], a1[2], bv[2];
#pragma unroll
    for (int k2 = 0; k2 < 2; ++k2) {
      lds_t* a = pv + (32 * k2 + 8 * g + q) * PV_ROW + 8 * pp;
      a0[k2] = cat(lds_tr(a), lds_tr(a + 4 * PV_ROW));
      bv[k2] = cat(lds_tr(a + 64), lds_tr(a + 4 * PV_ROW + 64));
      a1[k2] = cat(lds_tr(a + 32), lds_tr(a + 4 * PV_ROW + 32));
    }
    SB(); valu_to_mfma();
    mfma_vv0(kv0, a0[0], bv[0]); mfma_vv0(z0, a0[0], ones); mfma_vv0(kv1, a1[0], bv[0]); mfma_vv0(z1, a1[0], ones);
    mfma_vv(kv0, a0[1], bv[1]); mfma_vv(z0, a0[1], ones); mfma_vv(kv1, a1[1], bv[1]); mfma_vv(z1, a1[1], ones);
    mfma_done();
  }
  {
    lds_t* mine = x.L + LD_PV + x.w * PV_WAVE + x.lane * 16;
    if (ph == 1) { *(LAS f4*)(mine) = kv0; *(LAS f4*)(mine + 1024) = kv1; *(LAS f4*)(mine + 2048) = z0; *(LAS f4*)(mine + 3072) = z1; }
    __syncthreads();
    if (ph == 0) {
      lds_t* o = x.L + LD_PV + (x.w + 2) * PV_WAVE + x.lane * 16;
      kv0 += *(LAS f4*)(o); kv1 += *(LAS f4*)(o + 1024); z0 += *(LAS f4*)(o + 2048); z1 += *(LAS f4*)(o + 3072);
      const int hr = (2 * x.hp + h) * 16 + x.rb;
      st16(x.ws, WS_KVP + x.layer * KVP_BYTES + hr * 1024 + x.lane * 16,
           u4{pack2(kv0[0], kv0[1]), pack2(kv0[2], kv0[3]), pack2(kv1[0], kv1[1]), pack2(kv1[2], kv1[3])});
      if (cl == 0) st16(x.ws, WS_ZP + x.layer * ZP_BYTES + hr * 64 + g * 16,
                        u4{pack2(z0[0], z0[1]), pack2(z0[2], z0[3]), pack2(z1[0], z1[1]), pack2(z1[2], z1[3])});
    }
  }
  {
    {
      const int cb = x.hp >> 1, q = cl >> 2, pos = 64 * (x.hp & 1) + 16 * x.w + 4 * (cl & 3);
      const int row = 64 * cb + 4 * g + q;
      unsigned ro = LD_XT + row * 256 + (((pos >> 3) ^ ((row & 7) << 1)) * 16) + (pos & 4) * 2;
      asm volatile("" : "+v"(ro));
#pragma unroll
      for (int ct = 0; ct < 4; ++ct) st.res[ct] = lds_tr(x.L + ro + ct * 4096);
    }
    f4 aq[4];
    u4 b[3][4];
    q_ldb<0>(ba, b[0]); q_ldb<1>(ba, b[1]);
    SB();
    QPass2<0>::run(ba, wf, b, aq);
    o_load_partials(x, st);
    SB();
    mfma_done();
    f4 t[4];
#pragma unroll
    for (int pt = 0; pt < 4; ++pt) t[pt] = aq[pt] - mean[pt] * wsum[0];
    softmax4(t, cs, csr, scr_w, scr_r, st.pq, [&]() {});
  }
}

DI void o_compute(const Ctx& x, QState& st) {
  const int h = x.w & 1, ph = x.w >> 1, g = x.lane >> 4, cl = x.lane & 15, hh = 2 * x.hp + h;
  const unsigned one = g == (cl >> 2) ? ((cl & 1) ? 0x3f800000u : 0x3f80u) : 0u;
  const unsigned e0 = (cl & 2) ? 0u : one, e1 = (cl & 2) ? one : 0u;
  const u4 sel0 = {e0, e1, 0u, 0u}, sel1 = {0u, 0u, e0, e1};
  f4 k0, k1, den[4];
  for (unsigned spin = 0;; ++spin) {
    f4 dp[4];
    SB(); valu_to_mfma();
#pragma unroll
    for (int pt = 0; pt < 4; ++pt) mfma_vv0(dp[pt], st.az, st.pq[pt]);
    mfma_vv0(k0, sel0, st.pk8[0]); mfma_vv0(k1, sel1, st.pk8[0]);
#pragma unroll
    for (int i = 1; i < 8; ++i) { mfma_vv(k0, sel0, st.pk8[i]); mfma_vv(k1, sel1, st.pk8[i]); }
    SB();
    u2 dq[4];
#pragma unroll
    for (int pt = 0; pt < 4; ++pt) { dq[pt] = u2{pack2(dp[pt][0], dp[pt][1]), pack2(dp[pt][2], dp[pt][3])}; den[pt] = f4{1e-6f, 1e-6f, 1e-6f, 1e-6f}; }
    const u2 ones = {0x3f803f80u, 0x3f803f80u};
    u2 o2 = ones;
    asm volatile("" : "+v"(o2));
    SB(); valu_to_mfma();
#pragma unroll
    for (int pt = 0; pt < 4; ++pt) mfma16_vvc(den[pt], o2, dq[pt]);
    mfma_done();
    const float chk = (k0[0] + k1[0]) + den[0][0];
    if (!__any(chk != chk)) break;
    if (spin > 4096u) { spin_fail(x); break; }
    o_load_partials(x, st);
  }
  lds_t* mine = x.L + LD_PV + x.w * PV_WAVE + (x.w >> 1) * 4096 + x.lane * 16;
  *(LAS f4*)(mine) = k0; *(LAS f4*)(mine + 1024) = k1;
  __syncthreads();
  lds_t* o = x.L + LD_PV + (x.w ^ 2) * PV_WAVE + ((x.w ^ 2) >> 1) * 4096 + x.lane * 16;
  k0 += *(LAS f4*)(o); k1 += *(LAS f4*)(o + 1024);
  const u4 akv = {pack2(k0[0], k0[1]), pack2(k0[2], k0[3]), pack2(k1[0], k1[1]), pack2(k1[2], k1[3])};
  f4 num[4];
  SB(); valu_to_mfma();
#pragma unroll
  for (int pt = 0; pt < 4; ++pt) mfma_vv0(num[pt], akv, st.pq[pt]);
  mfma_done();
  const unsigned oo = WS_OB + x.layer * OB_BYTES + ((2 * x.rb + ph) * 32 + hh) * 2048 + (16 * ((g & 1) ^ (2 * h)) + cl) * 32 + 16 * (g >> 1);
#pragma unroll
  for (int pt = 0; pt < 4; pt += 2) {
    const f4 va = num[pt] * __builtin_amdgcn_rcpf(den[pt][0]), vb = num[pt + 1] * __builtin_amdgcn_rcpf(den[pt + 1][0]);
    const auto s0 = __builtin_amdgcn_permlane16_swap(pack2(va[0], va[1]), pack2(vb[0], vb[1]), false, false);
    const auto s1 = __builtin_amdgcn_permlane16_swap(pack2(va[2], va[3]), pack2(vb[2], vb[3]), false, false);
    st16(x.ws, oo ^ (pt * 512), u4{s0[0], s1[0], s0[1], s1[1]});
  }
}

struct OAddr { lds_t* p[4]; };
template <int KS> DI void o_ldw(const OAddr& wb, u4 (&b)[4]) {
#pragma unroll
  for (int ct = 0; ct < 4; ++ct) b[ct] = cat(lds_tr(wb.p[ct] + KS * 4096), lds_tr(wb.p[ct] + KS * 4096 + 512));
}
DI unsigned umax3(unsigned a, unsigned b, unsigned c) { const unsigned m = a > b ? a : b; return m > c ? m : c; }
template <int KS> struct OGemm {
  static DI void run(const OAddr& wb, const u4 (&of)[16], u4 (&b)[3][4], f4 (&acc)[4], unsigned& top) {
#pragma unroll
    for (int ct = 0; ct < 4; ++ct) {
      if (KS == 0) mfma_vv0(acc[ct], b[0][ct], of[KS]); else mfma_vv(acc[ct], b[KS % 3][ct], of[KS]);
      if (KS < 14) b[(KS + 2) % 3][ct] = cat(lds_tr(wb.p[ct] + (KS + 2) * 4096), lds_tr(wb.p[ct] + (KS + 2) * 4096 + 512));
      if (ct == 1) top = umax3(top, of[KS].x, of[KS].y);
      if (ct == 3) top = umax3(top, of[KS].z, of[KS].w);
      SB();
    }
    OGemm<KS + 1>::run(wb, of, b, acc, top);
  }
};
template <> struct OGemm<16> { static DI void run(const OAddr&, const u4 (&)[16], u4 (&)[3][4], f4 (&)[4], unsigned&) {} };

DI void o_gemm(const Ctx& x, const Params& p, const QState& st) {
  const int g = x.lane >> 4, cl = x.lane & 15, pb = 2 * x.rb + (x.hp & 1), cb = x.hp >> 1;
  const unsigned oo = WS_OB + x.layer * OB_BYTES + ((pb * 32 + (g >> 1)) * 64 + 16 * x.w + cl) * 32 + 16 * (g & 1);
  OAddr wb;
  {
    const int q = cl >> 2, pp = cl & 3, f = ((q >> 1) & 1) | ((g & 1) << 1);
#pragma unroll
    for (int ct = 0; ct < 4; ++ct) wb.p[ct] = x.L + LD_XT + (8 * g + q) * 128 + (((2 * ct + (pp >> 1)) ^ (f << 1)) * 16) + 8 * (pp & 1);
  }
  f4 acc[4];
  o_dma_wo(x, p);
  for (unsigned spin = 0;; ++spin) {
    u4 of[16];
#pragma unroll
    for (int ks = 0; ks < 16; ++ks) of[ks] = ld16(x.ws, oo + ks * 4096);
    if (spin == 0) wait_vm_barrier<16>();
    u4 b[3][4];
    o_ldw<0>(wb, b[0]); o_ldw<1>(wb, b[1]);
    SB();
    unsigned top = 0;
    OGemm<0>::run(wb, of, b, acc, top);
    mfma_done();
    if (!__any(top == ~0u)) break;
    if (spin > 4096u) { spin_fail(x); break; }
  }
  f2 s2 = {0.f, 0.f}, q2 = s2;
  unsigned so = LD_OST + (16 * x.w + cl) * 144 + 8 * g;
  asm volatile("" : "+v"(so));
#pragma unroll
  for (int ct = 0; ct < 4; ++ct) {
    const f2 a = unpk(st.res[ct].x) + f2{acc[ct][0], acc[ct][1]}, b2 = unpk(st.res[ct].y) + f2{acc[ct][2], acc[ct][3]};
    s2 += a + b2; q2 += a * a; q2 += b2 * b2;
    *(LAS u2*)(x.L + so + 32 * ct) = u2{pk(a), pk(b2)};
  }
  if (x.layer == 0) {
    const f2 sq = xrow_sum2(s2.x + s2.y, q2.x + q2.y);
    if (g < 2) {
      GAS float* d = (GAS float*)(p.ws + WS_ST + 2048 * 8) + g * 2048 + 64 * pb + 16 * x.w + cl;
      __hip_atomic_fetch_add(d, g ? sq.y : sq.x, RLX_AGENT);
    }
  }
  u4 kd[3];
  conv_load_k(x, kd);
  __syncthreads();
  if (x.layer == 0) {
    const int qq = cl >> 2, pp = cl & 3;
#pragma unroll
    for (int j = 0; j < 2; ++j) {
      lds_t* a = x.L + LD_OST + (8 * (g + 4 * j) + qq) * 144 + (16 * x.w + 4 * pp) * 2;
      const u4 v = cat(lds_tr(a), lds_tr(a + 4 * 144));
      st16(x.ws, WS_XT + XT_BYTES + ((64 * cb + 16 * x.w + cl) * XT_STRIDE + 64 * pb + 8 * (g + 4 * j)) * 2, v);
    }
    SB();
    conv_build_k(x, kd);
  } else {
    const rsrc_t ro = make_rsrc(p.out, SEQ * DM * 2);
#pragma unroll
    for (int i = 0; i < 2; ++i) {
      const int k = i * 256 + x.tid, pos = k >> 3, q8 = k & 7;
      __builtin_amdgcn_raw_buffer_store_b128(*(LAS u4*)(x.L + LD_OST + pos * 144 + q8 * 16), ro, ((64 * pb + pos) * DM + 64 * cb + 8 * q8) * 2, 0, 0);
    }
  }
}

DI Ctx make_ctx(const Params& p, unsigned char* smem) {
  Ctx x;
  x.L = (lds_t*)smem; x.ws = make_rsrc(p.ws, WS_END); x.cw = (GAS unsigned*)(p.ws + WS_CTRL);
  x.tid = threadIdx.x; x.lane = x.tid & 63; x.w = __builtin_amdgcn_readfirstlane(x.tid >> 6); x.c = blockIdx.x;
  x.cp = (x.c & 7) * 32 + (x.c >> 3);
  x.rb = 2 * (x.c & 7) + (x.c >> 7); x.hp = (x.c >> 3) & 15; x.layer = 0;
  return x;
}

DI Ctx fresh(const Ctx& x, int layer) { Ctx y = x; int t = x.tid; asm volatile("" : "+v"(t)); y.tid = t; y.lane = t & 63; y.layer = layer; return y; }

__global__ void __launch_bounds__(256, 1) k_main(Params p) {
  extern __shared__ __attribute__((aligned(16))) unsigned char smem[];
  const unsigned char* code0;
  asm volatile("s_getpc_b64 %0" : "=s"(code0));
  const Ctx x0 = make_ctx(p, smem);
  u4 wf[48];
  f4 wsum[3];
  const float dsk = bf2f(p.dsk[2 * x0.cp + (x0.w >> 1)]);
  unsigned tag;
  {
    const Ctx x = fresh(x0, 0);
    u4 kd[3], a[32];
    ProIn pin;
    pro_load(x, p, pin);
    pro_init(x);
    tag = pin.epoch + 1u;
    pro_publish_x(x, pin);
    SB();
    pro_load_w2(x, p, pin);
    asm volatile("s_waitcnt vmcnt(12)" ::: "memory");
    __syncthreads();
    if (x.w == 0) __builtin_amdgcn_raw_buffer_store_b32(tag, x.ws, WS_FLAG + x.c * 64, 0, AUX_SC1);
    unsigned fl = __builtin_amdgcn_raw_buffer_load_b32(x.ws, WS_FLAG + x.tid * 64, 0, AUX_SC1);
    pro_tables(x, pin);
    pro_fold_w(x, 6 * x.c + x.w, pin.w[0]);
    if (x.w < 2) pro_fold_w(x, 6 * x.c + 4 + x.w, pin.w[1]);
    drain();
    asm volatile("" : "+v"(fl));
    __syncthreads();
    fl = __builtin_amdgcn_raw_buffer_load_b32(x.ws, WS_FLAG + x.tid * 64, 0, AUX_SC1) == tag ? tag : fl;
    __builtin_amdgcn_raw_buffer_store_b32(tag, x.ws, WS_FLAG2 + x.c * 64, 0, AUX_SC1);
    pro_kgemm(x);
    pro_wait(x, WS_FLAG, tag, fl);
    ConvIn in;
    conv_load_in(x, 0, in);
    SB();
    pro_expand(x);
    __syncthreads();
    pro_take_k(x, kd);
    conv_build_k(x, kd);
    conv_build_u(x, in);
    const u4 fl2 = pro_peek_wave(x, WS_FLAG2);
    __syncthreads();
    pro_wait_wave(x, WS_FLAG2, tag, fl2);
    Warm wm;
    wm.a = pro_warm_code(x, code0);
    pro_warm(x, p, wm);
    if (x.w & 1) conv_wave<1, true>(x, dsk, a); else conv_wave<0, true>(x, dsk, a);
    pro_warm_retire(wm);
    a2a_post(fresh(x0, 0), 0);
    poison_o(fresh(x0, 0), 1);
    q_take_w(fresh(x0, 0), wf);
    q_wsum(wf, wsum);
  }
#pragma unroll 1
  for (int layer = 0; layer < 2; ++layer) {
    Ctx x = fresh(x0, layer);
    a2a_wait(x, 2 * layer, 0u);
    QState st;
    x = fresh(x0, layer);
    q_phase(x, p, wf, wsum, st);
    x = fresh(x0, layer);
    o_compute(x, st);
    x = fresh(x0, layer);
    o_gemm(x, p, st);
    if (layer == 0) {
      u4 a[32];
      a2a_signal(x, 1);
      x = fresh(x0, 1);
      unsigned first;
      if (x.w & 1) { conv_load_a<1, 0>(x, a); first = a2a_peek(x, 1); conv_load_a<1, 2>(x, a); }
      else { conv_load_a<0, 0>(x, a); first = a2a_peek(x, 1); conv_load_a<0, 2>(x, a); }
      SB();
      a2a_wait(x, 1, first);
      ConvIn in;
      conv_load_in(x, 1, in);
      conv_build_u(x, in);
        __syncthreads();
      if (x.w & 1) conv_wave<1, false>(x, dsk, a); else conv_wave<0, false>(x, dsk, a);
      a2a_post(fresh(x0, 1), 2);
    }
  }
  if (x0.c == 0 && x0.tid == 0) __builtin_amdgcn_raw_buffer_store_b32(tag, x0.ws, WS_EPOCH, 0, AUX_SC1);
}

extern "C" void kernel_launch(void* const* d_in, const int* in_sizes, int n_in, void* d_out, int out_size, void* d_ws, size_t ws_size,
                              hipStream_t stream) {
  Params p;
  p.u = (const unsigned short*)d_in[0]; p.lar = (const unsigned short*)d_in[1]; p.aim = (const unsigned short*)d_in[2];
  p.cre = (const unsigned short*)d_in[3]; p.cim = (const unsigned short*)d_in[4]; p.ldt = (const unsigned short*)d_in[5];
  p.dsk = (const unsigned short*)d_in[6]; p.wqkv = (const unsigned short*)d_in[8]; p.wphi = (const unsigned short*)d_in[9];
  p.wo = (const unsigned short*)d_in[10]; p.out = (unsigned short*)d_out; p.ws = (unsigned char*)d_ws;
  (void)hipFuncSetAttribute((const void*)k_main, hipFuncAttributeMaxDynamicSharedMemorySize, LDS_BYTES);
  k_main<<<256, 256, LDS_BYTES, stream>>>(p);
}
```
